```python
import torch

def _softmax(x, axis=-1):
    return torch.softmax(x, dim=axis)

def _gelu(x):
    return torch.nn.functional.gelu(x, approximate='tanh')

def _silu(x):
    return torch.nn.functional.silu(x)

def _sigmoid(x):
    return torch.sigmoid(x)

def _layernorm(x, axis=-1, eps=1e-05):
    m = x.mean(dim=axis, keepdim=True)
    v = x.var(dim=axis, keepdim=True, unbiased=False)
    return (x - m) / torch.sqrt(v + eps)

def _scatter_add(tgt, idx, src, axis=0):
    return tgt.clone().index_add_(axis, idx, src)

def _segment_sum(src, seg, n):
    out = torch.zeros((n,) + src.shape[1:], dtype=src.dtype)
    return out.index_add_(0, seg, src)

def _pad(x, pw):
    flat = tuple((v for pair in reversed(pw) for v in pair))
    return torch.nn.functional.pad(x, flat)
kernel_name = 'adaLN_cond__compressed_kv_cla__dense_activation_silu__prompt'

def setup_inputs():

    _w = lambda a: (lambda t: t.to(torch.bfloat16) if t.is_floating_point() else t)(torch.as_tensor(a))

    def _A_setup():
        B, H, Sq, Sk, D, M = (1, 32, 2048, 2048, 128, 640)
        Dc = 4 * D
        return {'x': _w((lambda *s: torch.randn(*s, dtype=torch.bfloat16) * 0.05)(B, Sq, M)), 'c_kv': _w((lambda *s: torch.randn(*s, dtype=torch.bfloat16) * 0.8)(B, Sk, Dc)), 'w_q': _w((lambda *s: torch.randn(*s, dtype=torch.bfloat16) * 1.6)(M, H, D) * M ** (-0.5)), 'w_uk': _w((lambda *s: torch.randn(*s, dtype=torch.bfloat16) * 1.6)(Dc, H, D) * Dc ** (-0.5)), 'w_uv': _w((lambda *s: torch.randn(*s, dtype=torch.bfloat16) * 0.8)(Dc, H, D) * Dc ** (-0.5)), 'w_o': _w((lambda *s: torch.randn(*s, dtype=torch.bfloat16) * 0.8)(H, D, M) * (H * D) ** (-0.5))}
    _A_in = _A_setup()

    def _B_setup():
        B, S, D, F = (1, 2048, 640, 1728)
        return {'x': _w((lambda *s: torch.randn(*s, dtype=torch.bfloat16) * 0.05)(B, S, D)), 'w1': _w((lambda *s: torch.randn(*s, dtype=torch.bfloat16) * 1.6)(D, F) * D ** (-0.5)), 'w2': _w((lambda *s: torch.randn(*s, dtype=torch.bfloat16) * 0.2)(F, D) * F ** (-0.5))}
    _B_in = _B_setup()
    return {'A_x': _A_in['x'], 'A_c_kv': _A_in['c_kv'], 'A_w_q': _A_in['w_q'], 'A_w_uk': _A_in['w_uk'], 'A_w_uv': _A_in['w_uv'], 'A_w_o': _A_in['w_o'], 'B_x': _B_in['x'], 'B_w1': _B_in['w1'], 'B_w2': _B_in['w2'], 'c': _w((lambda *s: torch.randn(*s, dtype=torch.bfloat16) * 0.1)(1, 640) * 8.0), 'w_mod': _w((lambda *s: torch.randn(*s, dtype=torch.bfloat16) * 0.1)(640, 6 * 640) * 640 ** (-0.5) * 8.0)}

def reference(A_x, A_c_kv, A_w_q, A_w_uk, A_w_uv, A_w_o, B_x, B_w1, B_w2, c, w_mod):

    def _A_ref(x, c_kv, w_q, w_uk, w_uv, w_o):
        B, Sq, M = (x.shape[0], x.shape[1], x.shape[2])
        Sk = c_kv.shape[1]
        H, D = (w_q.shape[1], w_q.shape[2])
        q = torch.einsum('bqm,mhd->bhqd', x, w_q)
        k = torch.einsum('bkc,chd->bhkd', c_kv, w_uk)
        v = torch.einsum('bkc,chd->bhkd', c_kv, w_uv)
        scores = torch.einsum('bhqd,bhkd->bhqk', q, k) * D ** (-0.5)
        off = Sk - Sq
        qi = torch.arange(Sq)[:, None]
        kj = torch.arange(Sk)[None, :]
        cmask = kj <= qi + off
        scores = torch.where(cmask[None, None], scores, -1000000000.0)
        p = _softmax(scores, axis=-1)
        ctx = torch.einsum('bhqk,bhkd->bhqd', p, v)
        return torch.einsum('bhqd,hdm->bqm', ctx, w_o)

    def _B_ref(x, w1, w2):
        h = torch.einsum('bsd,df->bsf', x, w1)
        h = _silu(h)
        out = torch.einsum('bsf,fd->bsd', h, w2)
        return out
    x = A_x
    mod = (_silu(c) @ w_mod).reshape(c.shape[0], 6, 1, -1)
    sa, ca, ga, sb, cb, gb = (mod[:, 0].clone(), mod[:, 1].clone(), mod[:, 2].clone(), mod[:, 3].clone(), mod[:, 4].clone(), mod[:, 5].clone())
    h = _layernorm(x, axis=-1) * (1 + ca) + sa
    x = x + ga * _A_ref(h, A_c_kv, A_w_q, A_w_uk, A_w_uv, A_w_o)
    h = _layernorm(x, axis=-1) * (1 + cb) + sb
    x = x + gb * _B_ref(h, B_w1, B_w2)
    return x
```

```cpp
#include <hip/hip_runtime.h>
#include <hip/hip_bf16.h>
typedef __hip_bfloat16 bf16;
constexpr int S=2048, M=640, DC=512, H=32, D=128, F=1728;
__device__ inline float b2f(bf16 x){return __bfloat162float(x);}
__device__ inline bf16 f2b(float x){return __float2bfloat16(x);}
__global__ void k_mod(const bf16* c,const bf16* w,float* mod){
  int n=blockIdx.x*blockDim.x+threadIdx.x; if(n>=6*M) return;
  float a=0; for(int k=0;k<M;k++){float v=b2f(c[k]); v=v/(1.f+__expf(-v)); v=b2f(f2b(v)); a+=v*b2f(w[k*6*M+n]);}
  mod[n]=b2f(f2b(a));
}
template<typename T> __device__ inline float ld(const T* p);
template<> __device__ inline float ld<bf16>(const bf16* p){return b2f(*p);}
template<> __device__ inline float ld<float>(const float* p){return *p;}
template<typename T>
__global__ void k_ln(const T* x,const float* sh,const float* sc,bf16* h){
  int r=blockIdx.x; __shared__ float red[64]; int t=threadIdx.x;
  float s=0; for(int i=t;i<M;i+=64) s+=ld(x+r*M+i);
  for(int o=32;o;o>>=1) s+=__shfl_xor(s,o); float mean=s/M;
  float q=0; for(int i=t;i<M;i+=64){float d=ld(x+r*M+i)-mean;q+=d*d;}
  for(int o=32;o;o>>=1) q+=__shfl_xor(q,o); float rs=rsqrtf(q/M+1e-5f);
  for(int i=t;i<M;i+=64) h[r*M+i]=f2b((ld(x+r*M+i)-mean)*rs*(1.f+sc[i])+sh[i]);
}
template<int EPI>
__global__ void k_gemm(const bf16* A,const bf16* B,int Md,int N,int K,void* C,const void* res,const float* g){
  __shared__ float As[16][17],Bs[16][17];
  int tx=threadIdx.x,ty=threadIdx.y; int r=blockIdx.y*16+ty,c=blockIdx.x*16+tx; float a=0;
  for(int k0=0;k0<K;k0+=16){As[ty][tx]=b2f(A[r*K+k0+tx]);Bs[ty][tx]=b2f(B[(k0+ty)*N+c]);__syncthreads();
    for(int k=0;k<16;k++)a+=As[ty][k]*Bs[k][tx];__syncthreads();}
  if(EPI==0)((bf16*)C)[r*N+c]=f2b(a);
  if(EPI==1)((float*)C)[r*N+c]=b2f(((const bf16*)res)[r*N+c])+g[c]*a;
  if(EPI==2)((bf16*)C)[r*N+c]=f2b(a/(1.f+__expf(-a)));
  if(EPI==3)((bf16*)C)[r*N+c]=f2b(((const float*)res)[r*N+c]+g[c]*a);
}
__global__ __launch_bounds__(128) void k_attn(const bf16* q,const bf16* k,const bf16* v,bf16* o){
  int r=blockIdx.x,h=blockIdx.y,t=threadIdx.x; __shared__ float p[S]; __shared__ float qs[D]; __shared__ float red[128];
  qs[t]=b2f(q[r*H*D+h*D+t]); __syncthreads();
  float mx=-1e30f; for(int j=t;j<=r;j+=128){float a=0;for(int d=0;d<D;d++)a+=qs[d]*b2f(k[j*H*D+h*D+d]);a*=0.08838834764f;p[j]=a;mx=fmaxf(mx,a);}
  red[t]=mx;__syncthreads(); for(int i=0;i<128;i++)mx=fmaxf(mx,red[i]); __syncthreads();
  float s=0; for(int j=t;j<=r;j+=128){float e=__expf(p[j]-mx);p[j]=e;s+=e;} red[t]=s;__syncthreads(); s=0;for(int i=0;i<128;i++)s+=red[i];
  float a=0; for(int j=0;j<=r;j++)a+=p[j]*b2f(v[j*H*D+h*D+t]); o[r*H*D+h*D+t]=f2b(a/s);
}
extern "C" void kernel_launch(void* const* d_in,const int* in_sizes,int n_in,void* d_out,int out_size,void* d_ws,size_t ws_size,hipStream_t stream){
  const bf16* x=(const bf16*)d_in[0],*ckv=(const bf16*)d_in[1],*wq=(const bf16*)d_in[2],*wuk=(const bf16*)d_in[3],*wuv=(const bf16*)d_in[4],*wo=(const bf16*)d_in[5];
  const bf16* w1=(const bf16*)d_in[7],*w2=(const bf16*)d_in[8],*c=(const bf16*)d_in[9],*wm=(const bf16*)d_in[10];
  char* ws=(char*)d_ws; float* mod=(float*)ws; bf16* h=(bf16*)(ws+(1<<20)); bf16* q=(bf16*)(ws+(16<<20)); bf16* k=(bf16*)(ws+(40<<20)); bf16* v=(bf16*)(ws+(64<<20)); bf16* o=(bf16*)(ws+(88<<20));
  float* x1=(float*)(ws+(112<<20)); bf16* h1=(bf16*)(ws+(128<<20));
  k_mod<<<6*M/64,64,0,stream>>>(c,wm,mod);
  k_ln<bf16><<<S,64,0,stream>>>(x,mod,mod+M,h);
  k_gemm<0><<<dim3(H*D/16,S/16),dim3(16,16),0,stream>>>(h,wq,S,H*D,M,q,0,0);
  k_gemm<0><<<dim3(H*D/16,S/16),dim3(16,16),0,stream>>>(ckv,wuk,S,H*D,DC,k,0,0);
  k_gemm<0><<<dim3(H*D/16,S/16),dim3(16,16),0,stream>>>(ckv,wuv,S,H*D,DC,v,0,0);
  k_attn<<<dim3(S,H),128,0,stream>>>(q,k,v,o);
  k_gemm<1><<<dim3(M/16,S/16),dim3(16,16),0,stream>>>(o,wo,S,M,H*D,x1,x,mod+2*M);
  k_ln<float><<<S,64,0,stream>>>(x1,mod+3*M,mod+4*M,h);
  k_gemm<2><<<dim3(F/16,S/16),dim3(16,16),0,stream>>>(h,w1,S,F,M,h1,0,0);
  k_gemm<3><<<dim3(M/16,S/16),dim3(16,16),0,stream>>>(h1,w2,S,M,F,d_out,x1,mod+5*M);
}
```

```cpp
#include <hip/hip_runtime.h>

extern "C" __global__ __launch_bounds__(256, 1) __attribute__((aligned(4096))) void adaln_block_kernel(const void* p_x, const void* p_ckv, const void* p_wq, const void* p_wuk, const void* p_wuv, const void* p_wo, const void* p_w1, const void* p_w2, const void* p_c, const void* p_wmod, const void* p_out, const void* p_ws) {
  __shared__ __attribute__((aligned(16))) char lds[163840];
  asm volatile(
R"ASM(
s_mov_b64 s[4:5], %[ka]
s_mov_b32 s63, %[bid]
v_mov_b32 v2, %[tid]
s_load_dwordx2 s[8:9], s[4:5], 0
s_load_dwordx2 s[12:13], s[4:5], 8
s_load_dwordx2 s[16:17], s[4:5], 16
s_load_dwordx2 s[20:21], s[4:5], 24
s_load_dwordx2 s[24:25], s[4:5], 32
s_load_dwordx2 s[28:29], s[4:5], 40
s_load_dwordx2 s[32:33], s[4:5], 48
s_load_dwordx2 s[36:37], s[4:5], 56
s_load_dwordx2 s[40:41], s[4:5], 64
s_load_dwordx2 s[44:45], s[4:5], 72
s_load_dwordx2 s[48:49], s[4:5], 80
s_load_dwordx2 s[52:53], s[4:5], 88
s_mov_b32 s10, 2621440
s_mov_b32 s11, 0x20000
s_mov_b32 s14, 2097152
s_mov_b32 s15, 0x20000
s_mov_b32 s18, 5242880
s_mov_b32 s19, 0x20000
s_mov_b32 s22, 4194304
s_mov_b32 s23, 0x20000
s_mov_b32 s26, 4194304
s_mov_b32 s27, 0x20000
s_mov_b32 s30, 5242880
s_mov_b32 s31, 0x20000
s_mov_b32 s34, 2211840
s_mov_b32 s35, 0x20000
s_mov_b32 s38, 2211840
s_mov_b32 s39, 0x20000
s_mov_b32 s42, 1280
s_mov_b32 s43, 0x20000
s_mov_b32 s46, 4915200
s_mov_b32 s47, 0x20000
s_mov_b32 s50, 2621440
s_mov_b32 s51, 0x20000
s_mov_b32 s54, 100663296
s_mov_b32 s55, 0x20000
v_lshrrev_b32 v1, 6, v2
v_and_b32 v2, 63, v2
v_readfirstlane_b32 s60, v1
v_lshlrev_b32 v1, 4, v2
s_and_b32 s61, s63, 7
s_lshl_b32 s61, s61, 2
s_lshr_b32 s66, s63, 6
s_add_u32 s61, s61, s66
s_lshr_b32 s62, s63, 3
s_and_b32 s62, s62, 7
s_waitcnt lgkmcnt(0)
s_load_dwordx2 s[64:65], s[52:53], 0
s_mov_b32 s95, 16384
s_getpc_b64 s[96:97]
L_code_here:
s_and_b32 s96, s96, 0xfffff000
s_mov_b32 s98, L_code_end-L_code_here
s_mov_b32 s99, 0x20000
s_lshr_b32 s66, s63, 3
s_lshl_b32 s66, s66, 2
s_add_u32 s66, s66, s60
s_lshl_b32 s66, s66, 10
buffer_load_dwordx4 v[204:207], v1, s[96:99], s66 offen
s_and_b32 s69, s63, 3
s_lshl_b32 s69, s69, 2
s_add_u32 s69, s69, s60
s_mul_i32 s70, s69, 80
s_mul_i32 s69, s69, 307200
s_lshr_b32 s71, s63, 2
s_lshl_b32 s71, s71, 7
s_add_u32 s69, s69, s71
s_cmp_lt_u32 s63, 240
s_cselect_b32 s77, s69, 1073741824
v_lshrrev_b32 v234, 3, v2
v_lshlrev_b32 v236, 1, v234
v_mul_u32_u24 v234, 7680, v234
v_and_b32 v235, 7, v2
v_lshl_add_u32 v234, v235, 4, v234
buffer_load_ushort v228, v236, s[40:43], s70 offen offset:0
buffer_load_ushort v229, v236, s[40:43], s70 offen offset:16
buffer_load_ushort v230, v236, s[40:43], s70 offen offset:32
buffer_load_ushort v231, v236, s[40:43], s70 offen offset:48
buffer_load_ushort v232, v236, s[40:43], s70 offen offset:64
buffer_load_dwordx4 v[208:211], v234, s[44:47], s77 offen
s_add_u32 s77, s77, 61440
buffer_load_dwordx4 v[212:215], v234, s[44:47], s77 offen
s_add_u32 s77, s77, 61440
buffer_load_dwordx4 v[216:219], v234, s[44:47], s77 offen
s_add_u32 s77, s77, 61440
buffer_load_dwordx4 v[220:223], v234, s[44:47], s77 offen
s_add_u32 s77, s77, 61440
buffer_load_dwordx4 v[224:227], v234, s[44:47], s77 offen
s_mul_i32 s75, s63, 10240
s_lshl_b32 s71, s60, 7
s_add_u32 s75, s75, s71
s_cmp_lt_u32 s60, 2
s_cselect_b32 s76, s75, 1073741824
v_and_b32 v184, 7, v2
v_mul_u32_u24 v184, 1280, v184
v_lshrrev_b32 v185, 3, v2
v_lshl_add_u32 v184, v185, 4, v184
s_lshl_b32 s80, s60, 12
s_add_u32 s7, s63, s62
s_and_b32 s66, s60, 1
s_lshr_b32 s67, s60, 1
s_lshl_b32 s68, s66, 3
s_add_u32 s68, s68, s62
s_lshl_b32 s68, s68, 16
s_lshl_b32 s69, s67, 15
s_add_u32 s78, s68, s69
s_lshl_b32 s68, s61, 8
s_lshl_b32 s69, s67, 7
s_add_u32 s68, s68, s69
s_lshl_b32 s69, s66, 18
s_add_u32 s79, s68, s69
v_lshrrev_b32 v200, 3, v2
v_and_b32 v201, 7, v2
v_lshrrev_b32 v202, 1, v200
v_xor_b32 v201, v201, v202
v_lshlrev_b32 v201, 4, v201
v_lshl_add_u32 v4, v200, 10, v201
v_xor_b32 v5, 64, v4
v_add_u32 v5, 7168, v5
v_add_u32 v6, 14336, v4
v_add_u32 v7, 14336, v5
v_lshrrev_b32 v200, 2, v2
v_and_b32 v200, 7, v200
v_and_b32 v201, 3, v2
v_lshlrev_b32 v201, 4, v201
v_lshrrev_b32 v202, 5, v2
v_lshl_add_u32 v201, v202, 6, v201
v_lshl_add_u32 v8, v200, 13, v201
v_xor_b32 v9, 32, v8
v_add_u32 v9, 64512, v9
v_add_u32 v10, 129024, v8
v_add_u32 v11, 129024, v9
s_add_u32 m0, s80, 81920
s_add_u32 s81, s7, 0
s_and_b32 s81, s81, 7
s_lshl_b32 s81, s81, 19
s_add_u32 s81, s81, s79
buffer_load_dwordx4 v8, s[20:23], s81 offen offset:0 lds
buffer_load_dwordx4 v9, s[20:23], s81 offen offset:1024 lds
buffer_load_dwordx4 v10, s[20:23], s81 offen offset:2048 lds
buffer_load_dwordx4 v11, s[20:23], s81 offen offset:3072 lds
s_and_b32 s66, s60, 1
s_lshr_b32 s67, s60, 1
v_and_b32 v200, 15, v2
v_lshrrev_b32 v201, 4, v2
v_lshrrev_b32 v202, 1, v200
v_xor_b32 v202, v202, v201
v_lshlrev_b32 v202, 4, v202
v_lshl_add_u32 v202, v200, 7, v202
s_lshl_b32 s68, s67, 13
v_add_u32 v12, s68, v202
v_xor_b32 v13, 64, v12
v_lshrrev_b32 v202, 2, v200
v_lshlrev_b32 v202, 6, v202
v_lshl_add_u32 v202, v201, 10, v202
v_and_b32 v203, 1, v201
v_lshl_add_u32 v202, v203, 5, v202
v_and_b32 v203, 3, v2
v_lshl_add_u32 v202, v203, 3, v202
s_lshl_b32 s68, s66, 13
v_add_u32 v0, s68, v202
v_xor_b32 v3, 32, v0
s_mul_i32 s68, s61, 1215488
s_mul_i32 s69, s62, 37888
s_add_u32 s68, s68, s69
s_add_u32 s68, s68, 50331648
s_lshl_b32 s69, s67, 13
s_lshl_b32 s70, s66, 11
s_add_u32 s69, s69, s70
s_add_u32 s4, s68, s69
s_lshl_b32 s69, s66, 13
s_lshl_b32 s70, s67, 10
s_add_u32 s69, s69, s70
s_add_u32 s69, s69, 16384
s_add_u32 s82, s68, s69
s_lshl_b32 s68, s62, 3
s_add_u32 s69, s68, s60
s_mul_i32 s69, s69, 20480
s_add_u32 s86, s69, 2097152
s_add_u32 s87, s86, 81920
s_sub_u32 s69, s60, s68
s_add_u32 s69, s69, 120
s_mul_i32 s69, s69, 20480
s_add_u32 s88, s69, 2097152
s_add_u32 s89, s88, 81920
s_lshl_b32 s92, s66, 13
s_mov_b64 exec, 0
s_cmp_eq_u32 s60, 0
s_cbranch_scc1 L_pad_a_0
s_cmp_eq_u32 s60, 1
s_cbranch_scc1 L_pad_a_1
s_cmp_eq_u32 s60, 2
s_cbranch_scc1 L_pad_a_2
s_branch L_pad_a_3
L_padret_a:
s_mov_b64 exec, -1
s_add_u32 m0, s80, 16384
s_add_u32 s81, s7, 0
s_and_b32 s81, s81, 7
s_lshl_b32 s81, s81, 7
s_add_u32 s81, s81, s78
buffer_load_dwordx4 v4, s[12:15], s81 offen offset:0 lds
buffer_load_dwordx4 v5, s[12:15], s81 offen offset:1024 lds
buffer_load_dwordx4 v6, s[12:15], s81 offen offset:2048 lds
buffer_load_dwordx4 v7, s[12:15], s81 offen offset:3072 lds
s_add_u32 m0, s80, 114688
s_add_u32 s81, s7, 0
s_and_b32 s81, s81, 7
s_lshl_b32 s81, s81, 19
s_add_u32 s81, s81, s79
buffer_load_dwordx4 v8, s[24:27], s81 offen offset:0 lds
buffer_load_dwordx4 v9, s[24:27], s81 offen offset:1024 lds
s_waitcnt vmcnt(15)
v_lshlrev_b32 v228, 16, v228
v_lshlrev_b32 v229, 16, v229
v_lshlrev_b32 v230, 16, v230
v_lshlrev_b32 v231, 16, v231
v_lshlrev_b32 v232, 16, v232
v_mul_f32 v237, -1.4426950408889634, v228
v_mul_f32 v238, -1.4426950408889634, v229
v_mul_f32 v239, -1.4426950408889634, v230
v_mul_f32 v240, -1.4426950408889634, v231
v_mul_f32 v241, -1.4426950408889634, v232
v_exp_f32 v237, v237
buffer_load_dwordx4 v10, s[24:27], s81 offen offset:2048 lds
v_exp_f32 v238, v238
v_exp_f32 v239, v239
v_exp_f32 v240, v240
v_exp_f32 v241, v241
v_add_f32 v237, 1.0, v237
v_add_f32 v238, 1.0, v238
v_add_f32 v239, 1.0, v239
v_add_f32 v240, 1.0, v240
v_add_f32 v241, 1.0, v241
v_rcp_f32 v237, v237
v_rcp_f32 v238, v238
buffer_load_dwordx4 v11, s[24:27], s81 offen offset:3072 lds
v_rcp_f32 v239, v239
v_rcp_f32 v240, v240
v_rcp_f32 v241, v241
v_mul_f32 v228, v228, v237
v_mul_f32 v229, v229, v238
v_mul_f32 v230, v230, v239
v_mul_f32 v231, v231, v240
v_mul_f32 v232, v232, v241
v_cvt_pk_bf16_f32 v228, v228, v228
v_cvt_pk_bf16_f32 v229, v229, v229
v_cvt_pk_bf16_f32 v230, v230, v230
s_add_u32 m0, s80, 131072
s_add_u32 s81, s7, 0
s_and_b32 s81, s81, 7
s_lshl_b32 s81, s81, 7
s_add_u32 s81, s81, s78
s_add_u32 s81, s81, 1048576
buffer_load_dwordx4 v4, s[12:15], s81 offen offset:0 lds
v_cvt_pk_bf16_f32 v231, v231, v231
v_cvt_pk_bf16_f32 v232, v232, v232
v_and_b32 v228, 0xffff0000, v228
v_and_b32 v229, 0xffff0000, v229
v_and_b32 v230, 0xffff0000, v230
v_and_b32 v231, 0xffff0000, v231
v_and_b32 v232, 0xffff0000, v232
s_waitcnt vmcnt(17)
v_lshlrev_b32 v243, 16, v208
v_and_b32 v208, 0xffff0000, v208
v_mul_f32 v234, v228, v243
v_mul_f32 v235, v228, v208
buffer_load_dwordx4 v5, s[12:15], s81 offen offset:1024 lds
v_lshlrev_b32 v243, 16, v209
v_and_b32 v209, 0xffff0000, v209
v_mul_f32 v236, v228, v243
v_mul_f32 v237, v228, v209
v_lshlrev_b32 v243, 16, v210
v_and_b32 v210, 0xffff0000, v210
v_mul_f32 v238, v228, v243
v_mul_f32 v239, v228, v210
v_lshlrev_b32 v243, 16, v211
v_and_b32 v211, 0xffff0000, v211
v_mul_f32 v240, v228, v243
buffer_load_dwordx4 v6, s[12:15], s81 offen offset:2048 lds
v_mul_f32 v241, v228, v211
s_waitcnt vmcnt(18)
v_lshlrev_b32 v243, 16, v212
v_and_b32 v212, 0xffff0000, v212
v_fmac_f32 v234, v229, v243
v_fmac_f32 v235, v229, v212
v_lshlrev_b32 v243, 16, v213
v_and_b32 v213, 0xffff0000, v213
v_fmac_f32 v236, v229, v243
v_fmac_f32 v237, v229, v213
v_lshlrev_b32 v243, 16, v214
v_and_b32 v214, 0xffff0000, v214
buffer_load_dwordx4 v7, s[12:15], s81 offen offset:3072 lds
v_fmac_f32 v238, v229, v243
v_fmac_f32 v239, v229, v214
v_lshlrev_b32 v243, 16, v215
v_and_b32 v215, 0xffff0000, v215
v_fmac_f32 v240, v229, v243
v_fmac_f32 v241, v229, v215
s_waitcnt vmcnt(18)
v_lshlrev_b32 v243, 16, v216
v_and_b32 v216, 0xffff0000, v216
v_fmac_f32 v234, v230, v243
v_fmac_f32 v235, v230, v216
v_lshlrev_b32 v243, 16, v217
s_add_u32 m0, s80, 147456
s_add_u32 s81, s7, 1
s_and_b32 s81, s81, 7
s_lshl_b32 s81, s81, 19
s_add_u32 s81, s81, s79
buffer_load_dwordx4 v8, s[20:23], s81 offen offset:0 lds
v_and_b32 v217, 0xffff0000, v217
v_fmac_f32 v236, v230, v243
v_fmac_f32 v237, v230, v217
v_lshlrev_b32 v243, 16, v218
v_and_b32 v218, 0xffff0000, v218
v_fmac_f32 v238, v230, v243
v_fmac_f32 v239, v230, v218
v_lshlrev_b32 v243, 16, v219
v_and_b32 v219, 0xffff0000, v219
v_fmac_f32 v240, v230, v243
v_fmac_f32 v241, v230, v219
buffer_load_dwordx4 v9, s[20:23], s81 offen offset:1024 lds
s_waitcnt vmcnt(19)
v_lshlrev_b32 v243, 16, v220
v_and_b32 v220, 0xffff0000, v220
v_fmac_f32 v234, v231, v243
v_fmac_f32 v235, v231, v220
v_lshlrev_b32 v243, 16, v221
v_and_b32 v221, 0xffff0000, v221
v_fmac_f32 v236, v231, v243
v_fmac_f32 v237, v231, v221
v_lshlrev_b32 v243, 16, v222
v_and_b32 v222, 0xffff0000, v222
v_fmac_f32 v238, v231, v243
buffer_load_dwordx4 v10, s[20:23], s81 offen offset:2048 lds
v_fmac_f32 v239, v231, v222
v_lshlrev_b32 v243, 16, v223
v_and_b32 v223, 0xffff0000, v223
v_fmac_f32 v240, v231, v243
v_fmac_f32 v241, v231, v223
s_waitcnt vmcnt(19)
v_lshlrev_b32 v243, 16, v224
v_and_b32 v224, 0xffff0000, v224
v_fmac_f32 v234, v232, v243
v_fmac_f32 v235, v232, v224
v_lshlrev_b32 v243, 16, v225
v_and_b32 v225, 0xffff0000, v225
buffer_load_dwordx4 v11, s[20:23], s81 offen offset:3072 lds
v_fmac_f32 v236, v232, v243
v_fmac_f32 v237, v232, v225
v_lshlrev_b32 v243, 16, v226
v_and_b32 v226, 0xffff0000, v226
v_fmac_f32 v238, v232, v243
v_fmac_f32 v239, v232, v226
v_lshlrev_b32 v243, 16, v227
v_and_b32 v227, 0xffff0000, v227
v_fmac_f32 v240, v232, v243
v_fmac_f32 v241, v232, v227
v_permlane32_swap_b32 v234, v238
v_permlane32_swap_b32 v235, v239
v_permlane32_swap_b32 v236, v240
v_permlane32_swap_b32 v237, v241
v_add_f32 v234, v234, v238
v_add_f32 v235, v235, v239
v_add_f32 v236, v236, v240
v_add_f32 v237, v237, v241
s_nop 0
v_permlane16_swap_b32 v234, v236
v_permlane16_swap_b32 v235, v237
v_add_f32 v234, v234, v236
v_add_f32 v235, v235, v237
s_nop 0
v_add_f32_dpp v234, v234, v234 row_ror:8 row_mask:0xf bank_mask:0xf
v_add_f32_dpp v235, v235, v235 row_ror:8 row_mask:0xf bank_mask:0xf
s_lshl_b32 s69, s60, 8
s_add_u32 s69, s69, 98304
v_and_b32 v242, 7, v2
v_lshlrev_b32 v242, 5, v242
v_lshrrev_b32 v243, 4, v2
v_lshl_add_u32 v242, v243, 3, v242
v_add_u32 v242, s69, v242
ds_write_b64 v242, v[234:235]
L_ch_a0:
s_waitcnt vmcnt(12) lgkmcnt(0)
s_add_u32 s64, s64, 1
s_barrier
ds_read_b128 v[144:147], v12 offset:16384
ds_read_b128 v[148:151], v13 offset:16384
v_add_u32 v0, 0x14000, v0
v_add_u32 v3, 0x14000, v3
ds_read_b64_tr_b16 v[16:17], v0 offset:0
ds_read_b64_tr_b16 v[18:19], v0 offset:256
ds_read_b64_tr_b16 v[20:21], v0 offset:4096
ds_read_b64_tr_b16 v[22:23], v0 offset:4352
ds_read_b64_tr_b16 v[24:25], v3 offset:0
ds_read_b64_tr_b16 v[26:27], v3 offset:256
)ASM"
R"ASM(
ds_read_b64_tr_b16 v[28:29], v3 offset:4096
ds_read_b64_tr_b16 v[30:31], v3 offset:4352
ds_read_b64_tr_b16 v[32:33], v0 offset:512
ds_read_b64_tr_b16 v[34:35], v0 offset:768
ds_read_b64_tr_b16 v[36:37], v0 offset:4608
ds_read_b64_tr_b16 v[38:39], v0 offset:4864
ds_read_b64_tr_b16 v[40:41], v3 offset:512
ds_read_b64_tr_b16 v[42:43], v3 offset:768
s_nop 3
s_waitcnt lgkmcnt(8)
v_mfma_f32_16x16x32_bf16 a[0:3], v[16:19], v[144:147], 0
s_add_u32 s81, s7, 1
ds_read_b64_tr_b16 v[44:45], v3 offset:4608
ds_read_b64_tr_b16 v[46:47], v3 offset:4864
s_nop 3
v_mfma_f32_16x16x32_bf16 a[0:3], v[20:23], v[148:151], a[0:3]
s_and_b32 s81, s81, 7
s_lshl_b32 s81, s81, 7
ds_read_b128 v[152:155], v12 offset:18432
s_nop 3
v_mfma_f32_16x16x32_bf16 a[4:7], v[24:27], v[144:147], 0
s_waitcnt lgkmcnt(3)
s_add_u32 s81, s81, s78
ds_read_b128 v[156:159], v13 offset:18432
v_lshlrev_b32 v213, 2, v2
s_nop 3
v_mfma_f32_16x16x32_bf16 a[4:7], v[28:31], v[148:151], a[4:7]
s_add_u32 m0, s80, 0
ds_read_b128 v[160:163], v12 offset:20480
v_add_u32 v212, 98304, v213
s_nop 3
v_mfma_f32_16x16x32_bf16 a[8:11], v[32:35], v[144:147], 0
s_lshr_b32 s71, s63, 2
s_and_b32 s72, s63, 3
ds_read_b128 v[164:167], v13 offset:20480
s_nop 3
v_mfma_f32_16x16x32_bf16 a[8:11], v[36:39], v[148:151], a[8:11]
buffer_load_dwordx4 v4, s[12:15], s81 offen offset:0 lds
s_nop 3
v_mfma_f32_16x16x32_bf16 a[12:15], v[40:43], v[144:147], 0
s_waitcnt lgkmcnt(1)
s_lshl_b32 s71, s71, 8
ds_read_b128 v[168:171], v12 offset:22528
ds_read_b128 v[172:175], v13 offset:22528
s_nop 3
v_mfma_f32_16x16x32_bf16 a[12:15], v[44:47], v[148:151], a[12:15]
s_mul_i32 s72, s72, 15360
v_add_u32 v12, 0x20000, v12
v_add_u32 v13, 0x20000, v13
s_nop 3
v_mfma_f32_16x16x32_bf16 a[32:35], v[16:19], v[152:155], 0
s_add_u32 s71, s71, s72
s_lshl_b32 s73, s63, 8
s_lshl_b32 s74, s60, 8
s_nop 3
v_mfma_f32_16x16x32_bf16 a[32:35], v[20:23], v[156:159], a[32:35]
buffer_load_dwordx4 v5, s[12:15], s81 offen offset:1024 lds
s_nop 3
v_mfma_f32_16x16x32_bf16 a[36:39], v[24:27], v[152:155], 0
s_add_u32 s71, s71, 1114112
s_add_u32 s73, s73, 65536
s_add_u32 s74, s74, s63
s_nop 3
v_mfma_f32_16x16x32_bf16 a[36:39], v[28:31], v[156:159], a[36:39]
s_cmp_lt_u32 s74, 240
s_cselect_b32 s71, s71, 1073741824
s_cselect_b32 s73, s73, 1073741824
s_nop 3
v_mfma_f32_16x16x32_bf16 a[40:43], v[32:35], v[152:155], 0
s_waitcnt vmcnt(10) lgkmcnt(0)
s_barrier
ds_read_b64_tr_b16 v[48:49], v0 offset:32768
v_lshrrev_b32 v218, 4, v2
s_nop 3
v_mfma_f32_16x16x32_bf16 a[40:43], v[36:39], v[156:159], a[40:43]
buffer_load_dwordx4 v6, s[12:15], s81 offen offset:2048 lds
s_nop 3
v_mfma_f32_16x16x32_bf16 a[44:47], v[40:43], v[152:155], 0
s_lshl_b32 s70, s61, 2
ds_read_b64_tr_b16 v[50:51], v0 offset:33024
v_lshlrev_b32 v218, 28, v218
s_nop 3
v_mfma_f32_16x16x32_bf16 a[44:47], v[44:47], v[156:159], a[44:47]
s_add_u32 s70, s70, s62
ds_read_b64_tr_b16 v[52:53], v0 offset:36864
v_mov_b32 v216, s64
s_nop 3
v_mfma_f32_16x16x32_bf16 a[64:67], v[16:19], v[160:163], 0
s_add_u32 s70, s70, 16
ds_read_b64_tr_b16 v[54:55], v0 offset:37120
v_mov_b32 v217, s65
s_nop 3
v_mfma_f32_16x16x32_bf16 a[64:67], v[20:23], v[164:167], a[64:67]
buffer_load_dwordx4 v7, s[12:15], s81 offen offset:3072 lds
s_nop 3
v_mfma_f32_16x16x32_bf16 a[68:71], v[24:27], v[160:163], 0
s_add_u32 s81, s7, 1
s_and_b32 s81, s81, 7
ds_read_b64_tr_b16 v[56:57], v3 offset:32768
s_nop 3
v_mfma_f32_16x16x32_bf16 a[68:71], v[28:31], v[164:167], a[68:71]
s_add_u32 m0, s80, 32768
s_lshl_b32 s81, s81, 19
ds_read_b64_tr_b16 v[58:59], v3 offset:33024
s_nop 3
v_mfma_f32_16x16x32_bf16 a[72:75], v[32:35], v[160:163], 0
s_add_u32 s81, s81, s79
ds_read_b64_tr_b16 v[60:61], v3 offset:36864
v_lshl_add_u32 v218, v2, 3, v218
s_nop 3
v_mfma_f32_16x16x32_bf16 a[72:75], v[36:39], v[164:167], a[72:75]
buffer_load_dwordx4 v8, s[24:27], s81 offen offset:0 lds
s_nop 3
v_mfma_f32_16x16x32_bf16 a[76:79], v[40:43], v[160:163], 0
s_lshl_b32 s101, s60, 3
s_min_u32 s70, s70, 59
ds_read_b64_tr_b16 v[62:63], v3 offset:37120
s_nop 3
v_mfma_f32_16x16x32_bf16 a[76:79], v[44:47], v[164:167], a[76:79]
s_add_u32 s101, s101, 99328
s_lshl_b32 s72, s61, 2
ds_read_b64_tr_b16 v[64:65], v0 offset:33280
s_nop 3
v_mfma_f32_16x16x32_bf16 a[96:99], v[16:19], v[168:171], 0
s_add_u32 s72, s72, s62
s_sub_u32 s72, s72, 4
ds_read_b64_tr_b16 v[66:67], v0 offset:33536
s_nop 3
v_mfma_f32_16x16x32_bf16 a[96:99], v[20:23], v[172:175], a[96:99]
buffer_load_dwordx4 v9, s[24:27], s81 offen offset:1024 lds
s_nop 3
v_mfma_f32_16x16x32_bf16 a[100:103], v[24:27], v[168:171], 0
s_cmp_lt_u32 s62, 4
s_cselect_b32 s72, 255, s72
ds_read_b64_tr_b16 v[68:69], v0 offset:37376
s_nop 3
v_mfma_f32_16x16x32_bf16 a[100:103], v[28:31], v[172:175], a[100:103]
s_lshl_b32 s69, s72, 8
ds_read_b64_tr_b16 v[70:71], v0 offset:37632
v_add_u32 v0, 0x10000, v0
s_nop 3
v_mfma_f32_16x16x32_bf16 a[104:107], v[32:35], v[168:171], 0
ds_read_b64_tr_b16 v[72:73], v3 offset:33280
ds_read_b64_tr_b16 v[74:75], v3 offset:33536
ds_read_b64_tr_b16 v[76:77], v3 offset:37376
ds_read_b64_tr_b16 v[78:79], v3 offset:37632
ds_read_b32 v208, v212 offset:0
ds_read_b32 v209, v212 offset:256
ds_read_b32 v210, v212 offset:512
ds_read_b32 v211, v212 offset:768
s_nop 3
v_mfma_f32_16x16x32_bf16 a[104:107], v[36:39], v[172:175], a[104:107]
buffer_load_dwordx4 v10, s[24:27], s81 offen offset:2048 lds
s_nop 3
v_mfma_f32_16x16x32_bf16 a[108:111], v[40:43], v[168:171], 0
s_lshr_b32 s66, s63, 1
s_add_u32 s69, s69, 1119232
v_add_u32 v3, 0x10000, v3
s_nop 3
v_mfma_f32_16x16x32_bf16 a[108:111], v[44:47], v[172:175], a[108:111]
s_waitcnt lgkmcnt(9)
s_mul_i32 s66, s66, 20480
s_and_b32 s67, s63, 1
s_lshl_b32 s67, s67, 7
s_nop 3
v_mfma_f32_16x16x32_bf16 a[16:19], v[144:147], v[48:51], 0
s_add_u32 s66, s66, s67
s_lshl_b32 s67, s60, 11
s_add_u32 s66, s66, s67
s_nop 3
v_mfma_f32_16x16x32_bf16 a[16:19], v[148:151], v[52:55], a[16:19]
buffer_load_dwordx4 v11, s[24:27], s81 offen offset:3072 lds
s_nop 3
v_mfma_f32_16x16x32_bf16 a[20:23], v[144:147], v[56:59], 0
s_waitcnt vmcnt(8) lgkmcnt(0)
s_barrier
s_add_u32 s81, s7, 1
ds_read_b128 v[176:179], v12 offset:0
s_nop 3
v_mfma_f32_16x16x32_bf16 a[20:23], v[148:151], v[60:63], a[20:23]
s_and_b32 s81, s81, 7
ds_read_b128 v[180:183], v13 offset:0
v_add_f32 v208, v208, v209
s_nop 3
v_mfma_f32_16x16x32_bf16 a[24:27], v[144:147], v[64:67], 0
s_lshl_b32 s81, s81, 7
ds_read_b128 v[188:191], v12 offset:4096
v_add_f32 v210, v210, v211
s_nop 3
v_mfma_f32_16x16x32_bf16 a[24:27], v[148:151], v[68:71], a[24:27]
s_add_u32 m0, s80, 49152
ds_read_b128 v[192:195], v13 offset:4096
v_add_f32 v208, v208, v210
s_nop 3
v_mfma_f32_16x16x32_bf16 a[28:31], v[144:147], v[72:75], 0
buffer_store_dword v208, v213, s[52:55], s71 offen sc1
s_nop 3
v_mfma_f32_16x16x32_bf16 a[28:31], v[148:151], v[76:79], a[28:31]
s_add_u32 s81, s81, s78
s_add_u32 s81, s81, 1048576
ds_read_b128 v[144:147], v12 offset:2048
s_nop 3
v_mfma_f32_16x16x32_bf16 a[48:51], v[152:155], v[48:51], 0
s_lshl_b32 s71, s60, 8
s_add_u32 s71, s71, s72
ds_read_b128 v[148:151], v13 offset:2048
s_nop 3
v_mfma_f32_16x16x32_bf16 a[48:51], v[156:159], v[52:55], a[48:51]
s_add_u32 s66, s66, 2097152
s_add_u32 s67, s66, 8192
ds_read_b64_tr_b16 v[80:81], v0 offset:0
s_nop 3
v_mfma_f32_16x16x32_bf16 a[52:55], v[152:155], v[56:59], 0
buffer_load_dwordx4 v4, s[12:15], s81 offen offset:0 lds
s_nop 3
v_mfma_f32_16x16x32_bf16 a[52:55], v[156:159], v[60:63], a[52:55]
s_add_u32 s68, s66, 16384
s_add_u32 s94, s62, 0
ds_read_b64_tr_b16 v[82:83], v0 offset:256
s_nop 3
v_mfma_f32_16x16x32_bf16 a[56:59], v[152:155], v[64:67], 0
s_cmp_lt_u32 s60, 2
s_cselect_b32 s68, s68, 1073741824
ds_read_b64_tr_b16 v[84:85], v0 offset:4096
s_nop 3
v_mfma_f32_16x16x32_bf16 a[56:59], v[156:159], v[68:71], a[56:59]
s_sub_u32 s5, s94, 10
s_min_u32 s94, s94, s5
ds_read_b64_tr_b16 v[86:87], v0 offset:4352
s_nop 3
v_mfma_f32_16x16x32_bf16 a[60:63], v[152:155], v[72:75], 0
buffer_load_dwordx4 v5, s[12:15], s81 offen offset:1024 lds
s_nop 3
v_mfma_f32_16x16x32_bf16 a[60:63], v[156:159], v[76:79], a[60:63]
s_lshl_b32 s94, s94, 11
s_add_u32 s0, s4, 0
ds_read_b64_tr_b16 v[88:89], v3 offset:0
s_nop 3
v_mfma_f32_16x16x32_bf16 a[80:83], v[160:163], v[48:51], 0
s_add_u32 s1, s4, 4096
s_add_u32 s2, s82, 0
ds_read_b64_tr_b16 v[90:91], v3 offset:256
s_nop 3
v_mfma_f32_16x16x32_bf16 a[80:83], v[164:167], v[52:55], a[80:83]
s_add_u32 s3, s82, 4096
s_lshl_b32 s93, s61, 3
ds_read_b64_tr_b16 v[92:93], v3 offset:4096
s_nop 3
v_mfma_f32_16x16x32_bf16 a[84:87], v[160:163], v[56:59], 0
buffer_load_dwordx4 v6, s[12:15], s81 offen offset:2048 lds
s_nop 3
v_mfma_f32_16x16x32_bf16 a[84:87], v[164:167], v[60:63], a[84:87]
s_add_u32 s93, s93, s62
s_lshl_b32 s93, s93, 8
ds_read_b64_tr_b16 v[94:95], v3 offset:4352
s_nop 3
v_mfma_f32_16x16x32_bf16 a[88:91], v[160:163], v[64:67], 0
s_add_u32 s93, s93, 196608
s_mov_b32 s59, 0x20000
ds_read_b64_tr_b16 v[96:97], v0 offset:512
s_nop 3
v_mfma_f32_16x16x32_bf16 a[88:91], v[164:167], v[68:71], a[88:91]
s_cmp_eq_u32 s60, 3
s_cselect_b32 s93, s93, 1073741824
ds_read_b64_tr_b16 v[98:99], v0 offset:768
s_nop 3
v_mfma_f32_16x16x32_bf16 a[92:95], v[160:163], v[72:75], 0
buffer_load_dwordx4 v7, s[12:15], s81 offen offset:3072 lds
s_nop 3
v_mfma_f32_16x16x32_bf16 a[92:95], v[164:167], v[76:79], a[92:95]
s_lshl_b32 s77, s60, 4
s_mov_b32 s85, 442368
ds_read_b64_tr_b16 v[100:101], v0 offset:4608
s_nop 3
v_mfma_f32_16x16x32_bf16 a[112:115], v[168:171], v[48:51], 0
ds_read_b64_tr_b16 v[102:103], v0 offset:4864
ds_read_b64_tr_b16 v[104:105], v3 offset:512
ds_read_b64_tr_b16 v[106:107], v3 offset:768
ds_read_b64_tr_b16 v[108:109], v3 offset:4608
ds_read_b64_tr_b16 v[110:111], v3 offset:4864
ds_read_b128 v[196:199], v12 offset:6144
ds_read_b128 v[200:203], v13 offset:6144
s_nop 3
v_mfma_f32_16x16x32_bf16 a[112:115], v[172:175], v[52:55], a[112:115]
s_lshl_b32 s90, s60, 12
v_add_u32 v12, 0xfffe0000, v12
v_add_u32 v13, 0xfffe0000, v13
s_nop 3
v_mfma_f32_16x16x32_bf16 a[116:119], v[168:171], v[56:59], 0
s_add_u32 s90, s90, 131072
v_add_u32 v0, 0xfffe4000, v0
v_add_u32 v3, 0xfffe4000, v3
s_nop 3
v_mfma_f32_16x16x32_bf16 a[116:119], v[172:175], v[60:63], a[116:119]
s_mov_b32 s83, 0x20000
s_nop 3
v_mfma_f32_16x16x32_bf16 a[120:123], v[168:171], v[64:67], 0
s_add_u32 s81, s7, 2
s_and_b32 s81, s81, 7
s_lshl_b32 s81, s81, 19
s_nop 3
v_mfma_f32_16x16x32_bf16 a[120:123], v[172:175], v[68:71], a[120:123]
s_add_u32 s81, s81, s79
s_nop 3
v_mfma_f32_16x16x32_bf16 a[124:127], v[168:171], v[72:75], 0
s_waitcnt vmcnt(5) lgkmcnt(0)
L_pad_a_0:
s_cbranch_execz L_pad_a_0_1
s_barrier
ds_read_b128 v[168:171], v12 offset:0
s_nop 3
v_mfma_f32_16x16x32_bf16 a[124:127], v[172:175], v[76:79], a[124:127]
s_add_u32 m0, s80, 65536
ds_read_b128 v[172:175], v13 offset:0
v_mfma_f32_16x16x32_bf16 a[128:131], v[16:19], v[176:179], 0
ds_read_b128 v[152:155], v12 offset:6144
s_nop 3
v_mfma_f32_16x16x32_bf16 a[128:131], v[20:23], v[180:183], a[128:131]
buffer_load_dwordx4 v8, s[20:23], s81 offen offset:0 lds
v_mfma_f32_16x16x32_bf16 a[132:135], v[24:27], v[176:179], 0
ds_read_b64_tr_b16 v[112:113], v0 offset:0
s_nop 3
v_mfma_f32_16x16x32_bf16 a[132:135], v[28:31], v[180:183], a[132:135]
ds_read_b128 v[156:159], v13 offset:6144
v_mfma_f32_16x16x32_bf16 a[136:139], v[32:35], v[176:179], 0
s_nop 3
v_mfma_f32_16x16x32_bf16 a[136:139], v[36:39], v[180:183], a[136:139]
ds_read_b64_tr_b16 v[114:115], v0 offset:256
v_mfma_f32_16x16x32_bf16 a[140:143], v[40:43], v[176:179], 0
buffer_load_dwordx4 v9, s[20:23], s81 offen offset:1024 lds
s_nop 3
v_mfma_f32_16x16x32_bf16 a[140:143], v[44:47], v[180:183], a[140:143]
v_mfma_f32_16x16x32_bf16 a[144:147], v[176:179], v[48:51], 0
ds_read_b64_tr_b16 v[116:117], v0 offset:4096
s_nop 3
v_mfma_f32_16x16x32_bf16 a[144:147], v[180:183], v[52:55], a[144:147]
v_mfma_f32_16x16x32_bf16 a[148:151], v[176:179], v[56:59], 0
s_nop 3
v_mfma_f32_16x16x32_bf16 a[148:151], v[180:183], v[60:63], a[148:151]
buffer_load_dwordx4 v10, s[20:23], s81 offen offset:2048 lds
v_mfma_f32_16x16x32_bf16 a[152:155], v[176:179], v[64:67], 0
ds_read_b64_tr_b16 v[118:119], v0 offset:4352
s_nop 3
v_mfma_f32_16x16x32_bf16 a[152:155], v[180:183], v[68:71], a[152:155]
v_mfma_f32_16x16x32_bf16 a[156:159], v[176:179], v[72:75], 0
ds_read_b64_tr_b16 v[120:121], v3 offset:0
s_nop 3
v_mfma_f32_16x16x32_bf16 a[156:159], v[180:183], v[76:79], a[156:159]
ds_read_b128 v[176:179], v12 offset:2048
v_mfma_f32_16x16x32_bf16 a[160:163], v[16:19], v[144:147], 0
buffer_load_dwordx4 v11, s[20:23], s81 offen offset:3072 lds
s_nop 3
v_mfma_f32_16x16x32_bf16 a[160:163], v[20:23], v[148:151], a[160:163]
s_add_u32 s81, s7, 2
s_and_b32 s81, s81, 7
ds_read_b128 v[180:183], v13 offset:2048
v_mfma_f32_16x16x32_bf16 a[164:167], v[24:27], v[144:147], 0
s_lshl_b32 s81, s81, 7
s_add_u32 s81, s81, s78
ds_read_b64_tr_b16 v[122:123], v3 offset:256
s_nop 3
v_mfma_f32_16x16x32_bf16 a[164:167], v[28:31], v[148:151], a[164:167]
s_add_u32 m0, s80, 81920
v_mfma_f32_16x16x32_bf16 a[168:171], v[32:35], v[144:147], 0
ds_read_b64_tr_b16 v[124:125], v3 offset:4096
s_nop 3
v_mfma_f32_16x16x32_bf16 a[168:171], v[36:39], v[148:151], a[168:171]
buffer_load_dwordx4 v4, s[12:15], s81 offen offset:0 lds
v_mfma_f32_16x16x32_bf16 a[172:175], v[40:43], v[144:147], 0
s_nop 3
v_mfma_f32_16x16x32_bf16 a[172:175], v[44:47], v[148:151], a[172:175]
ds_read_b64_tr_b16 v[126:127], v3 offset:4352
v_mfma_f32_16x16x32_bf16 a[176:179], v[144:147], v[48:51], 0
s_nop 3
v_mfma_f32_16x16x32_bf16 a[176:179], v[148:151], v[52:55], a[176:179]
v_mfma_f32_16x16x32_bf16 a[180:183], v[144:147], v[56:59], 0
buffer_load_dwordx4 v5, s[12:15], s81 offen offset:1024 lds
s_nop 3
v_mfma_f32_16x16x32_bf16 a[180:183], v[148:151], v[60:63], a[180:183]
ds_read_b64_tr_b16 v[128:129], v0 offset:512
v_mfma_f32_16x16x32_bf16 a[184:187], v[144:147], v[64:67], 0
s_nop 3
v_mfma_f32_16x16x32_bf16 a[184:187], v[148:151], v[68:71], a[184:187]
ds_read_b64_tr_b16 v[130:131], v0 offset:768
v_mfma_f32_16x16x32_bf16 a[188:191], v[144:147], v[72:75], 0
ds_read_b128 v[144:147], v12 offset:4096
s_nop 3
v_mfma_f32_16x16x32_bf16 a[188:191], v[148:151], v[76:79], a[188:191]
buffer_load_dwordx4 v6, s[12:15], s81 offen offset:2048 lds
v_mfma_f32_16x16x32_bf16 a[192:195], v[16:19], v[188:191], 0
ds_read_b64_tr_b16 v[132:133], v0 offset:4608
s_nop 3
v_mfma_f32_16x16x32_bf16 a[192:195], v[20:23], v[192:195], a[192:195]
ds_read_b128 v[148:151], v13 offset:4096
v_mfma_f32_16x16x32_bf16 a[196:199], v[24:27], v[188:191], 0
s_nop 3
v_mfma_f32_16x16x32_bf16 a[196:199], v[28:31], v[192:195], a[196:199]
ds_read_b64_tr_b16 v[134:135], v0 offset:4864
v_mfma_f32_16x16x32_bf16 a[200:203], v[32:35], v[188:191], 0
buffer_load_dwordx4 v7, s[12:15], s81 offen offset:3072 lds
)ASM"
R"ASM(
s_nop 3
v_mfma_f32_16x16x32_bf16 a[200:203], v[36:39], v[192:195], a[200:203]
s_add_u32 s81, s7, 2
s_and_b32 s81, s81, 7
s_lshl_b32 s81, s81, 19
v_mfma_f32_16x16x32_bf16 a[204:207], v[40:43], v[188:191], 0
s_add_u32 s81, s81, s79
ds_read_b64_tr_b16 v[136:137], v3 offset:512
s_nop 3
v_mfma_f32_16x16x32_bf16 a[204:207], v[44:47], v[192:195], a[204:207]
s_add_u32 m0, s80, 16384
v_mfma_f32_16x16x32_bf16 a[208:211], v[188:191], v[48:51], 0
s_nop 3
v_mfma_f32_16x16x32_bf16 a[208:211], v[192:195], v[52:55], a[208:211]
buffer_load_dwordx4 v8, s[24:27], s81 offen offset:0 lds
v_mfma_f32_16x16x32_bf16 a[212:215], v[188:191], v[56:59], 0
ds_read_b64_tr_b16 v[138:139], v3 offset:768
s_nop 3
v_mfma_f32_16x16x32_bf16 a[212:215], v[192:195], v[60:63], a[212:215]
v_mfma_f32_16x16x32_bf16 a[216:219], v[188:191], v[64:67], 0
ds_read_b64_tr_b16 v[140:141], v3 offset:4608
s_nop 3
v_mfma_f32_16x16x32_bf16 a[216:219], v[192:195], v[68:71], a[216:219]
v_mfma_f32_16x16x32_bf16 a[220:223], v[188:191], v[72:75], 0
buffer_load_dwordx4 v9, s[24:27], s81 offen offset:1024 lds
s_nop 3
v_mfma_f32_16x16x32_bf16 a[220:223], v[192:195], v[76:79], a[220:223]
ds_read_b64_tr_b16 v[142:143], v3 offset:4864
v_mfma_f32_16x16x32_bf16 a[224:227], v[16:19], v[196:199], 0
s_nop 3
v_mfma_f32_16x16x32_bf16 a[224:227], v[20:23], v[200:203], a[224:227]
v_mfma_f32_16x16x32_bf16 a[228:231], v[24:27], v[196:199], 0
s_nop 3
v_mfma_f32_16x16x32_bf16 a[228:231], v[28:31], v[200:203], a[228:231]
buffer_load_dwordx4 v10, s[24:27], s81 offen offset:2048 lds
v_mfma_f32_16x16x32_bf16 a[232:235], v[32:35], v[196:199], 0
s_nop 3
v_mfma_f32_16x16x32_bf16 a[232:235], v[36:39], v[200:203], a[232:235]
v_mfma_f32_16x16x32_bf16 a[236:239], v[40:43], v[196:199], 0
s_nop 3
v_mfma_f32_16x16x32_bf16 a[236:239], v[44:47], v[200:203], a[236:239]
v_mfma_f32_16x16x32_bf16 a[240:243], v[196:199], v[48:51], 0
buffer_load_dwordx4 v11, s[24:27], s81 offen offset:3072 lds
s_nop 3
v_mfma_f32_16x16x32_bf16 a[240:243], v[200:203], v[52:55], a[240:243]
v_mfma_f32_16x16x32_bf16 a[244:247], v[196:199], v[56:59], 0
s_nop 3
v_mfma_f32_16x16x32_bf16 a[244:247], v[200:203], v[60:63], a[244:247]
v_mfma_f32_16x16x32_bf16 a[248:251], v[196:199], v[64:67], 0
buffer_load_dwordx4 v[244:247], v184, s[8:11], s75 offen offset:0
s_nop 3
v_mfma_f32_16x16x32_bf16 a[248:251], v[200:203], v[68:71], a[248:251]
s_add_u32 s81, s7, 2
s_and_b32 s81, s81, 7
s_lshl_b32 s81, s81, 7
v_mfma_f32_16x16x32_bf16 a[252:255], v[196:199], v[72:75], 0
s_waitcnt vmcnt(9) lgkmcnt(0)
s_barrier
s_add_u32 s81, s81, s78
ds_read_b128 v[160:163], v12 offset:49152
s_nop 3
v_mfma_f32_16x16x32_bf16 a[252:255], v[200:203], v[76:79], a[252:255]
s_add_u32 m0, s80, 114688
s_add_u32 s81, s81, 1048576
ds_read_b128 v[164:167], v13 offset:49152
v_mfma_f32_16x16x32_bf16 a[0:3], v[80:83], v[168:171], a[0:3]
v_mfma_f32_16x16x32_bf16 a[0:3], v[84:87], v[172:175], a[0:3]
buffer_load_dwordx4 v4, s[12:15], s81 offen offset:0 lds
v_mfma_f32_16x16x32_bf16 a[4:7], v[88:91], v[168:171], a[4:7]
ds_read_b64_tr_b16 v[16:17], v0 offset:32768
s_nop 3
v_mfma_f32_16x16x32_bf16 a[4:7], v[92:95], v[172:175], a[4:7]
v_mfma_f32_16x16x32_bf16 a[8:11], v[96:99], v[168:171], a[8:11]
v_mfma_f32_16x16x32_bf16 a[8:11], v[100:103], v[172:175], a[8:11]
buffer_load_dwordx4 v[248:251], v184, s[8:11], s75 offen offset:512
v_mfma_f32_16x16x32_bf16 a[12:15], v[104:107], v[168:171], a[12:15]
ds_read_b64_tr_b16 v[18:19], v0 offset:33024
s_nop 3
v_mfma_f32_16x16x32_bf16 a[12:15], v[108:111], v[172:175], a[12:15]
v_mfma_f32_16x16x32_bf16 a[16:19], v[168:171], v[112:115], a[16:19]
ds_read_b64_tr_b16 v[20:21], v0 offset:36864
v_mfma_f32_16x16x32_bf16 a[16:19], v[172:175], v[116:119], a[16:19]
v_mfma_f32_16x16x32_bf16 a[20:23], v[168:171], v[120:123], a[20:23]
buffer_load_dwordx4 v[252:255], v184, s[8:11], s76 offen offset:1024
s_nop 3
v_mfma_f32_16x16x32_bf16 a[20:23], v[172:175], v[124:127], a[20:23]
ds_read_b64_tr_b16 v[22:23], v0 offset:37120
v_mfma_f32_16x16x32_bf16 a[24:27], v[168:171], v[128:131], a[24:27]
v_mfma_f32_16x16x32_bf16 a[24:27], v[172:175], v[132:135], a[24:27]
v_mfma_f32_16x16x32_bf16 a[28:31], v[168:171], v[136:139], a[28:31]
buffer_store_dwordx2 v[216:217], v218, s[52:55], s73 offen sc1
s_nop 3
v_mfma_f32_16x16x32_bf16 a[28:31], v[172:175], v[140:143], a[28:31]
s_mov_b32 s73, s63
s_lshl_b32 s73, s73, 8
ds_read_b64_tr_b16 v[24:25], v3 offset:32768
v_mfma_f32_16x16x32_bf16 a[32:35], v[80:83], v[176:179], a[32:35]
s_add_u32 s73, s73, 131072
ds_read_b128 v[168:171], v12 offset:51200
v_mfma_f32_16x16x32_bf16 a[32:35], v[84:87], v[180:183], a[32:35]
s_cmp_eq_u32 s60, 3
s_cselect_b32 s73, s73, 1073741824
ds_read_b128 v[172:175], v13 offset:51200
v_mfma_f32_16x16x32_bf16 a[36:39], v[88:91], v[176:179], a[36:39]
buffer_load_dwordx4 v5, s[12:15], s81 offen offset:1024 lds
s_nop 3
v_mfma_f32_16x16x32_bf16 a[36:39], v[92:95], v[180:183], a[36:39]
ds_read_b64_tr_b16 v[26:27], v3 offset:33024
v_mfma_f32_16x16x32_bf16 a[40:43], v[96:99], v[176:179], a[40:43]
ds_read_b64_tr_b16 v[28:29], v3 offset:36864
v_mfma_f32_16x16x32_bf16 a[40:43], v[100:103], v[180:183], a[40:43]
v_mfma_f32_16x16x32_bf16 a[44:47], v[104:107], v[176:179], a[44:47]
s_nop 3
v_mfma_f32_16x16x32_bf16 a[44:47], v[108:111], v[180:183], a[44:47]
ds_read_b64_tr_b16 v[30:31], v3 offset:37120
v_mfma_f32_16x16x32_bf16 a[48:51], v[176:179], v[112:115], a[48:51]
v_mfma_f32_16x16x32_bf16 a[48:51], v[180:183], v[116:119], a[48:51]
v_mfma_f32_16x16x32_bf16 a[52:55], v[176:179], v[120:123], a[52:55]
ds_read_b64_tr_b16 v[32:33], v0 offset:33280
s_nop 3
v_mfma_f32_16x16x32_bf16 a[52:55], v[180:183], v[124:127], a[52:55]
v_mfma_f32_16x16x32_bf16 a[56:59], v[176:179], v[128:131], a[56:59]
v_mfma_f32_16x16x32_bf16 a[56:59], v[180:183], v[132:135], a[56:59]
ds_read_b64_tr_b16 v[34:35], v0 offset:33536
v_mfma_f32_16x16x32_bf16 a[60:63], v[176:179], v[136:139], a[60:63]
ds_read_b128 v[176:179], v12 offset:53248
s_nop 3
v_mfma_f32_16x16x32_bf16 a[60:63], v[180:183], v[140:143], a[60:63]
buffer_load_dwordx4 v6, s[12:15], s81 offen offset:2048 lds
v_mfma_f32_16x16x32_bf16 a[64:67], v[80:83], v[144:147], a[64:67]
ds_read_b64_tr_b16 v[36:37], v0 offset:37376
v_mfma_f32_16x16x32_bf16 a[64:67], v[84:87], v[148:151], a[64:67]
ds_read_b128 v[180:183], v13 offset:53248
v_mfma_f32_16x16x32_bf16 a[68:71], v[88:91], v[144:147], a[68:71]
s_nop 3
v_mfma_f32_16x16x32_bf16 a[68:71], v[92:95], v[148:151], a[68:71]
ds_read_b64_tr_b16 v[38:39], v0 offset:37632
v_add_u32 v0, 0xffffc000, v0
v_mfma_f32_16x16x32_bf16 a[72:75], v[96:99], v[144:147], a[72:75]
v_mfma_f32_16x16x32_bf16 a[72:75], v[100:103], v[148:151], a[72:75]
v_mfma_f32_16x16x32_bf16 a[76:79], v[104:107], v[144:147], a[76:79]
ds_read_b64_tr_b16 v[40:41], v3 offset:33280
s_nop 3
v_mfma_f32_16x16x32_bf16 a[76:79], v[108:111], v[148:151], a[76:79]
v_mfma_f32_16x16x32_bf16 a[80:83], v[144:147], v[112:115], a[80:83]
v_mfma_f32_16x16x32_bf16 a[80:83], v[148:151], v[116:119], a[80:83]
ds_read_b64_tr_b16 v[42:43], v3 offset:33536
v_mfma_f32_16x16x32_bf16 a[84:87], v[144:147], v[120:123], a[84:87]
s_nop 3
v_mfma_f32_16x16x32_bf16 a[84:87], v[148:151], v[124:127], a[84:87]
v_mfma_f32_16x16x32_bf16 a[88:91], v[144:147], v[128:131], a[88:91]
ds_read_b64_tr_b16 v[44:45], v3 offset:37376
v_mfma_f32_16x16x32_bf16 a[88:91], v[148:151], v[132:135], a[88:91]
v_mfma_f32_16x16x32_bf16 a[92:95], v[144:147], v[136:139], a[92:95]
buffer_load_dwordx4 v7, s[12:15], s81 offen offset:3072 lds
s_nop 3
v_mfma_f32_16x16x32_bf16 a[92:95], v[148:151], v[140:143], a[92:95]
ds_read_b64_tr_b16 v[46:47], v3 offset:37632
v_add_u32 v3, 0xffffc000, v3
v_mfma_f32_16x16x32_bf16 a[96:99], v[80:83], v[152:155], a[96:99]
ds_read_b128 v[144:147], v12 offset:55296
v_add_u32 v12, 0x14000, v12
v_mfma_f32_16x16x32_bf16 a[96:99], v[84:87], v[156:159], a[96:99]
ds_read_b128 v[148:151], v13 offset:55296
v_add_u32 v13, 0x14000, v13
v_mfma_f32_16x16x32_bf16 a[100:103], v[88:91], v[152:155], a[100:103]
s_nop 3
v_mfma_f32_16x16x32_bf16 a[100:103], v[92:95], v[156:159], a[100:103]
v_mfma_f32_16x16x32_bf16 a[104:107], v[96:99], v[152:155], a[104:107]
v_mfma_f32_16x16x32_bf16 a[104:107], v[100:103], v[156:159], a[104:107]
v_mfma_f32_16x16x32_bf16 a[108:111], v[104:107], v[152:155], a[108:111]
s_nop 3
v_mfma_f32_16x16x32_bf16 a[108:111], v[108:111], v[156:159], a[108:111]
v_mfma_f32_16x16x32_bf16 a[112:115], v[152:155], v[112:115], a[112:115]
v_mfma_f32_16x16x32_bf16 a[112:115], v[156:159], v[116:119], a[112:115]
v_mfma_f32_16x16x32_bf16 a[116:119], v[152:155], v[120:123], a[116:119]
s_nop 3
v_mfma_f32_16x16x32_bf16 a[116:119], v[156:159], v[124:127], a[116:119]
v_mfma_f32_16x16x32_bf16 a[120:123], v[152:155], v[128:131], a[120:123]
s_add_u32 s81, s7, 3
s_and_b32 s81, s81, 7
s_lshl_b32 s81, s81, 19
v_mfma_f32_16x16x32_bf16 a[120:123], v[156:159], v[132:135], a[120:123]
s_waitcnt vmcnt(8) lgkmcnt(0)
s_add_u32 s81, s81, s79
v_mfma_f32_16x16x32_bf16 a[124:127], v[152:155], v[136:139], a[124:127]
s_barrier
ds_read_b128 v[152:155], v12 offset:0
s_nop 3
v_mfma_f32_16x16x32_bf16 a[124:127], v[156:159], v[140:143], a[124:127]
s_add_u32 m0, s80, 131072
ds_read_b128 v[156:159], v13 offset:0
v_mfma_f32_16x16x32_bf16 a[128:131], v[80:83], v[160:163], a[128:131]
v_mfma_f32_16x16x32_bf16 a[128:131], v[84:87], v[164:167], a[128:131]
buffer_load_dwordx4 v8, s[20:23], s81 offen offset:0 lds
v_mfma_f32_16x16x32_bf16 a[132:135], v[88:91], v[160:163], a[132:135]
ds_read_b64_tr_b16 v[48:49], v0 offset:0
v_mfma_f32_16x16x32_bf16 a[132:135], v[92:95], v[164:167], a[132:135]
v_mfma_f32_16x16x32_bf16 a[136:139], v[96:99], v[160:163], a[136:139]
v_mfma_f32_16x16x32_bf16 a[136:139], v[100:103], v[164:167], a[136:139]
ds_read_b64_tr_b16 v[50:51], v0 offset:256
v_mfma_f32_16x16x32_bf16 a[140:143], v[104:107], v[160:163], a[140:143]
buffer_load_dwordx4 v9, s[20:23], s81 offen offset:1024 lds
s_nop 3
v_mfma_f32_16x16x32_bf16 a[140:143], v[108:111], v[164:167], a[140:143]
v_mfma_f32_16x16x32_bf16 a[144:147], v[160:163], v[112:115], a[144:147]
ds_read_b64_tr_b16 v[52:53], v0 offset:4096
v_mfma_f32_16x16x32_bf16 a[144:147], v[164:167], v[116:119], a[144:147]
v_mfma_f32_16x16x32_bf16 a[148:151], v[160:163], v[120:123], a[148:151]
v_mfma_f32_16x16x32_bf16 a[148:151], v[164:167], v[124:127], a[148:151]
buffer_load_dwordx4 v10, s[20:23], s81 offen offset:2048 lds
v_mfma_f32_16x16x32_bf16 a[152:155], v[160:163], v[128:131], a[152:155]
ds_read_b64_tr_b16 v[54:55], v0 offset:4352
v_mfma_f32_16x16x32_bf16 a[152:155], v[164:167], v[132:135], a[152:155]
v_mfma_f32_16x16x32_bf16 a[156:159], v[160:163], v[136:139], a[156:159]
ds_read_b64_tr_b16 v[56:57], v3 offset:0
s_nop 3
v_mfma_f32_16x16x32_bf16 a[156:159], v[164:167], v[140:143], a[156:159]
ds_read_b128 v[160:163], v12 offset:2048
v_mfma_f32_16x16x32_bf16 a[160:163], v[80:83], v[168:171], a[160:163]
buffer_load_dwordx4 v11, s[20:23], s81 offen offset:3072 lds
v_mfma_f32_16x16x32_bf16 a[160:163], v[84:87], v[172:175], a[160:163]
s_add_u32 s81, s7, 3
s_and_b32 s81, s81, 7
ds_read_b128 v[164:167], v13 offset:2048
v_mfma_f32_16x16x32_bf16 a[164:167], v[88:91], v[168:171], a[164:167]
s_lshl_b32 s81, s81, 7
s_add_u32 s81, s81, s78
ds_read_b64_tr_b16 v[58:59], v3 offset:256
v_mfma_f32_16x16x32_bf16 a[164:167], v[92:95], v[172:175], a[164:167]
s_add_u32 m0, s80, 147456
v_mfma_f32_16x16x32_bf16 a[168:171], v[96:99], v[168:171], a[168:171]
ds_read_b64_tr_b16 v[60:61], v3 offset:4096
v_mfma_f32_16x16x32_bf16 a[168:171], v[100:103], v[172:175], a[168:171]
buffer_load_dwordx4 v4, s[12:15], s81 offen offset:0 lds
v_mfma_f32_16x16x32_bf16 a[172:175], v[104:107], v[168:171], a[172:175]
s_nop 3
v_mfma_f32_16x16x32_bf16 a[172:175], v[108:111], v[172:175], a[172:175]
s_waitcnt vmcnt(12)
ds_read_b64_tr_b16 v[62:63], v3 offset:4352
v_lshlrev_b32 v184, 16, v244
v_mfma_f32_16x16x32_bf16 a[176:179], v[168:171], v[112:115], a[176:179]
v_and_b32 v187, 0xffff0000, v245
v_lshlrev_b32 v186, 16, v245
v_mfma_f32_16x16x32_bf16 a[176:179], v[172:175], v[116:119], a[176:179]
v_and_b32 v185, 0xffff0000, v244
v_mul_f32 v245, v184, v184
v_mfma_f32_16x16x32_bf16 a[180:183], v[168:171], v[120:123], a[180:183]
buffer_load_dwordx4 v5, s[12:15], s81 offen offset:1024 lds
v_mfma_f32_16x16x32_bf16 a[180:183], v[172:175], v[124:127], a[180:183]
ds_read_b64_tr_b16 v[64:65], v0 offset:512
v_add_f32 v244, v184, v185
v_mfma_f32_16x16x32_bf16 a[184:187], v[168:171], v[128:131], a[184:187]
v_fmac_f32 v245, v185, v185
v_add_f32 v244, v244, v186
v_mfma_f32_16x16x32_bf16 a[184:187], v[172:175], v[132:135], a[184:187]
ds_read_b64_tr_b16 v[66:67], v0 offset:768
v_fmac_f32 v245, v186, v186
v_mfma_f32_16x16x32_bf16 a[188:191], v[168:171], v[136:139], a[188:191]
ds_read_b128 v[168:171], v12 offset:4096
v_lshlrev_b32 v188, 16, v246
s_nop 3
v_mfma_f32_16x16x32_bf16 a[188:191], v[172:175], v[140:143], a[188:191]
buffer_load_dwordx4 v6, s[12:15], s81 offen offset:2048 lds
v_mfma_f32_16x16x32_bf16 a[192:195], v[80:83], v[176:179], a[192:195]
ds_read_b64_tr_b16 v[68:69], v0 offset:4608
v_add_f32 v244, v244, v187
v_mfma_f32_16x16x32_bf16 a[192:195], v[84:87], v[180:183], a[192:195]
ds_read_b128 v[172:175], v13 offset:4096
v_fmac_f32 v245, v187, v187
v_mfma_f32_16x16x32_bf16 a[196:199], v[88:91], v[176:179], a[196:199]
v_and_b32 v189, 0xffff0000, v246
v_add_f32 v244, v244, v188
v_mfma_f32_16x16x32_bf16 a[196:199], v[92:95], v[180:183], a[196:199]
ds_read_b64_tr_b16 v[70:71], v0 offset:4864
v_add_u32 v0, 0x1c000, v0
v_mfma_f32_16x16x32_bf16 a[200:203], v[96:99], v[176:179], a[200:203]
buffer_load_dwordx4 v7, s[12:15], s81 offen offset:3072 lds
v_mfma_f32_16x16x32_bf16 a[200:203], v[100:103], v[180:183], a[200:203]
s_add_u32 s81, s7, 3
s_and_b32 s81, s81, 7
s_lshl_b32 s81, s81, 19
v_mfma_f32_16x16x32_bf16 a[204:207], v[104:107], v[176:179], a[204:207]
s_add_u32 s81, s81, s79
ds_read_b64_tr_b16 v[72:73], v3 offset:512
v_fmac_f32 v245, v188, v188
s_nop 3
v_mfma_f32_16x16x32_bf16 a[204:207], v[108:111], v[180:183], a[204:207]
s_add_u32 m0, s80, 0
v_lshlrev_b32 v190, 16, v247
v_add_f32 v244, v244, v189
v_mfma_f32_16x16x32_bf16 a[208:211], v[176:179], v[112:115], a[208:211]
v_and_b32 v191, 0xffff0000, v247
v_fmac_f32 v245, v189, v189
v_mfma_f32_16x16x32_bf16 a[208:211], v[180:183], v[116:119], a[208:211]
buffer_load_dwordx4 v8, s[24:27], s81 offen offset:0 lds
v_mfma_f32_16x16x32_bf16 a[212:215], v[176:179], v[120:123], a[212:215]
ds_read_b64_tr_b16 v[74:75], v3 offset:768
v_add_f32 v244, v244, v190
v_mfma_f32_16x16x32_bf16 a[212:215], v[180:183], v[124:127], a[212:215]
s_waitcnt vmcnt(14)
v_lshlrev_b32 v192, 16, v248
v_fmac_f32 v245, v190, v190
v_mfma_f32_16x16x32_bf16 a[216:219], v[176:179], v[128:131], a[216:219]
ds_read_b64_tr_b16 v[76:77], v3 offset:4608
v_add_f32 v244, v244, v191
v_mfma_f32_16x16x32_bf16 a[216:219], v[180:183], v[132:135], a[216:219]
v_and_b32 v193, 0xffff0000, v248
v_fmac_f32 v245, v191, v191
v_mfma_f32_16x16x32_bf16 a[220:223], v[176:179], v[136:139], a[220:223]
buffer_load_dwordx4 v9, s[24:27], s81 offen offset:1024 lds
s_nop 3
v_mfma_f32_16x16x32_bf16 a[220:223], v[180:183], v[140:143], a[220:223]
ds_read_b64_tr_b16 v[78:79], v3 offset:4864
v_add_u32 v3, 0x1c000, v3
v_mfma_f32_16x16x32_bf16 a[224:227], v[80:83], v[144:147], a[224:227]
ds_read_b128 v[176:179], v12 offset:6144
v_add_f32 v244, v244, v192
v_mfma_f32_16x16x32_bf16 a[224:227], v[84:87], v[148:151], a[224:227]
ds_read_b128 v[180:183], v13 offset:6144
v_fmac_f32 v245, v192, v192
v_mfma_f32_16x16x32_bf16 a[228:231], v[88:91], v[144:147], a[228:231]
v_lshlrev_b32 v194, 16, v249
v_add_f32 v244, v244, v193
v_mfma_f32_16x16x32_bf16 a[228:231], v[92:95], v[148:151], a[228:231]
buffer_load_dwordx4 v10, s[24:27], s81 offen offset:2048 lds
v_mfma_f32_16x16x32_bf16 a[232:235], v[96:99], v[144:147], a[232:235]
v_and_b32 v195, 0xffff0000, v249
v_fmac_f32 v245, v193, v193
v_mfma_f32_16x16x32_bf16 a[232:235], v[100:103], v[148:151], a[232:235]
v_add_f32 v244, v244, v194
v_fmac_f32 v245, v194, v194
v_mfma_f32_16x16x32_bf16 a[236:239], v[104:107], v[144:147], a[236:239]
v_lshlrev_b32 v196, 16, v250
v_add_f32 v244, v244, v195
s_nop 3
v_mfma_f32_16x16x32_bf16 a[236:239], v[108:111], v[148:151], a[236:239]
v_and_b32 v197, 0xffff0000, v250
v_fmac_f32 v245, v195, v195
v_mfma_f32_16x16x32_bf16 a[240:243], v[144:147], v[112:115], a[240:243]
buffer_load_dwordx4 v11, s[24:27], s81 offen offset:3072 lds
v_mfma_f32_16x16x32_bf16 a[240:243], v[148:151], v[116:119], a[240:243]
v_add_f32 v244, v244, v196
v_fmac_f32 v245, v196, v196
v_mfma_f32_16x16x32_bf16 a[244:247], v[144:147], v[120:123], a[244:247]
v_lshlrev_b32 v198, 16, v251
v_add_f32 v244, v244, v197
v_mfma_f32_16x16x32_bf16 a[244:247], v[148:151], v[124:127], a[244:247]
v_and_b32 v199, 0xffff0000, v251
v_fmac_f32 v245, v197, v197
v_mfma_f32_16x16x32_bf16 a[248:251], v[144:147], v[128:131], a[248:251]
s_add_u32 s81, s7, 3
s_and_b32 s81, s81, 7
s_lshl_b32 s81, s81, 7
v_mfma_f32_16x16x32_bf16 a[248:251], v[148:151], v[132:135], a[248:251]
s_add_u32 s81, s81, s78
s_add_u32 s81, s81, 1048576
v_add_f32 v244, v244, v198
v_mfma_f32_16x16x32_bf16 a[252:255], v[144:147], v[136:139], a[252:255]
s_waitcnt vmcnt(8) lgkmcnt(0)
s_barrier
ds_read_b128 v[144:147], v12 offset:32768
v_fmac_f32 v245, v198, v198
s_nop 3
v_mfma_f32_16x16x32_bf16 a[252:255], v[148:151], v[140:143], a[252:255]
s_add_u32 m0, s80, 32768
ds_read_b128 v[148:151], v13 offset:32768
v_lshlrev_b32 v200, 16, v252
v_mfma_f32_16x16x32_bf16 a[0:3], v[16:19], v[152:155], a[0:3]
v_add_f32 v244, v244, v199
v_fmac_f32 v245, v199, v199
v_mfma_f32_16x16x32_bf16 a[0:3], v[20:23], v[156:159], a[0:3]
buffer_load_dwordx4 v4, s[12:15], s81 offen offset:0 lds
v_mfma_f32_16x16x32_bf16 a[4:7], v[24:27], v[152:155], a[4:7]
ds_read_b64_tr_b16 v[80:81], v0 offset:0
v_and_b32 v201, 0xffff0000, v252
v_mfma_f32_16x16x32_bf16 a[4:7], v[28:31], v[156:159], a[4:7]
v_add_f32 v244, v244, v200
v_fmac_f32 v245, v200, v200
v_mfma_f32_16x16x32_bf16 a[8:11], v[32:35], v[152:155], a[8:11]
v_lshlrev_b32 v202, 16, v253
v_add_f32 v244, v244, v201
v_mfma_f32_16x16x32_bf16 a[8:11], v[36:39], v[156:159], a[8:11]
ds_read_b64_tr_b16 v[82:83], v0 offset:256
v_fmac_f32 v245, v201, v201
v_mfma_f32_16x16x32_bf16 a[12:15], v[40:43], v[152:155], a[12:15]
buffer_load_dwordx4 v5, s[12:15], s81 offen offset:1024 lds
v_mfma_f32_16x16x32_bf16 a[12:15], v[44:47], v[156:159], a[12:15]
v_and_b32 v203, 0xffff0000, v253
v_add_f32 v244, v244, v202
v_mfma_f32_16x16x32_bf16 a[16:19], v[152:155], v[48:51], a[16:19]
ds_read_b64_tr_b16 v[84:85], v0 offset:4096
v_fmac_f32 v245, v202, v202
v_mfma_f32_16x16x32_bf16 a[16:19], v[156:159], v[52:55], a[16:19]
v_lshlrev_b32 v204, 16, v254
v_add_f32 v244, v244, v203
v_mfma_f32_16x16x32_bf16 a[20:23], v[152:155], v[56:59], a[20:23]
v_and_b32 v205, 0xffff0000, v254
v_fmac_f32 v245, v203, v203
v_mfma_f32_16x16x32_bf16 a[20:23], v[156:159], v[60:63], a[20:23]
)ASM"
R"ASM(
buffer_load_dwordx4 v6, s[12:15], s81 offen offset:2048 lds
v_mfma_f32_16x16x32_bf16 a[24:27], v[152:155], v[64:67], a[24:27]
ds_read_b64_tr_b16 v[86:87], v0 offset:4352
v_add_f32 v244, v244, v204
v_mfma_f32_16x16x32_bf16 a[24:27], v[156:159], v[68:71], a[24:27]
v_lshlrev_b32 v206, 16, v255
v_fmac_f32 v245, v204, v204
v_mfma_f32_16x16x32_bf16 a[28:31], v[152:155], v[72:75], a[28:31]
ds_read_b64_tr_b16 v[88:89], v3 offset:0
v_add_f32 v244, v244, v205
v_mfma_f32_16x16x32_bf16 a[28:31], v[156:159], v[76:79], a[28:31]
ds_read_b128 v[152:155], v12 offset:34816
v_fmac_f32 v245, v205, v205
v_mfma_f32_16x16x32_bf16 a[32:35], v[16:19], v[160:163], a[32:35]
buffer_load_dwordx4 v7, s[12:15], s81 offen offset:3072 lds
v_mfma_f32_16x16x32_bf16 a[32:35], v[20:23], v[164:167], a[32:35]
s_add_u32 s81, s7, 4
s_and_b32 s81, s81, 7
ds_read_b128 v[156:159], v13 offset:34816
v_mfma_f32_16x16x32_bf16 a[36:39], v[24:27], v[160:163], a[36:39]
s_lshl_b32 s81, s81, 19
s_add_u32 s81, s81, s79
ds_read_b64_tr_b16 v[90:91], v3 offset:256
v_mfma_f32_16x16x32_bf16 a[36:39], v[28:31], v[164:167], a[36:39]
s_add_u32 m0, s80, 49152
v_and_b32 v207, 0xffff0000, v255
v_add_f32 v244, v244, v206
v_mfma_f32_16x16x32_bf16 a[40:43], v[32:35], v[160:163], a[40:43]
ds_read_b64_tr_b16 v[92:93], v3 offset:4096
v_fmac_f32 v245, v206, v206
v_mfma_f32_16x16x32_bf16 a[40:43], v[36:39], v[164:167], a[40:43]
buffer_load_dwordx4 v8, s[20:23], s81 offen offset:0 lds
v_mfma_f32_16x16x32_bf16 a[44:47], v[40:43], v[160:163], a[44:47]
v_add_f32 v244, v244, v207
v_fmac_f32 v245, v207, v207
v_mfma_f32_16x16x32_bf16 a[44:47], v[44:47], v[164:167], a[44:47]
ds_read_b64_tr_b16 v[94:95], v3 offset:4352
v_add_f32_dpp v244, v244, v244 row_ror:8 row_mask:0xf bank_mask:0xf
v_mfma_f32_16x16x32_bf16 a[48:51], v[160:163], v[48:51], a[48:51]
v_add_f32_dpp v245, v245, v245 row_ror:8 row_mask:0xf bank_mask:0xf
v_mov_b32 v246, v244
v_mfma_f32_16x16x32_bf16 a[48:51], v[164:167], v[52:55], a[48:51]
v_mov_b32 v247, v245
v_lshrrev_b32 v254, 3, v2
v_mfma_f32_16x16x32_bf16 a[52:55], v[160:163], v[56:59], a[52:55]
buffer_load_dwordx4 v9, s[20:23], s81 offen offset:1024 lds
v_mfma_f32_16x16x32_bf16 a[52:55], v[164:167], v[60:63], a[52:55]
ds_read_b64_tr_b16 v[96:97], v0 offset:512
v_lshl_add_u32 v254, v254, 2, s60
v_mfma_f32_16x16x32_bf16 a[56:59], v[160:163], v[64:67], a[56:59]
v_permlane16_swap_b32 v244, v246
v_mfma_f32_16x16x32_bf16 a[56:59], v[164:167], v[68:71], a[56:59]
ds_read_b64_tr_b16 v[98:99], v0 offset:768
v_min_u32 v254, 9, v254
v_mfma_f32_16x16x32_bf16 a[60:63], v[160:163], v[72:75], a[60:63]
ds_read_b128 v[160:163], v12 offset:36864
v_bfe_u32 v255, v2, 2, 1
v_mfma_f32_16x16x32_bf16 a[60:63], v[164:167], v[76:79], a[60:63]
buffer_load_dwordx4 v10, s[20:23], s81 offen offset:2048 lds
v_mfma_f32_16x16x32_bf16 a[64:67], v[16:19], v[168:171], a[64:67]
ds_read_b64_tr_b16 v[100:101], v0 offset:4608
v_add_f32 v244, v244, v246
v_mfma_f32_16x16x32_bf16 a[64:67], v[20:23], v[172:175], a[64:67]
ds_read_b128 v[164:167], v13 offset:36864
v_mad_u32_u24 v254, v255, 10, v254
v_mfma_f32_16x16x32_bf16 a[68:71], v[24:27], v[168:171], a[68:71]
v_permlane16_swap_b32 v245, v247
v_mfma_f32_16x16x32_bf16 a[68:71], v[28:31], v[172:175], a[68:71]
ds_read_b64_tr_b16 v[102:103], v0 offset:4864
v_add_u32 v0, 0xfffe0000, v0
v_mfma_f32_16x16x32_bf16 a[72:75], v[32:35], v[168:171], a[72:75]
buffer_load_dwordx4 v11, s[20:23], s81 offen offset:3072 lds
v_mfma_f32_16x16x32_bf16 a[72:75], v[36:39], v[172:175], a[72:75]
s_add_u32 s81, s7, 4
v_add_f32 v245, v245, v247
v_mov_b32 v246, v244
v_mfma_f32_16x16x32_bf16 a[76:79], v[40:43], v[168:171], a[76:79]
s_and_b32 s81, s81, 7
ds_read_b64_tr_b16 v[104:105], v3 offset:512
v_mov_b32 v247, v245
v_mfma_f32_16x16x32_bf16 a[76:79], v[44:47], v[172:175], a[76:79]
s_add_u32 m0, s80, 65536
v_and_b32 v248, 7, v2
v_mov_b32 v255, s70
v_mfma_f32_16x16x32_bf16 a[80:83], v[168:171], v[48:51], a[80:83]
s_lshl_b32 s81, s81, 7
v_permlane32_swap_b32 v244, v246
v_mfma_f32_16x16x32_bf16 a[80:83], v[172:175], v[52:55], a[80:83]
s_add_u32 s81, s81, s78
ds_read_b64_tr_b16 v[106:107], v3 offset:768
v_lshlrev_b32 v248, 5, v248
v_mfma_f32_16x16x32_bf16 a[84:87], v[168:171], v[56:59], a[84:87]
buffer_load_dwordx4 v4, s[12:15], s81 offen offset:0 lds
v_mfma_f32_16x16x32_bf16 a[84:87], v[172:175], v[60:63], a[84:87]
s_lshl_b32 s70, s72, 7
v_permlane32_swap_b32 v245, v247
v_mfma_f32_16x16x32_bf16 a[88:91], v[168:171], v[64:67], a[88:91]
s_add_u32 s70, s70, 1051136
ds_read_b64_tr_b16 v[108:109], v3 offset:4608
v_add_f32 v244, v244, v246
v_mfma_f32_16x16x32_bf16 a[88:91], v[172:175], v[68:71], a[88:91]
v_cmp_gt_u32 vcc, 24, v2
s_nop 1
v_cndmask_b32 v254, v255, v254, vcc
v_mfma_f32_16x16x32_bf16 a[92:95], v[168:171], v[72:75], a[92:95]
buffer_load_dwordx4 v5, s[12:15], s81 offen offset:1024 lds
v_mfma_f32_16x16x32_bf16 a[92:95], v[172:175], v[76:79], a[92:95]
L_pad_a_1:
s_cbranch_execz L_pad_a_1_1
ds_read_b64_tr_b16 v[110:111], v3 offset:4864
v_add_u32 v3, 0xfffe0000, v3
v_mfma_f32_16x16x32_bf16 a[96:99], v[16:19], v[176:179], a[96:99]
ds_read_b128 v[168:171], v12 offset:38912
v_add_u32 v12, 0x10000, v12
v_mfma_f32_16x16x32_bf16 a[96:99], v[20:23], v[180:183], a[96:99]
ds_read_b128 v[172:175], v13 offset:38912
v_add_u32 v13, 0x10000, v13
v_mfma_f32_16x16x32_bf16 a[100:103], v[24:27], v[176:179], a[100:103]
v_add_f32 v245, v245, v247
v_add_u32 v249, s101, v248
v_mfma_f32_16x16x32_bf16 a[100:103], v[28:31], v[180:183], a[100:103]
buffer_load_dwordx4 v6, s[12:15], s81 offen offset:2048 lds
v_mfma_f32_16x16x32_bf16 a[104:107], v[32:35], v[176:179], a[104:107]
s_lshl_b32 s101, s60, 8
v_and_b32 v255, 3, v2
v_lshl_add_u32 v254, v254, 2, v255
v_mfma_f32_16x16x32_bf16 a[104:107], v[36:39], v[180:183], a[104:107]
s_add_u32 s101, s101, 1114112
ds_write_b64 v249, v[244:245]
v_mfma_f32_16x16x32_bf16 a[108:111], v[40:43], v[176:179], a[108:111]
s_add_u32 s100, s101, 2560
v_lshlrev_b32 v254, 8, v254
v_add_u32 v254, 65536, v254
v_mfma_f32_16x16x32_bf16 a[108:111], v[44:47], v[180:183], a[108:111]
buffer_load_dwordx2 v[252:253], v254, s[52:55], 0 offen sc1
v_mfma_f32_16x16x32_bf16 a[112:115], v[176:179], v[48:51], a[112:115]
v_add_u32 v248, 99328, v248
v_and_b32 v249, 15, v2
v_mfma_f32_16x16x32_bf16 a[112:115], v[180:183], v[52:55], a[112:115]
s_cmp_lt_u32 s71, 40
s_cselect_b32 s69, s69, 1073741824
s_cselect_b32 s70, s70, 1073741824
v_mfma_f32_16x16x32_bf16 a[116:119], v[176:179], v[56:59], a[116:119]
s_sub_u32 s71, s72, 20
v_and_b32 v255, 7, v2
v_mfma_f32_16x16x32_bf16 a[116:119], v[180:183], v[60:63], a[116:119]
buffer_load_dwordx4 v7, s[12:15], s81 offen offset:3072 lds
v_mfma_f32_16x16x32_bf16 a[120:123], v[176:179], v[64:67], a[120:123]
s_add_u32 s81, s7, 4
s_and_b32 s81, s81, 7
s_lshl_b32 s81, s81, 19
v_mfma_f32_16x16x32_bf16 a[120:123], v[180:183], v[68:71], a[120:123]
s_add_u32 s81, s81, s79
s_cmp_lt_u32 s71, 10
s_cselect_b32 s71, 1.0, 0
v_mfma_f32_16x16x32_bf16 a[124:127], v[176:179], v[72:75], a[124:127]
s_waitcnt vmcnt(13) lgkmcnt(0)
s_barrier
ds_read_b128 v[176:179], v12 offset:0
v_mfma_f32_16x16x32_bf16 a[124:127], v[180:183], v[76:79], a[124:127]
s_add_u32 m0, s80, 81920
ds_read_b128 v[180:183], v13 offset:0
v_mfma_f32_16x16x32_bf16 a[128:131], v[16:19], v[144:147], a[128:131]
s_cmp_eq_u32 s62, 7
s_cselect_b32 s72, 1.0, 0
ds_read_b128 v[236:239], v248 offset:0
v_mfma_f32_16x16x32_bf16 a[128:131], v[20:23], v[148:151], a[128:131]
buffer_load_dwordx4 v8, s[24:27], s81 offen offset:0 lds
v_mfma_f32_16x16x32_bf16 a[132:135], v[24:27], v[144:147], a[132:135]
ds_read_b64_tr_b16 v[112:113], v0 offset:0
v_mfma_f32_16x16x32_bf16 a[132:135], v[28:31], v[148:151], a[132:135]
ds_read_b128 v[240:243], v248 offset:16
v_lshrrev_b32 v248, 4, v2
v_mfma_f32_16x16x32_bf16 a[136:139], v[32:35], v[144:147], a[136:139]
v_mul_u32_u24 v248, 15360, v248
v_lshl_add_u32 v248, v249, 4, v248
v_mfma_f32_16x16x32_bf16 a[136:139], v[36:39], v[148:151], a[136:139]
ds_read_b64_tr_b16 v[114:115], v0 offset:256
v_and_b32 v249, 3, v2
v_mfma_f32_16x16x32_bf16 a[140:143], v[40:43], v[144:147], a[140:143]
buffer_load_dwordx4 v9, s[24:27], s81 offen offset:1024 lds
v_mfma_f32_16x16x32_bf16 a[140:143], v[44:47], v[148:151], a[140:143]
s_waitcnt lgkmcnt(2)
v_add_f32 v236, v236, v238
v_add_f32 v237, v237, v239
v_mfma_f32_16x16x32_bf16 a[144:147], v[144:147], v[48:51], a[144:147]
ds_read_b64_tr_b16 v[116:117], v0 offset:4096
v_mfma_f32_16x16x32_bf16 a[144:147], v[148:151], v[52:55], a[144:147]
v_mfma_f32_16x16x32_bf16 a[148:151], v[144:147], v[56:59], a[148:151]
s_waitcnt lgkmcnt(1)
v_add_f32 v240, v240, v242
v_add_f32 v241, v241, v243
v_mfma_f32_16x16x32_bf16 a[148:151], v[148:151], v[60:63], a[148:151]
buffer_load_dwordx4 v10, s[24:27], s81 offen offset:2048 lds
v_mfma_f32_16x16x32_bf16 a[152:155], v[144:147], v[64:67], a[152:155]
ds_read_b64_tr_b16 v[118:119], v0 offset:4352
v_add_f32 v236, v236, v240
v_mfma_f32_16x16x32_bf16 a[152:155], v[148:151], v[68:71], a[152:155]
v_add_f32 v237, v237, v241
v_mul_f32 v236, 0.0015625, v236
v_mfma_f32_16x16x32_bf16 a[156:159], v[144:147], v[72:75], a[156:159]
ds_read_b64_tr_b16 v[120:121], v3 offset:0
v_mul_f32 v237, 0.0015625, v237
v_mfma_f32_16x16x32_bf16 a[156:159], v[148:151], v[76:79], a[156:159]
ds_read_b128 v[144:147], v12 offset:2048
v_fma_f32 v237, -v236, v236, v237
v_mfma_f32_16x16x32_bf16 a[160:163], v[16:19], v[152:155], a[160:163]
buffer_load_dwordx4 v11, s[24:27], s81 offen offset:3072 lds
v_mfma_f32_16x16x32_bf16 a[160:163], v[20:23], v[156:159], a[160:163]
s_add_u32 s81, s7, 4
s_and_b32 s81, s81, 7
ds_read_b128 v[148:151], v13 offset:2048
v_mfma_f32_16x16x32_bf16 a[164:167], v[24:27], v[152:155], a[164:167]
s_lshl_b32 s81, s81, 7
s_add_u32 s81, s81, s78
ds_read_b64_tr_b16 v[122:123], v3 offset:256
v_mfma_f32_16x16x32_bf16 a[164:167], v[28:31], v[156:159], a[164:167]
s_add_u32 m0, s80, 16384
s_add_u32 s81, s81, 1048576
v_max_f32 v237, 0, v237
v_mfma_f32_16x16x32_bf16 a[168:171], v[32:35], v[152:155], a[168:171]
ds_read_b64_tr_b16 v[124:125], v3 offset:4096
v_add_f32 v237, 1e-05, v237
v_mfma_f32_16x16x32_bf16 a[168:171], v[36:39], v[156:159], a[168:171]
buffer_load_dwordx4 v4, s[12:15], s81 offen offset:0 lds
v_mfma_f32_16x16x32_bf16 a[172:175], v[40:43], v[152:155], a[172:175]
v_rsq_f32 v237, v237
v_mfma_f32_16x16x32_bf16 a[172:175], v[44:47], v[156:159], a[172:175]
ds_read_b64_tr_b16 v[126:127], v3 offset:4352
v_mul_f32 v237, 0.12751743082459868, v237
v_mfma_f32_16x16x32_bf16 a[176:179], v[152:155], v[48:51], a[176:179]
v_sub_f32 v184, v184, v236
v_sub_f32 v185, v185, v236
v_mfma_f32_16x16x32_bf16 a[176:179], v[156:159], v[52:55], a[176:179]
v_sub_f32 v186, v186, v236
v_sub_f32 v187, v187, v236
v_mfma_f32_16x16x32_bf16 a[180:183], v[152:155], v[56:59], a[180:183]
buffer_load_dwordx4 v5, s[12:15], s81 offen offset:1024 lds
v_mfma_f32_16x16x32_bf16 a[180:183], v[156:159], v[60:63], a[180:183]
ds_read_b64_tr_b16 v[128:129], v0 offset:512
v_sub_f32 v188, v188, v236
v_mfma_f32_16x16x32_bf16 a[184:187], v[152:155], v[64:67], a[184:187]
v_sub_f32 v189, v189, v236
v_sub_f32 v190, v190, v236
v_mfma_f32_16x16x32_bf16 a[184:187], v[156:159], v[68:71], a[184:187]
ds_read_b64_tr_b16 v[130:131], v0 offset:768
v_sub_f32 v191, v191, v236
v_mfma_f32_16x16x32_bf16 a[188:191], v[152:155], v[72:75], a[188:191]
ds_read_b128 v[152:155], v12 offset:4096
v_sub_f32 v192, v192, v236
v_mfma_f32_16x16x32_bf16 a[188:191], v[156:159], v[76:79], a[188:191]
buffer_load_dwordx4 v6, s[12:15], s81 offen offset:2048 lds
v_mfma_f32_16x16x32_bf16 a[192:195], v[16:19], v[160:163], a[192:195]
ds_read_b64_tr_b16 v[132:133], v0 offset:4608
v_sub_f32 v193, v193, v236
v_mfma_f32_16x16x32_bf16 a[192:195], v[20:23], v[164:167], a[192:195]
ds_read_b128 v[156:159], v13 offset:4096
v_sub_f32 v194, v194, v236
v_mfma_f32_16x16x32_bf16 a[196:199], v[24:27], v[160:163], a[196:199]
v_sub_f32 v195, v195, v236
v_sub_f32 v196, v196, v236
v_mfma_f32_16x16x32_bf16 a[196:199], v[28:31], v[164:167], a[196:199]
ds_read_b64_tr_b16 v[134:135], v0 offset:4864
v_sub_f32 v197, v197, v236
v_mfma_f32_16x16x32_bf16 a[200:203], v[32:35], v[160:163], a[200:203]
buffer_load_dwordx4 v7, s[12:15], s81 offen offset:3072 lds
v_mfma_f32_16x16x32_bf16 a[200:203], v[36:39], v[164:167], a[200:203]
s_add_u32 s81, s7, 5
s_and_b32 s81, s81, 7
s_lshl_b32 s81, s81, 19
v_mfma_f32_16x16x32_bf16 a[204:207], v[40:43], v[160:163], a[204:207]
s_add_u32 s81, s81, s79
ds_read_b64_tr_b16 v[136:137], v3 offset:512
v_sub_f32 v198, v198, v236
v_mfma_f32_16x16x32_bf16 a[204:207], v[44:47], v[164:167], a[204:207]
s_add_u32 m0, s80, 114688
v_sub_f32 v199, v199, v236
v_sub_f32 v200, v200, v236
v_mfma_f32_16x16x32_bf16 a[208:211], v[160:163], v[48:51], a[208:211]
v_sub_f32 v201, v201, v236
v_sub_f32 v202, v202, v236
v_mfma_f32_16x16x32_bf16 a[208:211], v[164:167], v[52:55], a[208:211]
buffer_load_dwordx4 v8, s[20:23], s81 offen offset:0 lds
v_mfma_f32_16x16x32_bf16 a[212:215], v[160:163], v[56:59], a[212:215]
ds_read_b64_tr_b16 v[138:139], v3 offset:768
v_sub_f32 v203, v203, v236
v_mfma_f32_16x16x32_bf16 a[212:215], v[164:167], v[60:63], a[212:215]
v_sub_f32 v204, v204, v236
v_sub_f32 v205, v205, v236
v_mfma_f32_16x16x32_bf16 a[216:219], v[160:163], v[64:67], a[216:219]
ds_read_b64_tr_b16 v[140:141], v3 offset:4608
v_sub_f32 v206, v206, v236
v_mfma_f32_16x16x32_bf16 a[216:219], v[164:167], v[68:71], a[216:219]
v_mul_f32 v184, v184, v237
v_sub_f32 v207, v207, v236
v_mfma_f32_16x16x32_bf16 a[220:223], v[160:163], v[72:75], a[220:223]
buffer_load_dwordx4 v9, s[20:23], s81 offen offset:1024 lds
v_mfma_f32_16x16x32_bf16 a[220:223], v[164:167], v[76:79], a[220:223]
ds_read_b64_tr_b16 v[142:143], v3 offset:4864
v_mul_f32 v185, v185, v237
v_mfma_f32_16x16x32_bf16 a[224:227], v[16:19], v[168:171], a[224:227]
ds_read_b128 v[160:163], v12 offset:6144
v_add_u32 v12, 0xfffe4000, v12
v_mfma_f32_16x16x32_bf16 a[224:227], v[20:23], v[172:175], a[224:227]
ds_read_b128 v[164:167], v13 offset:6144
v_add_u32 v13, 0xfffe4000, v13
v_mfma_f32_16x16x32_bf16 a[228:231], v[24:27], v[168:171], a[228:231]
s_waitcnt vmcnt(11)
v_cmp_ne_u64 vcc, s[64:65], v[252:253]
s_nop 0
s_cbranch_vccnz L_stub_mod
L_ret_mod:
v_mfma_f32_16x16x32_bf16 a[228:231], v[28:31], v[172:175], a[228:231]
buffer_load_dwordx4 v10, s[20:23], s81 offen offset:2048 lds
v_mfma_f32_16x16x32_bf16 a[232:235], v[32:35], v[168:171], a[232:235]
v_lshrrev_b32 v252, 4, v2
v_mul_u32_u24 v252, 15360, v252
v_mfma_f32_16x16x32_bf16 a[232:235], v[36:39], v[172:175], a[232:235]
v_and_b32 v253, 15, v2
v_lshl_add_u32 v252, v253, 4, v252
v_mfma_f32_16x16x32_bf16 a[236:239], v[40:43], v[168:171], a[236:239]
v_lshrrev_b32 v254, 4, v2
v_lshl_add_u32 v253, v253, 2, v254
v_mfma_f32_16x16x32_bf16 a[236:239], v[44:47], v[172:175], a[236:239]
buffer_load_dwordx4 v[208:211], v252, s[52:55], s101 offen offset:0 sc1
v_mfma_f32_16x16x32_bf16 a[240:243], v[168:171], v[48:51], a[240:243]
v_mul_f32 v186, v186, v237
v_lshlrev_b32 v253, 2, v253
v_mfma_f32_16x16x32_bf16 a[240:243], v[172:175], v[52:55], a[240:243]
v_mul_f32 v187, v187, v237
v_mul_f32 v188, v188, v237
v_mfma_f32_16x16x32_bf16 a[244:247], v[168:171], v[56:59], a[244:247]
v_mul_f32 v189, v189, v237
v_mul_f32 v190, v190, v237
v_mfma_f32_16x16x32_bf16 a[244:247], v[172:175], v[60:63], a[244:247]
buffer_load_dwordx4 v11, s[20:23], s81 offen offset:3072 lds
v_mfma_f32_16x16x32_bf16 a[248:251], v[168:171], v[64:67], a[248:251]
s_add_u32 s81, s7, 5
s_and_b32 s81, s81, 7
s_lshl_b32 s81, s81, 7
v_mfma_f32_16x16x32_bf16 a[248:251], v[172:175], v[68:71], a[248:251]
s_add_u32 s81, s81, s78
v_mul_f32 v191, v191, v237
v_mul_f32 v192, v192, v237
v_mfma_f32_16x16x32_bf16 a[252:255], v[168:171], v[72:75], a[252:255]
s_waitcnt lgkmcnt(0)
s_barrier
ds_read_b128 v[168:171], v12 offset:0
v_mul_f32 v193, v193, v237
v_mfma_f32_16x16x32_bf16 a[252:255], v[172:175], v[76:79], a[252:255]
buffer_load_dwordx4 v[212:215], v252, s[52:55], s100 offen offset:0 sc1
v_mfma_f32_16x16x32_bf16 a[0:3], v[80:83], v[176:179], a[0:3]
s_add_u32 m0, s80, 131072
ds_read_b128 v[172:175], v13 offset:0
v_mul_f32 v194, v194, v237
v_mfma_f32_16x16x32_bf16 a[0:3], v[84:87], v[180:183], a[0:3]
v_mul_f32 v195, v195, v237
v_mul_f32 v196, v196, v237
v_mfma_f32_16x16x32_bf16 a[4:7], v[88:91], v[176:179], a[4:7]
ds_read_b64_tr_b16 v[16:17], v0 offset:49152
v_mul_f32 v197, v197, v237
v_mfma_f32_16x16x32_bf16 a[4:7], v[92:95], v[180:183], a[4:7]
buffer_load_dwordx4 v[216:219], v252, s[52:55], s101 offen offset:1024 sc1
v_mfma_f32_16x16x32_bf16 a[8:11], v[96:99], v[176:179], a[8:11]
v_mul_f32 v198, v198, v237
v_mul_f32 v199, v199, v237
v_mfma_f32_16x16x32_bf16 a[8:11], v[100:103], v[180:183], a[8:11]
ds_read_b64_tr_b16 v[18:19], v0 offset:49408
v_mul_f32 v200, v200, v237
v_mfma_f32_16x16x32_bf16 a[12:15], v[104:107], v[176:179], a[12:15]
v_mul_f32 v201, v201, v237
v_mul_f32 v202, v202, v237
v_mfma_f32_16x16x32_bf16 a[12:15], v[108:111], v[180:183], a[12:15]
buffer_load_dwordx4 v4, s[12:15], s81 offen offset:0 lds
v_mfma_f32_16x16x32_bf16 a[16:19], v[176:179], v[112:115], a[16:19]
ds_read_b64_tr_b16 v[20:21], v0 offset:53248
v_mul_f32 v203, v203, v237
v_mfma_f32_16x16x32_bf16 a[16:19], v[180:183], v[116:119], a[16:19]
v_mul_f32 v204, v204, v237
v_mul_f32 v205, v205, v237
v_mfma_f32_16x16x32_bf16 a[20:23], v[176:179], v[120:123], a[20:23]
v_mul_f32 v206, v206, v237
v_mul_f32 v207, v207, v237
v_mfma_f32_16x16x32_bf16 a[20:23], v[180:183], v[124:127], a[20:23]
buffer_load_dwordx4 v[220:223], v252, s[52:55], s100 offen offset:1024 sc1
v_mfma_f32_16x16x32_bf16 a[24:27], v[176:179], v[128:131], a[24:27]
ds_read_b64_tr_b16 v[22:23], v0 offset:53504
v_lshrrev_b32 v242, 4, v2
v_mfma_f32_16x16x32_bf16 a[24:27], v[180:183], v[132:135], a[24:27]
v_mov_b32 v240, s64
v_lshlrev_b32 v242, 28, v242
v_mfma_f32_16x16x32_bf16 a[28:31], v[176:179], v[136:139], a[28:31]
ds_read_b64_tr_b16 v[24:25], v3 offset:49152
v_mov_b32 v241, s65
v_mfma_f32_16x16x32_bf16 a[28:31], v[180:183], v[140:143], a[28:31]
buffer_load_dwordx4 v[224:227], v252, s[52:55], s101 offen offset:2048 sc1
v_mfma_f32_16x16x32_bf16 a[32:35], v[80:83], v[144:147], a[32:35]
s_mov_b32 s101, 327680
ds_read_b128 v[176:179], v12 offset:2048
v_lshl_add_u32 v242, v2, 3, v242
)ASM"
R"ASM(
v_mfma_f32_16x16x32_bf16 a[32:35], v[84:87], v[148:151], a[32:35]
ds_read_b128 v[180:183], v13 offset:2048
v_mfma_f32_16x16x32_bf16 a[36:39], v[88:91], v[144:147], a[36:39]
ds_read_b64_tr_b16 v[26:27], v3 offset:49408
v_mfma_f32_16x16x32_bf16 a[36:39], v[92:95], v[148:151], a[36:39]
buffer_load_dwordx4 v5, s[12:15], s81 offen offset:1024 lds
v_mfma_f32_16x16x32_bf16 a[40:43], v[96:99], v[144:147], a[40:43]
ds_read_b64_tr_b16 v[28:29], v3 offset:53248
v_mfma_f32_16x16x32_bf16 a[40:43], v[100:103], v[148:151], a[40:43]
v_mfma_f32_16x16x32_bf16 a[44:47], v[104:107], v[144:147], a[44:47]
v_mfma_f32_16x16x32_bf16 a[44:47], v[108:111], v[148:151], a[44:47]
buffer_load_dwordx4 v[228:231], v252, s[52:55], s100 offen offset:2048 sc1
v_mfma_f32_16x16x32_bf16 a[48:51], v[144:147], v[112:115], a[48:51]
s_mul_i32 s100, s60, 1536
s_add_u32 s100, s100, 100352
ds_read_b64_tr_b16 v[30:31], v3 offset:53504
v_mfma_f32_16x16x32_bf16 a[48:51], v[148:151], v[116:119], a[48:51]
v_add_u32 v253, s100, v253
v_lshrrev_b32 v252, 3, v2
v_mfma_f32_16x16x32_bf16 a[52:55], v[144:147], v[120:123], a[52:55]
ds_read_b64_tr_b16 v[32:33], v0 offset:49664
v_lshlrev_b32 v254, 5, v252
v_mfma_f32_16x16x32_bf16 a[52:55], v[148:151], v[124:127], a[52:55]
buffer_load_dwordx4 v6, s[12:15], s81 offen offset:2048 lds
v_mfma_f32_16x16x32_bf16 a[56:59], v[144:147], v[128:131], a[56:59]
v_add_u32 v254, s100, v254
v_lshlrev_b32 v252, 8, v252
v_mfma_f32_16x16x32_bf16 a[56:59], v[148:151], v[132:135], a[56:59]
ds_read_b64_tr_b16 v[34:35], v0 offset:49920
v_lshl_add_u32 v252, v255, 4, v252
v_mfma_f32_16x16x32_bf16 a[60:63], v[144:147], v[136:139], a[60:63]
ds_read_b128 v[144:147], v12 offset:4096
v_mfma_f32_16x16x32_bf16 a[60:63], v[148:151], v[140:143], a[60:63]
buffer_load_dwordx4 v7, s[12:15], s81 offen offset:3072 lds
v_mfma_f32_16x16x32_bf16 a[64:67], v[80:83], v[152:155], a[64:67]
s_add_u32 s81, s7, 5
s_and_b32 s81, s81, 7
ds_read_b64_tr_b16 v[36:37], v0 offset:53760
v_mfma_f32_16x16x32_bf16 a[64:67], v[84:87], v[156:159], a[64:67]
s_add_u32 m0, s80, 147456
s_lshl_b32 s81, s81, 19
ds_read_b128 v[148:151], v13 offset:4096
v_mfma_f32_16x16x32_bf16 a[68:71], v[88:91], v[152:155], a[68:71]
s_add_u32 s81, s81, s79
v_mfma_f32_16x16x32_bf16 a[68:71], v[92:95], v[156:159], a[68:71]
buffer_load_dwordx4 v8, s[24:27], s81 offen offset:0 lds
v_mfma_f32_16x16x32_bf16 a[72:75], v[96:99], v[152:155], a[72:75]
ds_read_b64_tr_b16 v[38:39], v0 offset:54016
v_add_u32 v0, 0x14000, v0
v_mfma_f32_16x16x32_bf16 a[72:75], v[100:103], v[156:159], a[72:75]
v_mfma_f32_16x16x32_bf16 a[76:79], v[104:107], v[152:155], a[76:79]
ds_read_b64_tr_b16 v[40:41], v3 offset:49664
v_mfma_f32_16x16x32_bf16 a[76:79], v[108:111], v[156:159], a[76:79]
buffer_load_dwordx4 v9, s[24:27], s81 offen offset:1024 lds
v_mfma_f32_16x16x32_bf16 a[80:83], v[152:155], v[112:115], a[80:83]
v_mfma_f32_16x16x32_bf16 a[80:83], v[156:159], v[116:119], a[80:83]
ds_read_b64_tr_b16 v[42:43], v3 offset:49920
v_mfma_f32_16x16x32_bf16 a[84:87], v[152:155], v[120:123], a[84:87]
v_mfma_f32_16x16x32_bf16 a[84:87], v[156:159], v[124:127], a[84:87]
buffer_load_dwordx4 v10, s[24:27], s81 offen offset:2048 lds
v_mfma_f32_16x16x32_bf16 a[88:91], v[152:155], v[128:131], a[88:91]
ds_read_b64_tr_b16 v[44:45], v3 offset:53760
v_mfma_f32_16x16x32_bf16 a[88:91], v[156:159], v[132:135], a[88:91]
v_mfma_f32_16x16x32_bf16 a[92:95], v[152:155], v[136:139], a[92:95]
ds_read_b128 v[152:155], v12 offset:6144
v_mfma_f32_16x16x32_bf16 a[92:95], v[156:159], v[140:143], a[92:95]
buffer_load_dwordx4 v[244:247], v248, s[52:55], s69 offen sc1
v_mfma_f32_16x16x32_bf16 a[96:99], v[80:83], v[160:163], a[96:99]
s_lshl_b32 s69, s62, 4
ds_read_b64_tr_b16 v[46:47], v3 offset:54016
v_add_u32 v3, 0x14000, v3
v_mfma_f32_16x16x32_bf16 a[96:99], v[84:87], v[164:167], a[96:99]
ds_read_b128 v[156:159], v13 offset:6144
v_lshrrev_b32 v248, 4, v2
v_mfma_f32_16x16x32_bf16 a[100:103], v[88:91], v[160:163], a[100:103]
v_lshlrev_b32 v248, 1, v248
v_lshl_add_u32 v248, v249, 4, v248
v_mfma_f32_16x16x32_bf16 a[100:103], v[92:95], v[164:167], a[100:103]
buffer_load_dwordx4 v11, s[24:27], s81 offen offset:3072 lds
v_mfma_f32_16x16x32_bf16 a[104:107], v[96:99], v[160:163], a[104:107]
v_and_b32 v249, 4, v2
v_lshl_add_u32 v248, v249, 1, v248
v_mfma_f32_16x16x32_bf16 a[104:107], v[100:103], v[164:167], a[104:107]
v_and_b32 v249, 8, v2
v_lshl_add_u32 v248, v249, 3, v248
v_mfma_f32_16x16x32_bf16 a[108:111], v[104:107], v[160:163], a[108:111]
v_mfma_f32_16x16x32_bf16 a[108:111], v[108:111], v[164:167], a[108:111]
v_mfma_f32_16x16x32_bf16 a[112:115], v[160:163], v[112:115], a[112:115]
s_waitcnt vmcnt(15)
v_permlane32_swap_b32 v208, v210
v_mfma_f32_16x16x32_bf16 a[112:115], v[164:167], v[116:119], a[112:115]
v_permlane32_swap_b32 v209, v211
v_mfma_f32_16x16x32_bf16 a[116:119], v[160:163], v[120:123], a[116:119]
v_add_f32 v208, v208, v210
v_add_f32 v209, v209, v211
v_mfma_f32_16x16x32_bf16 a[116:119], v[164:167], v[124:127], a[116:119]
s_nop 0
v_permlane16_swap_b32 v208, v209
v_mfma_f32_16x16x32_bf16 a[120:123], v[160:163], v[128:131], a[120:123]
s_add_u32 s81, s7, 5
s_and_b32 s81, s81, 7
s_lshl_b32 s81, s81, 7
v_mfma_f32_16x16x32_bf16 a[120:123], v[164:167], v[132:135], a[120:123]
s_add_u32 s81, s81, s78
s_add_u32 s81, s81, 1048576
v_add_f32 v208, v208, v209
v_mfma_f32_16x16x32_bf16 a[124:127], v[160:163], v[136:139], a[124:127]
s_waitcnt lgkmcnt(0)
s_barrier
ds_read_b128 v[160:163], v12 offset:32768
v_cvt_pk_bf16_f32 v208, v208, v208
v_mfma_f32_16x16x32_bf16 a[124:127], v[164:167], v[140:143], a[124:127]
s_add_u32 m0, s80, 0
ds_read_b128 v[164:167], v13 offset:32768
v_and_b32 v208, 0xffff0000, v208
v_mfma_f32_16x16x32_bf16 a[128:131], v[80:83], v[168:171], a[128:131]
v_mul_f32 v208, 0.12751743082459868, v208
v_mfma_f32_16x16x32_bf16 a[128:131], v[84:87], v[172:175], a[128:131]
buffer_load_dwordx4 v4, s[12:15], s81 offen offset:0 lds
v_mfma_f32_16x16x32_bf16 a[132:135], v[88:91], v[168:171], a[132:135]
ds_read_b64_tr_b16 v[48:49], v0 offset:0
v_mfma_f32_16x16x32_bf16 a[132:135], v[92:95], v[172:175], a[132:135]
ds_write_b32 v253, v208 offset:0
v_mfma_f32_16x16x32_bf16 a[136:139], v[96:99], v[168:171], a[136:139]
s_waitcnt vmcnt(14)
v_permlane32_swap_b32 v212, v214
v_mfma_f32_16x16x32_bf16 a[136:139], v[100:103], v[172:175], a[136:139]
ds_read_b64_tr_b16 v[50:51], v0 offset:256
v_add_f32 v212, v212, v214
v_mfma_f32_16x16x32_bf16 a[140:143], v[104:107], v[168:171], a[140:143]
buffer_load_dwordx4 v5, s[12:15], s81 offen offset:1024 lds
v_mfma_f32_16x16x32_bf16 a[140:143], v[108:111], v[172:175], a[140:143]
v_permlane32_swap_b32 v213, v215
v_mfma_f32_16x16x32_bf16 a[144:147], v[168:171], v[112:115], a[144:147]
ds_read_b64_tr_b16 v[52:53], v0 offset:4096
v_add_f32 v213, v213, v215
v_mfma_f32_16x16x32_bf16 a[144:147], v[172:175], v[116:119], a[144:147]
s_nop 0
v_permlane16_swap_b32 v212, v213
v_mfma_f32_16x16x32_bf16 a[148:151], v[168:171], v[120:123], a[148:151]
v_add_f32 v212, v212, v213
v_cvt_pk_bf16_f32 v212, v212, v212
v_mfma_f32_16x16x32_bf16 a[148:151], v[172:175], v[124:127], a[148:151]
buffer_load_dwordx4 v6, s[12:15], s81 offen offset:2048 lds
v_mfma_f32_16x16x32_bf16 a[152:155], v[168:171], v[128:131], a[152:155]
ds_read_b64_tr_b16 v[54:55], v0 offset:4352
v_and_b32 v212, 0xffff0000, v212
v_mfma_f32_16x16x32_bf16 a[152:155], v[172:175], v[132:135], a[152:155]
v_add_f32 v212, 1.0, v212
v_cvt_pk_bf16_f32 v212, v212, v212
v_mfma_f32_16x16x32_bf16 a[156:159], v[168:171], v[136:139], a[156:159]
ds_read_b64_tr_b16 v[56:57], v3 offset:0
v_and_b32 v212, 0xffff0000, v212
v_mfma_f32_16x16x32_bf16 a[156:159], v[172:175], v[140:143], a[156:159]
ds_read_b128 v[168:171], v12 offset:34816
v_mfma_f32_16x16x32_bf16 a[160:163], v[80:83], v[176:179], a[160:163]
buffer_load_dwordx4 v7, s[12:15], s81 offen offset:3072 lds
v_mfma_f32_16x16x32_bf16 a[160:163], v[84:87], v[180:183], a[160:163]
s_add_u32 s81, s7, 6
s_and_b32 s81, s81, 7
ds_read_b128 v[172:175], v13 offset:34816
v_mfma_f32_16x16x32_bf16 a[164:167], v[88:91], v[176:179], a[164:167]
s_lshl_b32 s81, s81, 19
s_add_u32 s81, s81, s79
ds_read_b64_tr_b16 v[58:59], v3 offset:256
v_mfma_f32_16x16x32_bf16 a[164:167], v[92:95], v[180:183], a[164:167]
s_add_u32 m0, s80, 32768
ds_write_b32 v253, v212 offset:256
v_mfma_f32_16x16x32_bf16 a[168:171], v[96:99], v[176:179], a[168:171]
ds_read_b64_tr_b16 v[60:61], v3 offset:4096
v_mfma_f32_16x16x32_bf16 a[168:171], v[100:103], v[180:183], a[168:171]
buffer_load_dwordx4 v8, s[20:23], s81 offen offset:0 lds
v_mfma_f32_16x16x32_bf16 a[172:175], v[104:107], v[176:179], a[172:175]
s_waitcnt vmcnt(17)
v_permlane32_swap_b32 v216, v218
v_mfma_f32_16x16x32_bf16 a[172:175], v[108:111], v[180:183], a[172:175]
ds_read_b64_tr_b16 v[62:63], v3 offset:4352
v_add_f32 v216, v216, v218
v_mfma_f32_16x16x32_bf16 a[176:179], v[176:179], v[112:115], a[176:179]
v_permlane32_swap_b32 v217, v219
v_mfma_f32_16x16x32_bf16 a[176:179], v[180:183], v[116:119], a[176:179]
ds_read_b128 v[232:235], v254 offset:0
v_add_f32 v217, v217, v219
v_mfma_f32_16x16x32_bf16 a[180:183], v[176:179], v[120:123], a[180:183]
buffer_load_dwordx4 v9, s[20:23], s81 offen offset:1024 lds
v_mfma_f32_16x16x32_bf16 a[180:183], v[180:183], v[124:127], a[180:183]
ds_read_b64_tr_b16 v[64:65], v0 offset:512
v_mfma_f32_16x16x32_bf16 a[184:187], v[176:179], v[128:131], a[184:187]
v_permlane16_swap_b32 v216, v217
v_mfma_f32_16x16x32_bf16 a[184:187], v[180:183], v[132:135], a[184:187]
ds_read_b64_tr_b16 v[66:67], v0 offset:768
v_add_f32 v216, v216, v217
v_mfma_f32_16x16x32_bf16 a[188:191], v[176:179], v[136:139], a[188:191]
ds_read_b128 v[176:179], v12 offset:36864
v_cvt_pk_bf16_f32 v216, v216, v216
v_mfma_f32_16x16x32_bf16 a[188:191], v[180:183], v[140:143], a[188:191]
buffer_load_dwordx4 v10, s[20:23], s81 offen offset:2048 lds
v_mfma_f32_16x16x32_bf16 a[192:195], v[80:83], v[144:147], a[192:195]
ds_read_b64_tr_b16 v[68:69], v0 offset:4608
v_and_b32 v216, 0xffff0000, v216
v_mfma_f32_16x16x32_bf16 a[192:195], v[84:87], v[148:151], a[192:195]
ds_read_b128 v[180:183], v13 offset:36864
v_mul_f32 v216, 0.12751743082459868, v216
v_mfma_f32_16x16x32_bf16 a[196:199], v[88:91], v[144:147], a[196:199]
ds_read_b128 v[236:239], v254 offset:16
v_mfma_f32_16x16x32_bf16 a[196:199], v[92:95], v[148:151], a[196:199]
ds_read_b64_tr_b16 v[70:71], v0 offset:4864
v_mfma_f32_16x16x32_bf16 a[200:203], v[96:99], v[144:147], a[200:203]
buffer_load_dwordx4 v11, s[20:23], s81 offen offset:3072 lds
v_mfma_f32_16x16x32_bf16 a[200:203], v[100:103], v[148:151], a[200:203]
s_add_u32 s81, s7, 6
s_and_b32 s81, s81, 7
ds_read_b128 v[208:211], v254 offset:256
v_mfma_f32_16x16x32_bf16 a[204:207], v[104:107], v[144:147], a[204:207]
s_lshl_b32 s81, s81, 7
s_add_u32 s81, s81, s78
ds_read_b64_tr_b16 v[72:73], v3 offset:512
v_mfma_f32_16x16x32_bf16 a[204:207], v[108:111], v[148:151], a[204:207]
s_add_u32 m0, s80, 49152
ds_read_b128 v[212:215], v254 offset:272
v_mfma_f32_16x16x32_bf16 a[208:211], v[144:147], v[112:115], a[208:211]
ds_write_b32 v253, v216 offset:512
v_mfma_f32_16x16x32_bf16 a[208:211], v[148:151], v[116:119], a[208:211]
buffer_load_dwordx4 v4, s[12:15], s81 offen offset:0 lds
v_mfma_f32_16x16x32_bf16 a[212:215], v[144:147], v[120:123], a[212:215]
ds_read_b64_tr_b16 v[74:75], v3 offset:768
v_mfma_f32_16x16x32_bf16 a[212:215], v[148:151], v[124:127], a[212:215]
s_waitcnt vmcnt(19)
v_permlane32_swap_b32 v220, v222
v_mfma_f32_16x16x32_bf16 a[216:219], v[144:147], v[128:131], a[216:219]
ds_read_b64_tr_b16 v[76:77], v3 offset:4608
v_add_f32 v220, v220, v222
v_mfma_f32_16x16x32_bf16 a[216:219], v[148:151], v[132:135], a[216:219]
v_permlane32_swap_b32 v221, v223
v_mfma_f32_16x16x32_bf16 a[220:223], v[144:147], v[136:139], a[220:223]
buffer_load_dwordx4 v5, s[12:15], s81 offen offset:1024 lds
v_mfma_f32_16x16x32_bf16 a[220:223], v[148:151], v[140:143], a[220:223]
ds_read_b64_tr_b16 v[78:79], v3 offset:4864
v_add_f32 v221, v221, v223
v_mfma_f32_16x16x32_bf16 a[224:227], v[80:83], v[152:155], a[224:227]
ds_read_b128 v[144:147], v12 offset:38912
v_add_u32 v12, 0xffffc000, v12
v_mfma_f32_16x16x32_bf16 a[224:227], v[84:87], v[156:159], a[224:227]
ds_read_b128 v[148:151], v13 offset:38912
v_add_u32 v13, 0xffffc000, v13
v_mfma_f32_16x16x32_bf16 a[228:231], v[88:91], v[152:155], a[228:231]
v_permlane16_swap_b32 v220, v221
v_mfma_f32_16x16x32_bf16 a[228:231], v[92:95], v[156:159], a[228:231]
buffer_load_dwordx4 v6, s[12:15], s81 offen offset:2048 lds
v_mfma_f32_16x16x32_bf16 a[232:235], v[96:99], v[152:155], a[232:235]
v_add_f32 v220, v220, v221
v_cvt_pk_bf16_f32 v220, v220, v220
v_mfma_f32_16x16x32_bf16 a[232:235], v[100:103], v[156:159], a[232:235]
s_waitcnt lgkmcnt(1)
v_and_b32 v220, 0xffff0000, v220
v_fmac_f32 v232, v184, v208
v_mfma_f32_16x16x32_bf16 a[236:239], v[104:107], v[152:155], a[236:239]
v_fmac_f32 v233, v185, v209
v_fmac_f32 v234, v186, v210
v_mfma_f32_16x16x32_bf16 a[236:239], v[108:111], v[156:159], a[236:239]
v_fmac_f32 v235, v187, v211
v_fmac_f32 v236, v188, v212
v_mfma_f32_16x16x32_bf16 a[240:243], v[152:155], v[112:115], a[240:243]
buffer_load_dwordx4 v7, s[12:15], s81 offen offset:3072 lds
v_mfma_f32_16x16x32_bf16 a[240:243], v[156:159], v[116:119], a[240:243]
v_fmac_f32 v237, v189, v213
v_fmac_f32 v238, v190, v214
v_mfma_f32_16x16x32_bf16 a[244:247], v[152:155], v[120:123], a[244:247]
v_add_f32 v220, 1.0, v220
v_fmac_f32 v239, v191, v215
v_mfma_f32_16x16x32_bf16 a[244:247], v[156:159], v[124:127], a[244:247]
v_cvt_pk_bf16_f32 v184, v232, v233
v_cvt_pk_bf16_f32 v185, v234, v235
v_mfma_f32_16x16x32_bf16 a[248:251], v[152:155], v[128:131], a[248:251]
s_add_u32 s81, s7, 6
v_cvt_pk_bf16_f32 v186, v236, v237
v_cvt_pk_bf16_f32 v187, v238, v239
v_mfma_f32_16x16x32_bf16 a[248:251], v[156:159], v[132:135], a[248:251]
buffer_store_dwordx4 v[184:187], v252, s[52:55], s66 offen sc1
v_mfma_f32_16x16x32_bf16 a[252:255], v[152:155], v[136:139], a[252:255]
s_waitcnt lgkmcnt(0)
s_barrier
ds_read_b128 v[152:155], v12 offset:0
v_cvt_pk_bf16_f32 v220, v220, v220
v_mfma_f32_16x16x32_bf16 a[252:255], v[156:159], v[140:143], a[252:255]
s_and_b32 s81, s81, 7
ds_read_b128 v[156:159], v13 offset:0
v_and_b32 v220, 0xffff0000, v220
v_mfma_f32_16x16x32_bf16 a[0:3], v[16:19], v[160:163], a[0:3]
s_add_u32 m0, s80, 65536
ds_write_b32 v253, v220 offset:768
v_mfma_f32_16x16x32_bf16 a[0:3], v[20:23], v[164:167], a[0:3]
s_waitcnt vmcnt(22)
s_lshl_b32 s81, s81, 19
v_permlane32_swap_b32 v224, v226
v_mfma_f32_16x16x32_bf16 a[4:7], v[24:27], v[160:163], a[4:7]
s_add_u32 s81, s81, s79
ds_read_b64_tr_b16 v[80:81], v0 offset:32768
v_add_f32 v224, v224, v226
v_mfma_f32_16x16x32_bf16 a[4:7], v[28:31], v[164:167], a[4:7]
s_mul_i32 s66, s61, 1215488
v_permlane32_swap_b32 v225, v227
v_mfma_f32_16x16x32_bf16 a[8:11], v[32:35], v[160:163], a[8:11]
buffer_load_dwordx4 v8, s[24:27], s81 offen offset:0 lds
v_mfma_f32_16x16x32_bf16 a[8:11], v[36:39], v[164:167], a[8:11]
s_add_u32 s66, s66, 50331648
ds_read_b64_tr_b16 v[82:83], v0 offset:33024
v_add_f32 v225, v225, v227
v_mfma_f32_16x16x32_bf16 a[12:15], v[40:43], v[160:163], a[12:15]
s_add_u32 s56, s52, s66
v_permlane16_swap_b32 v224, v225
v_mfma_f32_16x16x32_bf16 a[12:15], v[44:47], v[164:167], a[12:15]
v_add_f32 v224, v224, v225
v_cvt_pk_bf16_f32 v224, v224, v224
v_mfma_f32_16x16x32_bf16 a[16:19], v[160:163], v[48:51], a[16:19]
buffer_load_dwordx4 v9, s[24:27], s81 offen offset:1024 lds
v_mfma_f32_16x16x32_bf16 a[16:19], v[164:167], v[52:55], a[16:19]
L_pad_a_2:
s_cbranch_execz L_pad_a_2_1
ds_read_b64_tr_b16 v[84:85], v0 offset:36864
v_and_b32 v224, 0xffff0000, v224
v_mfma_f32_16x16x32_bf16 a[20:23], v[160:163], v[56:59], a[20:23]
ds_read_b128 v[216:219], v254 offset:512
v_mul_f32 v224, 0.12751743082459868, v224
v_mfma_f32_16x16x32_bf16 a[20:23], v[164:167], v[60:63], a[20:23]
ds_read_b64_tr_b16 v[86:87], v0 offset:37120
v_and_b32 v186, 1, v2
v_mfma_f32_16x16x32_bf16 a[24:27], v[160:163], v[64:67], a[24:27]
ds_read_b128 v[220:223], v254 offset:528
v_and_b32 v187, 2, v2
v_mfma_f32_16x16x32_bf16 a[24:27], v[164:167], v[68:71], a[24:27]
ds_read_b128 v[232:235], v254 offset:768
v_lshl_add_u32 v186, v187, 2, v186
v_mfma_f32_16x16x32_bf16 a[28:31], v[160:163], v[72:75], a[28:31]
ds_read_b64_tr_b16 v[88:89], v3 offset:32768
v_bfe_u32 v187, v2, 2, 1
v_mfma_f32_16x16x32_bf16 a[28:31], v[164:167], v[76:79], a[28:31]
buffer_load_dwordx4 v10, s[24:27], s81 offen offset:2048 lds
v_mfma_f32_16x16x32_bf16 a[32:35], v[16:19], v[168:171], a[32:35]
ds_read_b128 v[160:163], v12 offset:2048
v_mfma_f32_16x16x32_bf16 a[32:35], v[20:23], v[172:175], a[32:35]
ds_read_b128 v[164:167], v13 offset:2048
v_mfma_f32_16x16x32_bf16 a[36:39], v[24:27], v[168:171], a[36:39]
ds_read_b64_tr_b16 v[90:91], v3 offset:33024
v_mfma_f32_16x16x32_bf16 a[36:39], v[28:31], v[172:175], a[36:39]
ds_read_b128 v[236:239], v254 offset:784
v_mfma_f32_16x16x32_bf16 a[40:43], v[32:35], v[168:171], a[40:43]
s_waitcnt lgkmcnt(4)
ds_read_b64_tr_b16 v[92:93], v3 offset:36864
v_fmac_f32 v216, v192, v232
v_mfma_f32_16x16x32_bf16 a[40:43], v[36:39], v[172:175], a[40:43]
ds_write_b32 v253, v224 offset:1024
v_mfma_f32_16x16x32_bf16 a[44:47], v[40:43], v[168:171], a[44:47]
buffer_load_dwordx4 v11, s[24:27], s81 offen offset:3072 lds
v_mfma_f32_16x16x32_bf16 a[44:47], v[44:47], v[172:175], a[44:47]
ds_read_b64_tr_b16 v[94:95], v3 offset:37120
v_fmac_f32 v217, v193, v233
v_mfma_f32_16x16x32_bf16 a[48:51], v[168:171], v[48:51], a[48:51]
s_waitcnt vmcnt(24)
s_add_u32 s81, s7, 6
v_permlane32_swap_b32 v228, v230
v_mfma_f32_16x16x32_bf16 a[48:51], v[172:175], v[52:55], a[48:51]
s_and_b32 s81, s81, 7
v_permlane32_swap_b32 v229, v231
v_mfma_f32_16x16x32_bf16 a[52:55], v[168:171], v[56:59], a[52:55]
s_lshl_b32 s81, s81, 7
ds_read_b64_tr_b16 v[96:97], v0 offset:33280
v_add_f32 v228, v228, v230
v_mfma_f32_16x16x32_bf16 a[52:55], v[172:175], v[60:63], a[52:55]
s_add_u32 m0, s80, 81920
v_add_f32 v229, v229, v231
v_fmac_f32 v218, v194, v234
v_mfma_f32_16x16x32_bf16 a[56:59], v[168:171], v[64:67], a[56:59]
s_add_u32 s81, s81, s78
v_permlane16_swap_b32 v228, v229
v_mfma_f32_16x16x32_bf16 a[56:59], v[172:175], v[68:71], a[56:59]
s_add_u32 s81, s81, 1048576
ds_read_b64_tr_b16 v[98:99], v0 offset:33536
v_add_f32 v228, v228, v229
v_mfma_f32_16x16x32_bf16 a[60:63], v[168:171], v[72:75], a[60:63]
buffer_load_dwordx4 v4, s[12:15], s81 offen offset:0 lds
v_mfma_f32_16x16x32_bf16 a[60:63], v[172:175], v[76:79], a[60:63]
ds_read_b128 v[168:171], v12 offset:4096
v_cvt_pk_bf16_f32 v228, v228, v228
v_mfma_f32_16x16x32_bf16 a[64:67], v[16:19], v[176:179], a[64:67]
ds_read_b64_tr_b16 v[100:101], v0 offset:37376
v_and_b32 v228, 0xffff0000, v228
v_mfma_f32_16x16x32_bf16 a[64:67], v[20:23], v[180:183], a[64:67]
ds_read_b128 v[172:175], v13 offset:4096
v_fmac_f32 v219, v195, v235
v_mfma_f32_16x16x32_bf16 a[68:71], v[24:27], v[176:179], a[68:71]
s_waitcnt lgkmcnt(3)
v_fmac_f32 v220, v196, v236
v_fmac_f32 v221, v197, v237
v_mfma_f32_16x16x32_bf16 a[68:71], v[28:31], v[180:183], a[68:71]
ds_read_b64_tr_b16 v[102:103], v0 offset:37632
v_fmac_f32 v222, v198, v238
v_mfma_f32_16x16x32_bf16 a[72:75], v[32:35], v[176:179], a[72:75]
buffer_load_dwordx4 v5, s[12:15], s81 offen offset:1024 lds
v_mfma_f32_16x16x32_bf16 a[72:75], v[36:39], v[180:183], a[72:75]
)ASM"
R"ASM(
v_add_f32 v228, 1.0, v228
v_fmac_f32 v223, v199, v239
v_mfma_f32_16x16x32_bf16 a[76:79], v[40:43], v[176:179], a[76:79]
ds_read_b64_tr_b16 v[104:105], v3 offset:33280
v_cvt_pk_bf16_f32 v192, v216, v217
v_mfma_f32_16x16x32_bf16 a[76:79], v[44:47], v[180:183], a[76:79]
v_cvt_pk_bf16_f32 v193, v218, v219
v_cvt_pk_bf16_f32 v194, v220, v221
v_mfma_f32_16x16x32_bf16 a[80:83], v[176:179], v[48:51], a[80:83]
v_cvt_pk_bf16_f32 v228, v228, v228
v_cvt_pk_bf16_f32 v195, v222, v223
v_mfma_f32_16x16x32_bf16 a[80:83], v[180:183], v[52:55], a[80:83]
buffer_store_dwordx4 v[192:195], v252, s[52:55], s67 offen sc1
v_mfma_f32_16x16x32_bf16 a[84:87], v[176:179], v[56:59], a[84:87]
ds_read_b64_tr_b16 v[106:107], v3 offset:33536
v_and_b32 v228, 0xffff0000, v228
v_mfma_f32_16x16x32_bf16 a[84:87], v[180:183], v[60:63], a[84:87]
ds_write_b32 v253, v228 offset:1280
v_mfma_f32_16x16x32_bf16 a[88:91], v[176:179], v[64:67], a[88:91]
ds_read_b64_tr_b16 v[108:109], v3 offset:37376
v_add_u32 v0, 0x10000, v0
v_mfma_f32_16x16x32_bf16 a[88:91], v[180:183], v[68:71], a[88:91]
s_cmp_lt_u32 s56, s66
s_cselect_b32 s67, 1, 0
ds_read_b128 v[208:211], v254 offset:1024
v_mfma_f32_16x16x32_bf16 a[92:95], v[176:179], v[72:75], a[92:95]
s_mul_i32 s66, s62, 75776
ds_read_b128 v[176:179], v12 offset:6144
v_add_u32 v12, 0x1c000, v12
v_mfma_f32_16x16x32_bf16 a[92:95], v[180:183], v[76:79], a[92:95]
s_sub_u32 s58, 1207296, s66
ds_read_b64_tr_b16 v[110:111], v3 offset:37632
v_add_u32 v3, 0x10000, v3
v_mfma_f32_16x16x32_bf16 a[96:99], v[16:19], v[144:147], a[96:99]
buffer_load_dwordx4 v6, s[12:15], s81 offen offset:2048 lds
v_mfma_f32_16x16x32_bf16 a[96:99], v[20:23], v[148:151], a[96:99]
s_add_u32 s57, s53, s67
ds_read_b128 v[180:183], v13 offset:6144
v_add_u32 v13, 0x1c000, v13
v_mfma_f32_16x16x32_bf16 a[100:103], v[24:27], v[144:147], a[100:103]
s_lshl_b32 s66, s62, 3
s_add_u32 s66, s66, s60
ds_read_b128 v[212:215], v254 offset:1040
v_mfma_f32_16x16x32_bf16 a[100:103], v[28:31], v[148:151], a[100:103]
s_mul_i32 s66, s66, 136192
s_lshl_b32 s67, s61, 12
ds_read_b128 v[224:227], v254 offset:1280
v_mfma_f32_16x16x32_bf16 a[104:107], v[32:35], v[144:147], a[104:107]
buffer_load_dwordx4 v7, s[12:15], s81 offen offset:3072 lds
v_mfma_f32_16x16x32_bf16 a[104:107], v[36:39], v[148:151], a[104:107]
s_add_u32 s67, s67, 29360128
ds_read_b128 v[228:231], v254 offset:1296
v_mfma_f32_16x16x32_bf16 a[108:111], v[40:43], v[144:147], a[108:111]
v_mfma_f32_16x16x32_bf16 a[108:111], v[44:47], v[148:151], a[108:111]
v_mfma_f32_16x16x32_bf16 a[112:115], v[144:147], v[48:51], a[112:115]
v_mfma_f32_16x16x32_bf16 a[112:115], v[148:151], v[52:55], a[112:115]
v_mfma_f32_16x16x32_bf16 a[116:119], v[144:147], v[56:59], a[116:119]
s_waitcnt lgkmcnt(0)
v_fmac_f32 v208, v200, v224
v_fmac_f32 v209, v201, v225
v_mfma_f32_16x16x32_bf16 a[116:119], v[148:151], v[60:63], a[116:119]
v_fmac_f32 v210, v202, v226
v_fmac_f32 v211, v203, v227
v_mfma_f32_16x16x32_bf16 a[120:123], v[144:147], v[64:67], a[120:123]
s_add_u32 s81, s7, 7
v_fmac_f32 v212, v204, v228
v_fmac_f32 v213, v205, v229
v_mfma_f32_16x16x32_bf16 a[120:123], v[148:151], v[68:71], a[120:123]
s_and_b32 s81, s81, 7
v_fmac_f32 v214, v206, v230
v_fmac_f32 v215, v207, v231
v_mfma_f32_16x16x32_bf16 a[124:127], v[144:147], v[72:75], a[124:127]
s_waitcnt vmcnt(22)
s_barrier
v_cvt_pk_bf16_f32 v200, v208, v209
v_cvt_pk_bf16_f32 v201, v210, v211
v_mfma_f32_16x16x32_bf16 a[124:127], v[148:151], v[76:79], a[124:127]
s_add_u32 m0, s80, 16384
v_cvt_pk_bf16_f32 v202, v212, v213
v_cvt_pk_bf16_f32 v203, v214, v215
v_mfma_f32_16x16x32_bf16 a[128:131], v[16:19], v[152:155], a[128:131]
buffer_store_dwordx4 v[200:203], v252, s[52:55], s68 offen sc1
v_mfma_f32_16x16x32_bf16 a[128:131], v[20:23], v[156:159], a[128:131]
s_lshl_b32 s81, s81, 19
s_add_u32 s81, s81, s79
ds_read_b128 v[144:147], v12 offset:0
v_mfma_f32_16x16x32_bf16 a[132:135], v[24:27], v[152:155], a[132:135]
ds_read_b128 v[148:151], v13 offset:0
v_mfma_f32_16x16x32_bf16 a[132:135], v[28:31], v[156:159], a[132:135]
ds_read_b64_tr_b16 v[112:113], v0 offset:0
v_mfma_f32_16x16x32_bf16 a[136:139], v[32:35], v[152:155], a[136:139]
v_permlane32_swap_b32 v244, v246
v_mfma_f32_16x16x32_bf16 a[136:139], v[36:39], v[156:159], a[136:139]
ds_read_b64_tr_b16 v[114:115], v0 offset:256
v_add_f32 v244, v244, v246
v_mfma_f32_16x16x32_bf16 a[140:143], v[40:43], v[152:155], a[140:143]
v_permlane32_swap_b32 v245, v247
v_mfma_f32_16x16x32_bf16 a[140:143], v[44:47], v[156:159], a[140:143]
buffer_load_dwordx4 v8, s[20:23], s81 offen offset:0 lds
v_mfma_f32_16x16x32_bf16 a[144:147], v[152:155], v[48:51], a[144:147]
ds_read_b64_tr_b16 v[116:117], v0 offset:4096
v_add_f32 v245, v245, v247
v_mfma_f32_16x16x32_bf16 a[144:147], v[156:159], v[52:55], a[144:147]
s_nop 0
v_permlane16_swap_b32 v244, v245
v_mfma_f32_16x16x32_bf16 a[148:151], v[152:155], v[56:59], a[148:151]
v_add_f32 v244, v244, v245
v_cvt_pk_bf16_f32 v244, v244, v244
v_mfma_f32_16x16x32_bf16 a[148:151], v[156:159], v[60:63], a[148:151]
buffer_load_dwordx4 v9, s[20:23], s81 offen offset:1024 lds
v_mfma_f32_16x16x32_bf16 a[152:155], v[152:155], v[64:67], a[152:155]
ds_read_b64_tr_b16 v[118:119], v0 offset:4352
v_and_b32 v244, 0xffff0000, v244
v_mfma_f32_16x16x32_bf16 a[152:155], v[156:159], v[68:71], a[152:155]
v_add_f32 v244, s71, v244
v_cvt_pk_bf16_f32 v244, v244, v244
v_mfma_f32_16x16x32_bf16 a[156:159], v[152:155], v[72:75], a[156:159]
ds_read_b64_tr_b16 v[120:121], v3 offset:0
v_mfma_f32_16x16x32_bf16 a[156:159], v[156:159], v[76:79], a[156:159]
buffer_load_dwordx4 v10, s[20:23], s81 offen offset:2048 lds
v_mfma_f32_16x16x32_bf16 a[160:163], v[16:19], v[160:163], a[160:163]
ds_read_b128 v[152:155], v12 offset:2048
v_mfma_f32_16x16x32_bf16 a[160:163], v[20:23], v[164:167], a[160:163]
ds_read_b128 v[156:159], v13 offset:2048
v_mfma_f32_16x16x32_bf16 a[164:167], v[24:27], v[160:163], a[164:167]
ds_read_b64_tr_b16 v[122:123], v3 offset:256
v_mfma_f32_16x16x32_bf16 a[164:167], v[28:31], v[164:167], a[164:167]
buffer_store_short v244, v248, s[52:55], s70 offen sc1
v_mfma_f32_16x16x32_bf16 a[168:171], v[32:35], v[160:163], a[168:171]
s_lshl_b32 s70, s60, 1
s_add_u32 s71, s69, s70
ds_read_b64_tr_b16 v[124:125], v3 offset:4096
v_mfma_f32_16x16x32_bf16 a[168:171], v[36:39], v[164:167], a[168:171]
s_sub_u32 s70, s70, s69
s_add_u32 s70, s70, 240
s_sub_u32 s70, s70, s71
v_mfma_f32_16x16x32_bf16 a[172:175], v[40:43], v[160:163], a[172:175]
s_mov_b32 s69, 0
v_mul_u32_u24 v187, s70, v187
v_add3_u32 v186, v186, v187, s71
v_mfma_f32_16x16x32_bf16 a[172:175], v[44:47], v[164:167], a[172:175]
buffer_load_dwordx4 v11, s[20:23], s81 offen offset:3072 lds
v_mfma_f32_16x16x32_bf16 a[176:179], v[160:163], v[48:51], a[176:179]
s_add_u32 s81, s7, 7
s_and_b32 s81, s81, 7
ds_read_b64_tr_b16 v[126:127], v3 offset:4352
v_mfma_f32_16x16x32_bf16 a[176:179], v[164:167], v[52:55], a[176:179]
s_lshl_b32 s81, s81, 7
s_add_u32 s81, s81, s78
v_lshlrev_b32 v186, 8, v186
v_mfma_f32_16x16x32_bf16 a[180:183], v[160:163], v[56:59], a[180:183]
ds_read_b64_tr_b16 v[128:129], v0 offset:512
v_add_u32 v186, 131072, v186
v_mfma_f32_16x16x32_bf16 a[180:183], v[164:167], v[60:63], a[180:183]
s_add_u32 m0, s80, 114688
s_cmp_eq_u32 s62, 0
s_cselect_b32 s71, 1.0, 0
v_mfma_f32_16x16x32_bf16 a[184:187], v[160:163], v[64:67], a[184:187]
v_mfma_f32_16x16x32_bf16 a[184:187], v[164:167], v[68:71], a[184:187]
buffer_load_dwordx4 v4, s[12:15], s81 offen offset:0 lds
v_mfma_f32_16x16x32_bf16 a[188:191], v[160:163], v[72:75], a[188:191]
ds_read_b64_tr_b16 v[130:131], v0 offset:768
v_mfma_f32_16x16x32_bf16 a[188:191], v[164:167], v[76:79], a[188:191]
ds_read_b128 v[160:163], v12 offset:4096
v_mfma_f32_16x16x32_bf16 a[192:195], v[16:19], v[168:171], a[192:195]
ds_read_b64_tr_b16 v[132:133], v0 offset:4608
v_mfma_f32_16x16x32_bf16 a[192:195], v[20:23], v[172:175], a[192:195]
ds_read_b128 v[164:167], v13 offset:4096
v_mfma_f32_16x16x32_bf16 a[196:199], v[24:27], v[168:171], a[196:199]
v_mfma_f32_16x16x32_bf16 a[196:199], v[28:31], v[172:175], a[196:199]
ds_read_b64_tr_b16 v[134:135], v0 offset:4864
v_add_u32 v0, 0xfffe4000, v0
v_mfma_f32_16x16x32_bf16 a[200:203], v[32:35], v[168:171], a[200:203]
buffer_load_dwordx4 v5, s[12:15], s81 offen offset:1024 lds
v_mfma_f32_16x16x32_bf16 a[200:203], v[36:39], v[172:175], a[200:203]
v_mfma_f32_16x16x32_bf16 a[204:207], v[40:43], v[168:171], a[204:207]
ds_read_b64_tr_b16 v[136:137], v3 offset:512
v_mfma_f32_16x16x32_bf16 a[204:207], v[44:47], v[172:175], a[204:207]
v_mfma_f32_16x16x32_bf16 a[208:211], v[168:171], v[48:51], a[208:211]
v_mfma_f32_16x16x32_bf16 a[208:211], v[172:175], v[52:55], a[208:211]
ds_read_b64_tr_b16 v[138:139], v3 offset:768
v_mfma_f32_16x16x32_bf16 a[212:215], v[168:171], v[56:59], a[212:215]
v_mfma_f32_16x16x32_bf16 a[212:215], v[172:175], v[60:63], a[212:215]
buffer_load_dwordx4 v6, s[12:15], s81 offen offset:2048 lds
v_mfma_f32_16x16x32_bf16 a[216:219], v[168:171], v[64:67], a[216:219]
ds_read_b64_tr_b16 v[140:141], v3 offset:4608
v_mfma_f32_16x16x32_bf16 a[216:219], v[172:175], v[68:71], a[216:219]
v_mfma_f32_16x16x32_bf16 a[220:223], v[168:171], v[72:75], a[220:223]
ds_read_b128 v[168:171], v12 offset:6144
v_add_u32 v12, 0xfffe0000, v12
v_mfma_f32_16x16x32_bf16 a[220:223], v[172:175], v[76:79], a[220:223]
ds_read_b64_tr_b16 v[142:143], v3 offset:4864
v_add_u32 v3, 0xfffe4000, v3
v_mfma_f32_16x16x32_bf16 a[224:227], v[16:19], v[176:179], a[224:227]
ds_read_b128 v[172:175], v13 offset:6144
v_add_u32 v13, 0xfffe0000, v13
v_mfma_f32_16x16x32_bf16 a[224:227], v[20:23], v[180:183], a[224:227]
v_mfma_f32_16x16x32_bf16 a[228:231], v[24:27], v[176:179], a[228:231]
buffer_load_dwordx4 v7, s[12:15], s81 offen offset:3072 lds
v_mfma_f32_16x16x32_bf16 a[228:231], v[28:31], v[180:183], a[228:231]
v_mfma_f32_16x16x32_bf16 a[232:235], v[32:35], v[176:179], a[232:235]
v_mfma_f32_16x16x32_bf16 a[232:235], v[36:39], v[180:183], a[232:235]
v_mfma_f32_16x16x32_bf16 a[236:239], v[40:43], v[176:179], a[236:239]
v_mfma_f32_16x16x32_bf16 a[236:239], v[44:47], v[180:183], a[236:239]
v_mfma_f32_16x16x32_bf16 a[240:243], v[176:179], v[48:51], a[240:243]
v_mfma_f32_16x16x32_bf16 a[240:243], v[180:183], v[52:55], a[240:243]
v_mfma_f32_16x16x32_bf16 a[244:247], v[176:179], v[56:59], a[244:247]
v_mfma_f32_16x16x32_bf16 a[244:247], v[180:183], v[60:63], a[244:247]
v_mfma_f32_16x16x32_bf16 a[248:251], v[176:179], v[64:67], a[248:251]
s_add_u32 s81, s7, 7
s_and_b32 s81, s81, 7
s_lshl_b32 s81, s81, 19
v_mfma_f32_16x16x32_bf16 a[248:251], v[180:183], v[68:71], a[248:251]
s_waitcnt vmcnt(9) lgkmcnt(0)
s_add_u32 s81, s81, s79
v_mfma_f32_16x16x32_bf16 a[252:255], v[176:179], v[72:75], a[252:255]
s_barrier
ds_read_b128 v[176:179], v12 offset:0
v_mfma_f32_16x16x32_bf16 a[252:255], v[180:183], v[76:79], a[252:255]
s_add_u32 m0, s80, 131072
ds_read_b128 v[180:183], v13 offset:0
v_mfma_f32_16x16x32_bf16 a[0:3], v[80:83], v[144:147], a[0:3]
buffer_store_dwordx2 v[240:241], v242, s[52:55], s73 offen sc1
v_mfma_f32_16x16x32_bf16 a[0:3], v[84:87], v[148:151], a[0:3]
s_add_u32 s73, s66, s67
s_lshl_b32 s66, s62, 3
s_sub_u32 s66, s60, s66
v_mfma_f32_16x16x32_bf16 a[4:7], v[88:91], v[144:147], a[4:7]
s_add_u32 s66, s66, 120
s_mul_i32 s66, s66, 136192
ds_read_b64_tr_b16 v[16:17], v0 offset:0
v_mfma_f32_16x16x32_bf16 a[4:7], v[92:95], v[148:151], a[4:7]
s_add_u32 s75, s66, s67
s_lshl_b32 s66, s60, 12
s_add_u32 s68, s66, 98304
v_mfma_f32_16x16x32_bf16 a[8:11], v[96:99], v[144:147], a[8:11]
buffer_load_dwordx4 v8, s[24:27], s81 offen offset:0 lds
v_mfma_f32_16x16x32_bf16 a[8:11], v[100:103], v[148:151], a[8:11]
s_add_u32 s74, s73, 544768
s_add_u32 s76, s75, 544768
ds_read_b64_tr_b16 v[18:19], v0 offset:256
v_mfma_f32_16x16x32_bf16 a[12:15], v[104:107], v[144:147], a[12:15]
v_mfma_f32_16x16x32_bf16 a[12:15], v[108:111], v[148:151], a[12:15]
v_mfma_f32_16x16x32_bf16 a[16:19], v[144:147], v[112:115], a[16:19]
buffer_load_dwordx4 v9, s[24:27], s81 offen offset:1024 lds
v_mfma_f32_16x16x32_bf16 a[16:19], v[148:151], v[116:119], a[16:19]
ds_read_b64_tr_b16 v[20:21], v0 offset:4096
v_mfma_f32_16x16x32_bf16 a[20:23], v[144:147], v[120:123], a[20:23]
v_mfma_f32_16x16x32_bf16 a[20:23], v[148:151], v[124:127], a[20:23]
ds_read_b64_tr_b16 v[22:23], v0 offset:4352
v_mfma_f32_16x16x32_bf16 a[24:27], v[144:147], v[128:131], a[24:27]
buffer_load_dwordx4 v10, s[24:27], s81 offen offset:2048 lds
v_mfma_f32_16x16x32_bf16 a[24:27], v[148:151], v[132:135], a[24:27]
v_mfma_f32_16x16x32_bf16 a[28:31], v[144:147], v[136:139], a[28:31]
ds_read_b64_tr_b16 v[24:25], v3 offset:0
v_mfma_f32_16x16x32_bf16 a[28:31], v[148:151], v[140:143], a[28:31]
ds_read_b128 v[144:147], v12 offset:2048
v_mfma_f32_16x16x32_bf16 a[32:35], v[80:83], v[152:155], a[32:35]
buffer_load_dwordx4 v11, s[24:27], s81 offen offset:3072 lds
v_mfma_f32_16x16x32_bf16 a[32:35], v[84:87], v[156:159], a[32:35]
s_add_u32 s81, s7, 7
s_and_b32 s81, s81, 7
ds_read_b128 v[148:151], v13 offset:2048
v_mfma_f32_16x16x32_bf16 a[36:39], v[88:91], v[152:155], a[36:39]
s_lshl_b32 s81, s81, 7
s_add_u32 s81, s81, s78
ds_read_b64_tr_b16 v[26:27], v3 offset:256
v_mfma_f32_16x16x32_bf16 a[36:39], v[92:95], v[156:159], a[36:39]
s_add_u32 m0, s80, 147456
s_add_u32 s81, s81, 1048576
s_lshl_b32 s78, s61, 2
v_mfma_f32_16x16x32_bf16 a[40:43], v[96:99], v[152:155], a[40:43]
s_add_u32 s78, s78, s62
s_sub_u32 s78, s78, 4
ds_read_b64_tr_b16 v[28:29], v3 offset:4096
v_mfma_f32_16x16x32_bf16 a[40:43], v[100:103], v[156:159], a[40:43]
buffer_load_dwordx4 v4, s[12:15], s81 offen offset:0 lds
v_mfma_f32_16x16x32_bf16 a[44:47], v[104:107], v[152:155], a[44:47]
s_sub_u32 s100, s78, 64
s_min_u32 s100, s100, s78
v_add_u32 v4, s94, v1
v_mfma_f32_16x16x32_bf16 a[44:47], v[108:111], v[156:159], a[44:47]
s_add_u32 s94, s62, 1
s_sub_u32 s5, s94, 10
ds_read_b64_tr_b16 v[30:31], v3 offset:4352
v_mfma_f32_16x16x32_bf16 a[48:51], v[152:155], v[112:115], a[48:51]
s_min_u32 s94, s94, s5
s_lshl_b32 s94, s94, 11
v_mfma_f32_16x16x32_bf16 a[48:51], v[156:159], v[116:119], a[48:51]
v_mfma_f32_16x16x32_bf16 a[52:55], v[152:155], v[120:123], a[52:55]
buffer_load_dwordx4 v5, s[12:15], s81 offen offset:1024 lds
v_mfma_f32_16x16x32_bf16 a[52:55], v[156:159], v[124:127], a[52:55]
ds_read_b64_tr_b16 v[32:33], v0 offset:512
v_lshrrev_b32 v5, 4, v2
v_mfma_f32_16x16x32_bf16 a[56:59], v[152:155], v[128:131], a[56:59]
v_lshlrev_b32 v5, 28, v5
v_lshl_add_u32 v5, v2, 3, v5
v_mfma_f32_16x16x32_bf16 a[56:59], v[156:159], v[132:135], a[56:59]
ds_read_b64_tr_b16 v[34:35], v0 offset:768
v_mfma_f32_16x16x32_bf16 a[60:63], v[152:155], v[136:139], a[60:63]
ds_read_b128 v[152:155], v12 offset:4096
v_mfma_f32_16x16x32_bf16 a[60:63], v[156:159], v[140:143], a[60:63]
buffer_load_dwordx4 v6, s[12:15], s81 offen offset:2048 lds
v_mfma_f32_16x16x32_bf16 a[64:67], v[80:83], v[160:163], a[64:67]
ds_read_b64_tr_b16 v[36:37], v0 offset:4608
v_and_b32 v6, 7, v2
v_mfma_f32_16x16x32_bf16 a[64:67], v[84:87], v[164:167], a[64:67]
ds_read_b128 v[156:159], v13 offset:4096
v_lshlrev_b32 v6, 8, v6
v_mfma_f32_16x16x32_bf16 a[68:71], v[88:91], v[160:163], a[68:71]
v_mfma_f32_16x16x32_bf16 a[68:71], v[92:95], v[164:167], a[68:71]
ds_read_b64_tr_b16 v[38:39], v0 offset:4864
v_mfma_f32_16x16x32_bf16 a[72:75], v[96:99], v[160:163], a[72:75]
buffer_load_dwordx4 v7, s[12:15], s81 offen offset:3072 lds
v_mfma_f32_16x16x32_bf16 a[72:75], v[100:103], v[164:167], a[72:75]
s_add_u32 s81, s62, 0
s_sub_u32 s6, s81, 10
s_min_u32 s81, s81, s6
v_mfma_f32_16x16x32_bf16 a[76:79], v[104:107], v[160:163], a[76:79]
s_lshl_b32 s81, s81, 19
s_add_u32 s81, s81, s79
ds_read_b64_tr_b16 v[40:41], v3 offset:512
v_mfma_f32_16x16x32_bf16 a[76:79], v[108:111], v[164:167], a[76:79]
s_add_u32 m0, s80, 98304
v_and_b32 v7, 15, v2
v_mfma_f32_16x16x32_bf16 a[80:83], v[160:163], v[112:115], a[80:83]
v_mfma_f32_16x16x32_bf16 a[80:83], v[164:167], v[116:119], a[80:83]
buffer_load_dwordx4 v8, s[16:19], s81 offen offset:0 lds
v_mfma_f32_16x16x32_bf16 a[84:87], v[160:163], v[120:123], a[84:87]
ds_read_b64_tr_b16 v[42:43], v3 offset:768
v_mfma_f32_16x16x32_bf16 a[84:87], v[164:167], v[124:127], a[84:87]
v_mfma_f32_16x16x32_bf16 a[88:91], v[160:163], v[128:131], a[88:91]
ds_read_b64_tr_b16 v[44:45], v3 offset:4608
v_mfma_f32_16x16x32_bf16 a[88:91], v[164:167], v[132:135], a[88:91]
v_mfma_f32_16x16x32_bf16 a[92:95], v[160:163], v[136:139], a[92:95]
buffer_load_dwordx4 v9, s[16:19], s81 offen offset:1024 lds
v_mfma_f32_16x16x32_bf16 a[92:95], v[164:167], v[140:143], a[92:95]
ds_read_b64_tr_b16 v[46:47], v3 offset:4864
v_mfma_f32_16x16x32_bf16 a[96:99], v[80:83], v[168:171], a[96:99]
ds_read_b128 v[160:163], v12 offset:6144
v_mfma_f32_16x16x32_bf16 a[96:99], v[84:87], v[172:175], a[96:99]
ds_read_b128 v[164:167], v13 offset:6144
v_mfma_f32_16x16x32_bf16 a[100:103], v[88:91], v[168:171], a[100:103]
v_mfma_f32_16x16x32_bf16 a[100:103], v[92:95], v[172:175], a[100:103]
buffer_load_dwordx4 v10, s[16:19], s81 offen offset:2048 lds
v_mfma_f32_16x16x32_bf16 a[104:107], v[96:99], v[168:171], a[104:107]
v_mfma_f32_16x16x32_bf16 a[104:107], v[100:103], v[172:175], a[104:107]
v_mfma_f32_16x16x32_bf16 a[108:111], v[104:107], v[168:171], a[108:111]
v_mfma_f32_16x16x32_bf16 a[108:111], v[108:111], v[172:175], a[108:111]
v_mfma_f32_16x16x32_bf16 a[112:115], v[168:171], v[112:115], a[112:115]
buffer_load_dwordx4 v11, s[16:19], s81 offen offset:3072 lds
v_mfma_f32_16x16x32_bf16 a[112:115], v[172:175], v[116:119], a[112:115]
v_mfma_f32_16x16x32_bf16 a[116:119], v[168:171], v[120:123], a[116:119]
v_mfma_f32_16x16x32_bf16 a[116:119], v[172:175], v[124:127], a[116:119]
v_mfma_f32_16x16x32_bf16 a[120:123], v[168:171], v[128:131], a[120:123]
s_add_u32 s81, s62, 1
s_sub_u32 s6, s81, 10
s_min_u32 s81, s81, s6
v_mfma_f32_16x16x32_bf16 a[120:123], v[172:175], v[132:135], a[120:123]
s_waitcnt lgkmcnt(0)
s_lshl_b32 s81, s81, 19
s_add_u32 s81, s81, s79
v_mfma_f32_16x16x32_bf16 a[124:127], v[168:171], v[136:139], a[124:127]
s_barrier
ds_read_b128 v[168:171], v12 offset:49152
v_mfma_f32_16x16x32_bf16 a[124:127], v[172:175], v[140:143], a[124:127]
s_add_u32 m0, s80, 0
ds_read_b128 v[172:175], v13 offset:49152
v_mfma_f32_16x16x32_bf16 a[128:131], v[80:83], v[176:179], a[128:131]
v_mfma_f32_16x16x32_bf16 a[128:131], v[84:87], v[180:183], a[128:131]
buffer_load_dwordx4 v8, s[16:19], s81 offen offset:0 lds
v_mfma_f32_16x16x32_bf16 a[132:135], v[88:91], v[176:179], a[132:135]
ds_read_b64_tr_b16 v[48:49], v0 offset:32768
v_mfma_f32_16x16x32_bf16 a[132:135], v[92:95], v[180:183], a[132:135]
v_mfma_f32_16x16x32_bf16 a[136:139], v[96:99], v[176:179], a[136:139]
v_mfma_f32_16x16x32_bf16 a[136:139], v[100:103], v[180:183], a[136:139]
ds_read_b64_tr_b16 v[50:51], v0 offset:33024
v_mfma_f32_16x16x32_bf16 a[140:143], v[104:107], v[176:179], a[140:143]
v_mfma_f32_16x16x32_bf16 a[140:143], v[108:111], v[180:183], a[140:143]
v_mfma_f32_16x16x32_bf16 a[144:147], v[176:179], v[112:115], a[144:147]
buffer_load_dwordx4 v9, s[16:19], s81 offen offset:1024 lds
v_mfma_f32_16x16x32_bf16 a[144:147], v[180:183], v[116:119], a[144:147]
ds_read_b64_tr_b16 v[52:53], v0 offset:36864
v_mfma_f32_16x16x32_bf16 a[148:151], v[176:179], v[120:123], a[148:151]
v_mfma_f32_16x16x32_bf16 a[148:151], v[180:183], v[124:127], a[148:151]
ds_read_b64_tr_b16 v[54:55], v0 offset:37120
v_mfma_f32_16x16x32_bf16 a[152:155], v[176:179], v[128:131], a[152:155]
v_mfma_f32_16x16x32_bf16 a[152:155], v[180:183], v[132:135], a[152:155]
v_mfma_f32_16x16x32_bf16 a[156:159], v[176:179], v[136:139], a[156:159]
ds_read_b64_tr_b16 v[56:57], v3 offset:32768
v_mfma_f32_16x16x32_bf16 a[156:159], v[180:183], v[140:143], a[156:159]
buffer_load_dwordx4 v10, s[16:19], s81 offen offset:2048 lds
v_mfma_f32_16x16x32_bf16 a[160:163], v[80:83], v[144:147], a[160:163]
ds_read_b128 v[176:179], v12 offset:51200
v_mfma_f32_16x16x32_bf16 a[160:163], v[84:87], v[148:151], a[160:163]
ds_read_b128 v[180:183], v13 offset:51200
v_mfma_f32_16x16x32_bf16 a[164:167], v[88:91], v[144:147], a[164:167]
ds_read_b64_tr_b16 v[58:59], v3 offset:33024
v_mfma_f32_16x16x32_bf16 a[164:167], v[92:95], v[148:151], a[164:167]
v_mfma_f32_16x16x32_bf16 a[168:171], v[96:99], v[144:147], a[168:171]
)ASM"
R"ASM(
ds_read_b64_tr_b16 v[60:61], v3 offset:36864
v_mfma_f32_16x16x32_bf16 a[168:171], v[100:103], v[148:151], a[168:171]
v_mfma_f32_16x16x32_bf16 a[172:175], v[104:107], v[144:147], a[172:175]
buffer_load_dwordx4 v11, s[16:19], s81 offen offset:3072 lds
v_mfma_f32_16x16x32_bf16 a[172:175], v[108:111], v[148:151], a[172:175]
ds_read_b64_tr_b16 v[62:63], v3 offset:37120
v_mfma_f32_16x16x32_bf16 a[176:179], v[144:147], v[112:115], a[176:179]
s_add_u32 s81, s62, 2
s_sub_u32 s6, s81, 10
s_min_u32 s81, s81, s6
v_mfma_f32_16x16x32_bf16 a[176:179], v[148:151], v[116:119], a[176:179]
s_lshl_b32 s81, s81, 19
s_add_u32 s81, s81, s79
v_mfma_f32_16x16x32_bf16 a[180:183], v[144:147], v[120:123], a[180:183]
ds_read_b64_tr_b16 v[64:65], v0 offset:33280
v_mfma_f32_16x16x32_bf16 a[180:183], v[148:151], v[124:127], a[180:183]
s_add_u32 m0, s80, 32768
v_mfma_f32_16x16x32_bf16 a[184:187], v[144:147], v[128:131], a[184:187]
v_mfma_f32_16x16x32_bf16 a[184:187], v[148:151], v[132:135], a[184:187]
buffer_load_dwordx4 v8, s[16:19], s81 offen offset:0 lds
v_mfma_f32_16x16x32_bf16 a[188:191], v[144:147], v[136:139], a[188:191]
ds_read_b64_tr_b16 v[66:67], v0 offset:33536
v_mfma_f32_16x16x32_bf16 a[188:191], v[148:151], v[140:143], a[188:191]
ds_read_b128 v[144:147], v12 offset:53248
v_mfma_f32_16x16x32_bf16 a[192:195], v[80:83], v[152:155], a[192:195]
ds_read_b64_tr_b16 v[68:69], v0 offset:37376
v_mfma_f32_16x16x32_bf16 a[192:195], v[84:87], v[156:159], a[192:195]
ds_read_b128 v[148:151], v13 offset:53248
v_mfma_f32_16x16x32_bf16 a[196:199], v[88:91], v[152:155], a[196:199]
v_mfma_f32_16x16x32_bf16 a[196:199], v[92:95], v[156:159], a[196:199]
ds_read_b64_tr_b16 v[70:71], v0 offset:37632
v_add_u32 v0, 0xffffc000, v0
v_mfma_f32_16x16x32_bf16 a[200:203], v[96:99], v[152:155], a[200:203]
buffer_load_dwordx4 v9, s[16:19], s81 offen offset:1024 lds
v_mfma_f32_16x16x32_bf16 a[200:203], v[100:103], v[156:159], a[200:203]
v_mfma_f32_16x16x32_bf16 a[204:207], v[104:107], v[152:155], a[204:207]
ds_read_b64_tr_b16 v[72:73], v3 offset:33280
v_mfma_f32_16x16x32_bf16 a[204:207], v[108:111], v[156:159], a[204:207]
v_mfma_f32_16x16x32_bf16 a[208:211], v[152:155], v[112:115], a[208:211]
v_mfma_f32_16x16x32_bf16 a[208:211], v[156:159], v[116:119], a[208:211]
ds_read_b64_tr_b16 v[74:75], v3 offset:33536
v_mfma_f32_16x16x32_bf16 a[212:215], v[152:155], v[120:123], a[212:215]
v_mfma_f32_16x16x32_bf16 a[212:215], v[156:159], v[124:127], a[212:215]
buffer_load_dwordx4 v10, s[16:19], s81 offen offset:2048 lds
v_mfma_f32_16x16x32_bf16 a[216:219], v[152:155], v[128:131], a[216:219]
ds_read_b64_tr_b16 v[76:77], v3 offset:37376
v_mfma_f32_16x16x32_bf16 a[216:219], v[156:159], v[132:135], a[216:219]
v_mfma_f32_16x16x32_bf16 a[220:223], v[152:155], v[136:139], a[220:223]
ds_read_b128 v[152:155], v12 offset:55296
v_add_u32 v12, 0x14000, v12
v_mfma_f32_16x16x32_bf16 a[220:223], v[156:159], v[140:143], a[220:223]
ds_read_b64_tr_b16 v[78:79], v3 offset:37632
v_add_u32 v3, 0xffffc000, v3
v_mfma_f32_16x16x32_bf16 a[224:227], v[80:83], v[160:163], a[224:227]
ds_read_b128 v[156:159], v13 offset:55296
v_add_u32 v13, 0x14000, v13
v_mfma_f32_16x16x32_bf16 a[224:227], v[84:87], v[164:167], a[224:227]
v_mfma_f32_16x16x32_bf16 a[228:231], v[88:91], v[160:163], a[228:231]
buffer_load_dwordx4 v11, s[16:19], s81 offen offset:3072 lds
v_mfma_f32_16x16x32_bf16 a[228:231], v[92:95], v[164:167], a[228:231]
v_mfma_f32_16x16x32_bf16 a[232:235], v[96:99], v[160:163], a[232:235]
v_mfma_f32_16x16x32_bf16 a[232:235], v[100:103], v[164:167], a[232:235]
v_mfma_f32_16x16x32_bf16 a[236:239], v[104:107], v[160:163], a[236:239]
v_mfma_f32_16x16x32_bf16 a[236:239], v[108:111], v[164:167], a[236:239]
v_mfma_f32_16x16x32_bf16 a[240:243], v[160:163], v[112:115], a[240:243]
v_mfma_f32_16x16x32_bf16 a[240:243], v[164:167], v[116:119], a[240:243]
v_mfma_f32_16x16x32_bf16 a[244:247], v[160:163], v[120:123], a[244:247]
v_mfma_f32_16x16x32_bf16 a[244:247], v[164:167], v[124:127], a[244:247]
v_mfma_f32_16x16x32_bf16 a[248:251], v[160:163], v[128:131], a[248:251]
s_add_u32 s81, s62, 3
s_sub_u32 s6, s81, 10
s_min_u32 s81, s81, s6
v_mfma_f32_16x16x32_bf16 a[248:251], v[164:167], v[132:135], a[248:251]
s_waitcnt vmcnt(25) lgkmcnt(0)
s_lshl_b32 s81, s81, 19
s_add_u32 s81, s81, s79
v_mfma_f32_16x16x32_bf16 a[252:255], v[160:163], v[136:139], a[252:255]
s_barrier
ds_read_b128 v[160:163], v12 offset:0
v_mfma_f32_16x16x32_bf16 a[252:255], v[164:167], v[140:143], a[252:255]
s_add_u32 m0, s80, 49152
ds_read_b128 v[164:167], v13 offset:0
v_mfma_f32_16x16x32_bf16 a[0:3], v[16:19], v[168:171], a[0:3]
buffer_load_dwordx2 v[184:185], v186, s[52:55], 0 offen sc1
v_mfma_f32_16x16x32_bf16 a[0:3], v[20:23], v[172:175], a[0:3]
v_mfma_f32_16x16x32_bf16 a[4:7], v[24:27], v[168:171], a[4:7]
ds_read_b64_tr_b16 v[80:81], v0 offset:0
v_mfma_f32_16x16x32_bf16 a[4:7], v[28:31], v[172:175], a[4:7]
v_mfma_f32_16x16x32_bf16 a[8:11], v[32:35], v[168:171], a[8:11]
buffer_load_dwordx4 v8, s[16:19], s81 offen offset:0 lds
v_mfma_f32_16x16x32_bf16 a[8:11], v[36:39], v[172:175], a[8:11]
L_pad_a_3:
s_cbranch_execz L_pad_a_3_1
ds_read_b64_tr_b16 v[82:83], v0 offset:256
v_mfma_f32_16x16x32_bf16 a[12:15], v[40:43], v[168:171], a[12:15]
v_mfma_f32_16x16x32_bf16 a[12:15], v[44:47], v[172:175], a[12:15]
v_mfma_f32_16x16x32_bf16 a[16:19], v[168:171], v[48:51], a[16:19]
buffer_load_dwordx4 v9, s[16:19], s81 offen offset:1024 lds
v_mfma_f32_16x16x32_bf16 a[16:19], v[172:175], v[52:55], a[16:19]
ds_read_b64_tr_b16 v[84:85], v0 offset:4096
v_mfma_f32_16x16x32_bf16 a[20:23], v[168:171], v[56:59], a[20:23]
v_mfma_f32_16x16x32_bf16 a[20:23], v[172:175], v[60:63], a[20:23]
ds_read_b64_tr_b16 v[86:87], v0 offset:4352
v_mfma_f32_16x16x32_bf16 a[24:27], v[168:171], v[64:67], a[24:27]
v_mfma_f32_16x16x32_bf16 a[24:27], v[172:175], v[68:71], a[24:27]
v_mfma_f32_16x16x32_bf16 a[28:31], v[168:171], v[72:75], a[28:31]
ds_read_b64_tr_b16 v[88:89], v3 offset:0
v_mfma_f32_16x16x32_bf16 a[28:31], v[172:175], v[76:79], a[28:31]
buffer_load_dwordx4 v10, s[16:19], s81 offen offset:2048 lds
v_mfma_f32_16x16x32_bf16 a[32:35], v[16:19], v[176:179], a[32:35]
ds_read_b128 v[168:171], v12 offset:2048
v_mfma_f32_16x16x32_bf16 a[32:35], v[20:23], v[180:183], a[32:35]
ds_read_b128 v[172:175], v13 offset:2048
v_mfma_f32_16x16x32_bf16 a[36:39], v[24:27], v[176:179], a[36:39]
ds_read_b64_tr_b16 v[90:91], v3 offset:256
v_mfma_f32_16x16x32_bf16 a[36:39], v[28:31], v[180:183], a[36:39]
v_mfma_f32_16x16x32_bf16 a[40:43], v[32:35], v[176:179], a[40:43]
ds_read_b64_tr_b16 v[92:93], v3 offset:4096
v_mfma_f32_16x16x32_bf16 a[40:43], v[36:39], v[180:183], a[40:43]
v_mfma_f32_16x16x32_bf16 a[44:47], v[40:43], v[176:179], a[44:47]
buffer_load_dwordx4 v11, s[16:19], s81 offen offset:3072 lds
v_mfma_f32_16x16x32_bf16 a[44:47], v[44:47], v[180:183], a[44:47]
ds_read_b64_tr_b16 v[94:95], v3 offset:4352
v_mfma_f32_16x16x32_bf16 a[48:51], v[176:179], v[48:51], a[48:51]
s_add_u32 s81, s62, 4
s_sub_u32 s6, s81, 10
s_min_u32 s81, s81, s6
v_mfma_f32_16x16x32_bf16 a[48:51], v[180:183], v[52:55], a[48:51]
s_lshl_b32 s81, s81, 19
s_add_u32 s81, s81, s79
v_mfma_f32_16x16x32_bf16 a[52:55], v[176:179], v[56:59], a[52:55]
ds_read_b64_tr_b16 v[96:97], v0 offset:512
v_mfma_f32_16x16x32_bf16 a[52:55], v[180:183], v[60:63], a[52:55]
s_add_u32 m0, s80, 65536
v_mfma_f32_16x16x32_bf16 a[56:59], v[176:179], v[64:67], a[56:59]
v_mfma_f32_16x16x32_bf16 a[56:59], v[180:183], v[68:71], a[56:59]
buffer_load_dwordx4 v8, s[16:19], s81 offen offset:0 lds
v_mfma_f32_16x16x32_bf16 a[60:63], v[176:179], v[72:75], a[60:63]
ds_read_b64_tr_b16 v[98:99], v0 offset:768
v_mfma_f32_16x16x32_bf16 a[60:63], v[180:183], v[76:79], a[60:63]
ds_read_b128 v[176:179], v12 offset:4096
v_mfma_f32_16x16x32_bf16 a[64:67], v[16:19], v[144:147], a[64:67]
ds_read_b64_tr_b16 v[100:101], v0 offset:4608
v_mfma_f32_16x16x32_bf16 a[64:67], v[20:23], v[148:151], a[64:67]
ds_read_b128 v[180:183], v13 offset:4096
v_mfma_f32_16x16x32_bf16 a[68:71], v[24:27], v[144:147], a[68:71]
v_mfma_f32_16x16x32_bf16 a[68:71], v[28:31], v[148:151], a[68:71]
ds_read_b64_tr_b16 v[102:103], v0 offset:4864
v_add_u32 v0, 0x1c000, v0
v_mfma_f32_16x16x32_bf16 a[72:75], v[32:35], v[144:147], a[72:75]
buffer_load_dwordx4 v9, s[16:19], s81 offen offset:1024 lds
v_mfma_f32_16x16x32_bf16 a[72:75], v[36:39], v[148:151], a[72:75]
v_mfma_f32_16x16x32_bf16 a[76:79], v[40:43], v[144:147], a[76:79]
ds_read_b64_tr_b16 v[104:105], v3 offset:512
v_mfma_f32_16x16x32_bf16 a[76:79], v[44:47], v[148:151], a[76:79]
v_mfma_f32_16x16x32_bf16 a[80:83], v[144:147], v[48:51], a[80:83]
v_mfma_f32_16x16x32_bf16 a[80:83], v[148:151], v[52:55], a[80:83]
ds_read_b64_tr_b16 v[106:107], v3 offset:768
v_mfma_f32_16x16x32_bf16 a[84:87], v[144:147], v[56:59], a[84:87]
v_mfma_f32_16x16x32_bf16 a[84:87], v[148:151], v[60:63], a[84:87]
buffer_load_dwordx4 v10, s[16:19], s81 offen offset:2048 lds
v_mfma_f32_16x16x32_bf16 a[88:91], v[144:147], v[64:67], a[88:91]
ds_read_b64_tr_b16 v[108:109], v3 offset:4608
v_mfma_f32_16x16x32_bf16 a[88:91], v[148:151], v[68:71], a[88:91]
v_mfma_f32_16x16x32_bf16 a[92:95], v[144:147], v[72:75], a[92:95]
ds_read_b128 v[144:147], v12 offset:6144
v_mfma_f32_16x16x32_bf16 a[92:95], v[148:151], v[76:79], a[92:95]
ds_read_b64_tr_b16 v[110:111], v3 offset:4864
v_add_u32 v3, 0x1c000, v3
v_mfma_f32_16x16x32_bf16 a[96:99], v[16:19], v[152:155], a[96:99]
ds_read_b128 v[148:151], v13 offset:6144
v_mfma_f32_16x16x32_bf16 a[96:99], v[20:23], v[156:159], a[96:99]
v_mfma_f32_16x16x32_bf16 a[100:103], v[24:27], v[152:155], a[100:103]
buffer_load_dwordx4 v11, s[16:19], s81 offen offset:3072 lds
v_mfma_f32_16x16x32_bf16 a[100:103], v[28:31], v[156:159], a[100:103]
v_mfma_f32_16x16x32_bf16 a[104:107], v[32:35], v[152:155], a[104:107]
v_mfma_f32_16x16x32_bf16 a[104:107], v[36:39], v[156:159], a[104:107]
v_mfma_f32_16x16x32_bf16 a[108:111], v[40:43], v[152:155], a[108:111]
v_mfma_f32_16x16x32_bf16 a[108:111], v[44:47], v[156:159], a[108:111]
v_mfma_f32_16x16x32_bf16 a[112:115], v[152:155], v[48:51], a[112:115]
v_mfma_f32_16x16x32_bf16 a[112:115], v[156:159], v[52:55], a[112:115]
v_mfma_f32_16x16x32_bf16 a[116:119], v[152:155], v[56:59], a[116:119]
v_mfma_f32_16x16x32_bf16 a[116:119], v[156:159], v[60:63], a[116:119]
v_mfma_f32_16x16x32_bf16 a[120:123], v[152:155], v[64:67], a[120:123]
s_add_u32 s81, s62, 5
s_sub_u32 s6, s81, 10
s_min_u32 s81, s81, s6
v_mfma_f32_16x16x32_bf16 a[120:123], v[156:159], v[68:71], a[120:123]
s_waitcnt vmcnt(25) lgkmcnt(0)
s_lshl_b32 s81, s81, 19
s_add_u32 s81, s81, s79
v_mfma_f32_16x16x32_bf16 a[124:127], v[152:155], v[72:75], a[124:127]
s_barrier
ds_read_b128 v[152:155], v12 offset:32768
v_mfma_f32_16x16x32_bf16 a[124:127], v[156:159], v[76:79], a[124:127]
s_add_u32 m0, s80, 81920
ds_read_b128 v[156:159], v13 offset:32768
v_mfma_f32_16x16x32_bf16 a[128:131], v[16:19], v[160:163], a[128:131]
v_mfma_f32_16x16x32_bf16 a[128:131], v[20:23], v[164:167], a[128:131]
buffer_load_dwordx4 v8, s[16:19], s81 offen offset:0 lds
v_mfma_f32_16x16x32_bf16 a[132:135], v[24:27], v[160:163], a[132:135]
ds_read_b64_tr_b16 v[112:113], v0 offset:0
v_mfma_f32_16x16x32_bf16 a[132:135], v[28:31], v[164:167], a[132:135]
v_mfma_f32_16x16x32_bf16 a[136:139], v[32:35], v[160:163], a[136:139]
v_mfma_f32_16x16x32_bf16 a[136:139], v[36:39], v[164:167], a[136:139]
ds_read_b64_tr_b16 v[114:115], v0 offset:256
v_mfma_f32_16x16x32_bf16 a[140:143], v[40:43], v[160:163], a[140:143]
v_mfma_f32_16x16x32_bf16 a[140:143], v[44:47], v[164:167], a[140:143]
v_mfma_f32_16x16x32_bf16 a[144:147], v[160:163], v[48:51], a[144:147]
buffer_load_dwordx4 v9, s[16:19], s81 offen offset:1024 lds
v_mfma_f32_16x16x32_bf16 a[144:147], v[164:167], v[52:55], a[144:147]
ds_read_b64_tr_b16 v[116:117], v0 offset:4096
v_mfma_f32_16x16x32_bf16 a[148:151], v[160:163], v[56:59], a[148:151]
v_mfma_f32_16x16x32_bf16 a[148:151], v[164:167], v[60:63], a[148:151]
ds_read_b64_tr_b16 v[118:119], v0 offset:4352
v_mfma_f32_16x16x32_bf16 a[152:155], v[160:163], v[64:67], a[152:155]
v_mfma_f32_16x16x32_bf16 a[152:155], v[164:167], v[68:71], a[152:155]
v_mfma_f32_16x16x32_bf16 a[156:159], v[160:163], v[72:75], a[156:159]
ds_read_b64_tr_b16 v[120:121], v3 offset:0
v_mfma_f32_16x16x32_bf16 a[156:159], v[164:167], v[76:79], a[156:159]
buffer_load_dwordx4 v10, s[16:19], s81 offen offset:2048 lds
v_mfma_f32_16x16x32_bf16 a[160:163], v[16:19], v[168:171], a[160:163]
ds_read_b128 v[160:163], v12 offset:34816
v_mfma_f32_16x16x32_bf16 a[160:163], v[20:23], v[172:175], a[160:163]
ds_read_b128 v[164:167], v13 offset:34816
v_mfma_f32_16x16x32_bf16 a[164:167], v[24:27], v[168:171], a[164:167]
ds_read_b64_tr_b16 v[122:123], v3 offset:256
v_mfma_f32_16x16x32_bf16 a[164:167], v[28:31], v[172:175], a[164:167]
v_mfma_f32_16x16x32_bf16 a[168:171], v[32:35], v[168:171], a[168:171]
ds_read_b64_tr_b16 v[124:125], v3 offset:4096
v_mfma_f32_16x16x32_bf16 a[168:171], v[36:39], v[172:175], a[168:171]
v_mfma_f32_16x16x32_bf16 a[172:175], v[40:43], v[168:171], a[172:175]
buffer_load_dwordx4 v11, s[16:19], s81 offen offset:3072 lds
v_mfma_f32_16x16x32_bf16 a[172:175], v[44:47], v[172:175], a[172:175]
ds_read_b64_tr_b16 v[126:127], v3 offset:4352
v_mfma_f32_16x16x32_bf16 a[176:179], v[168:171], v[48:51], a[176:179]
s_add_u32 s81, s62, 6
s_sub_u32 s6, s81, 10
s_min_u32 s81, s81, s6
v_mfma_f32_16x16x32_bf16 a[176:179], v[172:175], v[52:55], a[176:179]
s_lshl_b32 s81, s81, 19
s_add_u32 s81, s81, s79
v_mfma_f32_16x16x32_bf16 a[180:183], v[168:171], v[56:59], a[180:183]
ds_read_b64_tr_b16 v[128:129], v0 offset:512
v_mfma_f32_16x16x32_bf16 a[180:183], v[172:175], v[60:63], a[180:183]
s_add_u32 m0, s80, 16384
v_mfma_f32_16x16x32_bf16 a[184:187], v[168:171], v[64:67], a[184:187]
v_mfma_f32_16x16x32_bf16 a[184:187], v[172:175], v[68:71], a[184:187]
buffer_load_dwordx4 v8, s[16:19], s81 offen offset:0 lds
v_mfma_f32_16x16x32_bf16 a[188:191], v[168:171], v[72:75], a[188:191]
ds_read_b64_tr_b16 v[130:131], v0 offset:768
v_mfma_f32_16x16x32_bf16 a[188:191], v[172:175], v[76:79], a[188:191]
ds_read_b128 v[168:171], v12 offset:36864
v_mfma_f32_16x16x32_bf16 a[192:195], v[16:19], v[176:179], a[192:195]
ds_read_b64_tr_b16 v[132:133], v0 offset:4608
v_mfma_f32_16x16x32_bf16 a[192:195], v[20:23], v[180:183], a[192:195]
ds_read_b128 v[172:175], v13 offset:36864
v_mfma_f32_16x16x32_bf16 a[196:199], v[24:27], v[176:179], a[196:199]
v_mfma_f32_16x16x32_bf16 a[196:199], v[28:31], v[180:183], a[196:199]
ds_read_b64_tr_b16 v[134:135], v0 offset:4864
v_subrev_u32 v0, s92, v0
v_mfma_f32_16x16x32_bf16 a[200:203], v[32:35], v[176:179], a[200:203]
buffer_load_dwordx4 v9, s[16:19], s81 offen offset:1024 lds
v_mfma_f32_16x16x32_bf16 a[200:203], v[36:39], v[180:183], a[200:203]
v_add_u32 v0, 0xffff8000, v0
v_mfma_f32_16x16x32_bf16 a[204:207], v[40:43], v[176:179], a[204:207]
ds_read_b64_tr_b16 v[136:137], v3 offset:512
v_mfma_f32_16x16x32_bf16 a[204:207], v[44:47], v[180:183], a[204:207]
v_mfma_f32_16x16x32_bf16 a[208:211], v[176:179], v[48:51], a[208:211]
v_mfma_f32_16x16x32_bf16 a[208:211], v[180:183], v[52:55], a[208:211]
ds_read_b64_tr_b16 v[138:139], v3 offset:768
v_mfma_f32_16x16x32_bf16 a[212:215], v[176:179], v[56:59], a[212:215]
v_mfma_f32_16x16x32_bf16 a[212:215], v[180:183], v[60:63], a[212:215]
buffer_load_dwordx4 v10, s[16:19], s81 offen offset:2048 lds
v_mfma_f32_16x16x32_bf16 a[216:219], v[176:179], v[64:67], a[216:219]
ds_read_b64_tr_b16 v[140:141], v3 offset:4608
v_mfma_f32_16x16x32_bf16 a[216:219], v[180:183], v[68:71], a[216:219]
v_mfma_f32_16x16x32_bf16 a[220:223], v[176:179], v[72:75], a[220:223]
ds_read_b128 v[176:179], v12 offset:38912
v_add_u32 v12, 0x10000, v12
v_mfma_f32_16x16x32_bf16 a[220:223], v[180:183], v[76:79], a[220:223]
ds_read_b64_tr_b16 v[142:143], v3 offset:4864
v_subrev_u32 v3, s92, v3
v_mfma_f32_16x16x32_bf16 a[224:227], v[16:19], v[144:147], a[224:227]
s_and_b32 s92, s100, 1
ds_read_b128 v[180:183], v13 offset:38912
v_add_u32 v13, 0x10000, v13
v_mfma_f32_16x16x32_bf16 a[224:227], v[20:23], v[148:151], a[224:227]
s_mul_i32 s84, s92, 640
s_mul_i32 s92, s92, 20
v_add_u32 v3, 0xffff8000, v3
v_mfma_f32_16x16x32_bf16 a[228:231], v[24:27], v[144:147], a[228:231]
s_waitcnt vmcnt(15)
v_cmp_ne_u64 vcc, s[64:65], v[184:185]
s_nop 0
s_cbranch_vccnz L_stub_h
L_ret_h:
v_mfma_f32_16x16x32_bf16 a[228:231], v[28:31], v[148:151], a[228:231]
buffer_load_dwordx4 v11, s[16:19], s81 offen offset:3072 lds
v_mfma_f32_16x16x32_bf16 a[232:235], v[32:35], v[144:147], a[232:235]
s_lshr_b32 s100, s100, 1
v_mfma_f32_16x16x32_bf16 a[232:235], v[36:39], v[148:151], a[232:235]
v_mfma_f32_16x16x32_bf16 a[236:239], v[40:43], v[144:147], a[236:239]
v_mfma_f32_16x16x32_bf16 a[236:239], v[44:47], v[148:151], a[236:239]
v_mfma_f32_16x16x32_bf16 a[240:243], v[144:147], v[48:51], a[240:243]
v_mfma_f32_16x16x32_bf16 a[240:243], v[148:151], v[52:55], a[240:243]
v_mfma_f32_16x16x32_bf16 a[244:247], v[144:147], v[56:59], a[244:247]
v_mfma_f32_16x16x32_bf16 a[244:247], v[148:151], v[60:63], a[244:247]
v_mfma_f32_16x16x32_bf16 a[248:251], v[144:147], v[64:67], a[248:251]
s_add_u32 s81, s62, 7
s_sub_u32 s6, s81, 10
s_min_u32 s81, s81, s6
v_mfma_f32_16x16x32_bf16 a[248:251], v[148:151], v[68:71], a[248:251]
s_waitcnt lgkmcnt(0)
s_lshl_b32 s81, s81, 19
s_add_u32 s81, s81, s79
v_mfma_f32_16x16x32_bf16 a[252:255], v[144:147], v[72:75], a[252:255]
s_barrier
ds_read_b128 v[144:147], v12 offset:0
v_mfma_f32_16x16x32_bf16 a[252:255], v[148:151], v[76:79], a[252:255]
s_add_u32 m0, s80, 114688
ds_read_b128 v[148:151], v13 offset:0
v_mfma_f32_16x16x32_bf16 a[0:3], v[80:83], v[152:155], a[0:3]
v_mfma_f32_16x16x32_bf16 a[0:3], v[84:87], v[156:159], a[0:3]
buffer_load_dwordx4 v8, s[16:19], s81 offen offset:0 lds
v_mfma_f32_16x16x32_bf16 a[4:7], v[88:91], v[152:155], a[4:7]
v_mfma_f32_16x16x32_bf16 a[4:7], v[92:95], v[156:159], a[4:7]
v_mfma_f32_16x16x32_bf16 a[8:11], v[96:99], v[152:155], a[8:11]
v_mfma_f32_16x16x32_bf16 a[8:11], v[100:103], v[156:159], a[8:11]
buffer_load_dwordx4 v[16:19], v4, s[52:55], s86 offen offset:0 sc1
v_mfma_f32_16x16x32_bf16 a[12:15], v[104:107], v[152:155], a[12:15]
s_nop 0
v_accvgpr_read_b32 v208, a0
v_accvgpr_read_b32 v209, a1
v_mfma_f32_16x16x32_bf16 a[12:15], v[108:111], v[156:159], a[12:15]
v_accvgpr_read_b32 v210, a2
v_accvgpr_read_b32 v211, a3
v_mfma_f32_16x16x32_bf16 a[16:19], v[152:155], v[112:115], a[16:19]
v_accvgpr_read_b32 v212, a4
v_accvgpr_read_b32 v213, a5
v_mfma_f32_16x16x32_bf16 a[16:19], v[156:159], v[116:119], a[16:19]
buffer_load_dwordx4 v9, s[16:19], s81 offen offset:1024 lds
v_mfma_f32_16x16x32_bf16 a[20:23], v[152:155], v[120:123], a[20:23]
v_accvgpr_read_b32 v214, a6
v_accvgpr_read_b32 v215, a7
v_mfma_f32_16x16x32_bf16 a[20:23], v[156:159], v[124:127], a[20:23]
v_cvt_pk_bf16_f32 v216, v208, v209
v_cvt_pk_bf16_f32 v217, v210, v211
v_mfma_f32_16x16x32_bf16 a[24:27], v[152:155], v[128:131], a[24:27]
v_cvt_pk_bf16_f32 v218, v212, v213
v_cvt_pk_bf16_f32 v219, v214, v215
v_mfma_f32_16x16x32_bf16 a[24:27], v[156:159], v[132:135], a[24:27]
buffer_load_dwordx4 v[20:23], v4, s[52:55], s86 offen offset:1024 sc1
v_mfma_f32_16x16x32_bf16 a[28:31], v[152:155], v[136:139], a[28:31]
ds_read_b128 v[152:155], v12 offset:2048
v_accvgpr_read_b32 v208, a8
v_mfma_f32_16x16x32_bf16 a[28:31], v[156:159], v[140:143], a[28:31]
ds_read_b128 v[156:159], v13 offset:2048
v_accvgpr_read_b32 v209, a9
v_mfma_f32_16x16x32_bf16 a[32:35], v[80:83], v[160:163], a[32:35]
v_accvgpr_read_b32 v210, a10
v_accvgpr_read_b32 v211, a11
v_mfma_f32_16x16x32_bf16 a[32:35], v[84:87], v[164:167], a[32:35]
buffer_load_dwordx4 v10, s[16:19], s81 offen offset:2048 lds
v_mfma_f32_16x16x32_bf16 a[36:39], v[88:91], v[160:163], a[36:39]
v_accvgpr_read_b32 v212, a12
v_accvgpr_read_b32 v213, a13
v_mfma_f32_16x16x32_bf16 a[36:39], v[92:95], v[164:167], a[36:39]
v_accvgpr_read_b32 v214, a14
v_accvgpr_read_b32 v215, a15
v_mfma_f32_16x16x32_bf16 a[40:43], v[96:99], v[160:163], a[40:43]
v_cvt_pk_bf16_f32 v220, v208, v209
v_cvt_pk_bf16_f32 v221, v210, v211
v_mfma_f32_16x16x32_bf16 a[40:43], v[100:103], v[164:167], a[40:43]
buffer_load_dwordx4 v[24:27], v4, s[52:55], s87 offen offset:0 sc1
v_mfma_f32_16x16x32_bf16 a[44:47], v[104:107], v[160:163], a[44:47]
v_cvt_pk_bf16_f32 v222, v212, v213
v_cvt_pk_bf16_f32 v223, v214, v215
v_mfma_f32_16x16x32_bf16 a[44:47], v[108:111], v[164:167], a[44:47]
v_accvgpr_read_b32 v208, a32
v_accvgpr_read_b32 v209, a33
v_mfma_f32_16x16x32_bf16 a[48:51], v[160:163], v[112:115], a[48:51]
v_accvgpr_read_b32 v210, a34
v_accvgpr_read_b32 v211, a35
v_mfma_f32_16x16x32_bf16 a[48:51], v[164:167], v[116:119], a[48:51]
buffer_load_dwordx4 v11, s[16:19], s81 offen offset:3072 lds
v_mfma_f32_16x16x32_bf16 a[52:55], v[160:163], v[120:123], a[52:55]
s_add_u32 s81, s62, 8
s_sub_u32 s6, s81, 10
s_min_u32 s81, s81, s6
v_mfma_f32_16x16x32_bf16 a[52:55], v[164:167], v[124:127], a[52:55]
s_add_u32 m0, s80, 131072
s_lshl_b32 s81, s81, 19
s_add_u32 s81, s81, s79
v_mfma_f32_16x16x32_bf16 a[56:59], v[160:163], v[128:131], a[56:59]
v_accvgpr_read_b32 v212, a36
v_accvgpr_read_b32 v213, a37
v_mfma_f32_16x16x32_bf16 a[56:59], v[164:167], v[132:135], a[56:59]
buffer_load_dwordx4 v[28:31], v4, s[52:55], s87 offen offset:1024 sc1
v_mfma_f32_16x16x32_bf16 a[60:63], v[160:163], v[136:139], a[60:63]
)ASM"
R"ASM(
ds_read_b128 v[160:163], v12 offset:4096
v_accvgpr_read_b32 v214, a38
v_mfma_f32_16x16x32_bf16 a[60:63], v[164:167], v[140:143], a[60:63]
ds_read_b128 v[164:167], v13 offset:4096
v_accvgpr_read_b32 v215, a39
v_mfma_f32_16x16x32_bf16 a[64:67], v[80:83], v[168:171], a[64:67]
v_cvt_pk_bf16_f32 v224, v208, v209
v_cvt_pk_bf16_f32 v225, v210, v211
v_mfma_f32_16x16x32_bf16 a[64:67], v[84:87], v[172:175], a[64:67]
buffer_load_dwordx4 v8, s[16:19], s81 offen offset:0 lds
v_mfma_f32_16x16x32_bf16 a[68:71], v[88:91], v[168:171], a[68:71]
v_cvt_pk_bf16_f32 v226, v212, v213
v_cvt_pk_bf16_f32 v227, v214, v215
v_mfma_f32_16x16x32_bf16 a[68:71], v[92:95], v[172:175], a[68:71]
v_accvgpr_read_b32 v208, a40
v_accvgpr_read_b32 v209, a41
v_mfma_f32_16x16x32_bf16 a[72:75], v[96:99], v[168:171], a[72:75]
v_accvgpr_read_b32 v210, a42
v_accvgpr_read_b32 v211, a43
v_mfma_f32_16x16x32_bf16 a[72:75], v[100:103], v[172:175], a[72:75]
buffer_load_dwordx4 v[32:35], v4, s[52:55], s88 offen offset:0 sc1
v_mfma_f32_16x16x32_bf16 a[76:79], v[104:107], v[168:171], a[76:79]
v_accvgpr_read_b32 v212, a44
v_accvgpr_read_b32 v213, a45
v_mfma_f32_16x16x32_bf16 a[76:79], v[108:111], v[172:175], a[76:79]
v_accvgpr_read_b32 v214, a46
v_accvgpr_read_b32 v215, a47
v_mfma_f32_16x16x32_bf16 a[80:83], v[168:171], v[112:115], a[80:83]
v_cvt_pk_bf16_f32 v228, v208, v209
v_cvt_pk_bf16_f32 v229, v210, v211
v_mfma_f32_16x16x32_bf16 a[80:83], v[172:175], v[116:119], a[80:83]
buffer_load_dwordx4 v9, s[16:19], s81 offen offset:1024 lds
v_mfma_f32_16x16x32_bf16 a[84:87], v[168:171], v[120:123], a[84:87]
v_cvt_pk_bf16_f32 v230, v212, v213
v_cvt_pk_bf16_f32 v231, v214, v215
v_mfma_f32_16x16x32_bf16 a[84:87], v[172:175], v[124:127], a[84:87]
v_accvgpr_read_b32 v208, a16
v_accvgpr_read_b32 v209, a17
v_mfma_f32_16x16x32_bf16 a[88:91], v[168:171], v[128:131], a[88:91]
v_accvgpr_read_b32 v210, a18
v_accvgpr_read_b32 v211, a19
v_mfma_f32_16x16x32_bf16 a[88:91], v[172:175], v[132:135], a[88:91]
buffer_store_dwordx4 v[216:219], v1, s[52:55], s0 offen offset:0 sc1
v_mfma_f32_16x16x32_bf16 a[92:95], v[168:171], v[136:139], a[92:95]
ds_read_b128 v[168:171], v12 offset:6144
v_accvgpr_read_b32 v212, a48
v_mfma_f32_16x16x32_bf16 a[92:95], v[172:175], v[140:143], a[92:95]
ds_read_b128 v[172:175], v13 offset:6144
v_accvgpr_read_b32 v213, a49
v_mfma_f32_16x16x32_bf16 a[96:99], v[80:83], v[176:179], a[96:99]
v_accvgpr_read_b32 v214, a50
v_accvgpr_read_b32 v215, a51
v_mfma_f32_16x16x32_bf16 a[96:99], v[84:87], v[180:183], a[96:99]
v_cvt_pk_bf16_f32 v216, v208, v209
v_cvt_pk_bf16_f32 v217, v210, v211
v_mfma_f32_16x16x32_bf16 a[100:103], v[88:91], v[176:179], a[100:103]
v_cvt_pk_bf16_f32 v218, v212, v213
v_cvt_pk_bf16_f32 v219, v214, v215
v_mfma_f32_16x16x32_bf16 a[100:103], v[92:95], v[180:183], a[100:103]
v_accvgpr_read_b32 v208, a20
v_accvgpr_read_b32 v209, a21
v_mfma_f32_16x16x32_bf16 a[104:107], v[96:99], v[176:179], a[104:107]
buffer_load_dwordx4 v[36:39], v4, s[52:55], s88 offen offset:1024 sc1
v_mfma_f32_16x16x32_bf16 a[104:107], v[100:103], v[180:183], a[104:107]
v_accvgpr_read_b32 v210, a22
v_accvgpr_read_b32 v211, a23
v_mfma_f32_16x16x32_bf16 a[108:111], v[104:107], v[176:179], a[108:111]
v_accvgpr_read_b32 v212, a52
v_accvgpr_read_b32 v213, a53
v_mfma_f32_16x16x32_bf16 a[108:111], v[108:111], v[180:183], a[108:111]
v_accvgpr_read_b32 v214, a54
v_accvgpr_read_b32 v215, a55
v_mfma_f32_16x16x32_bf16 a[112:115], v[176:179], v[112:115], a[112:115]
buffer_load_dwordx4 v10, s[16:19], s81 offen offset:2048 lds
v_mfma_f32_16x16x32_bf16 a[112:115], v[180:183], v[116:119], a[112:115]
v_mov_b32 v13, 0
v_add_u32 v12, 98304, v1
v_mfma_f32_16x16x32_bf16 a[116:119], v[176:179], v[120:123], a[116:119]
v_mfma_f32_16x16x32_bf16 a[116:119], v[180:183], v[124:127], a[116:119]
v_mfma_f32_16x16x32_bf16 a[120:123], v[176:179], v[128:131], a[120:123]
buffer_load_dwordx4 v[40:43], v4, s[52:55], s89 offen offset:0 sc1
v_mfma_f32_16x16x32_bf16 a[120:123], v[180:183], v[132:135], a[120:123]
buffer_load_dwordx4 v11, s[16:19], s81 offen offset:3072 lds
v_mfma_f32_16x16x32_bf16 a[124:127], v[176:179], v[136:139], a[124:127]
s_waitcnt lgkmcnt(0)
s_barrier
s_add_u32 s81, s62, 9
s_sub_u32 s6, s81, 10
v_mfma_f32_16x16x32_bf16 a[124:127], v[180:183], v[140:143], a[124:127]
s_add_u32 m0, s80, 147456
s_min_u32 s81, s81, s6
s_lshl_b32 s81, s81, 19
v_mfma_f32_16x16x32_bf16 a[128:131], v[80:83], v[144:147], a[128:131]
s_add_u32 s81, s81, s79
v_mfma_f32_16x16x32_bf16 a[128:131], v[84:87], v[148:151], a[128:131]
buffer_load_dwordx4 v[44:47], v4, s[52:55], s89 offen offset:1024 sc1
v_mfma_f32_16x16x32_bf16 a[132:135], v[88:91], v[144:147], a[132:135]
v_add_u32 v4, s94, v1
v_mfma_f32_16x16x32_bf16 a[132:135], v[92:95], v[148:151], a[132:135]
s_add_u32 s94, s62, 2
s_sub_u32 s5, s94, 10
s_min_u32 s94, s94, s5
v_mfma_f32_16x16x32_bf16 a[136:139], v[96:99], v[144:147], a[136:139]
s_lshl_b32 s94, s94, 11
v_mfma_f32_16x16x32_bf16 a[136:139], v[100:103], v[148:151], a[136:139]
buffer_load_dwordx4 v[48:51], v4, s[52:55], s86 offen offset:0 sc1
v_mfma_f32_16x16x32_bf16 a[140:143], v[104:107], v[144:147], a[140:143]
v_mfma_f32_16x16x32_bf16 a[140:143], v[108:111], v[148:151], a[140:143]
v_mfma_f32_16x16x32_bf16 a[144:147], v[144:147], v[112:115], a[144:147]
v_mfma_f32_16x16x32_bf16 a[144:147], v[148:151], v[116:119], a[144:147]
buffer_store_dwordx4 v[220:223], v1, s[52:55], s0 offen offset:1024 sc1
v_mfma_f32_16x16x32_bf16 a[148:151], v[144:147], v[120:123], a[148:151]
s_add_u32 s0, s4, 303104
v_cvt_pk_bf16_f32 v220, v208, v209
v_cvt_pk_bf16_f32 v221, v210, v211
v_mfma_f32_16x16x32_bf16 a[148:151], v[148:151], v[124:127], a[148:151]
v_cvt_pk_bf16_f32 v222, v212, v213
v_cvt_pk_bf16_f32 v223, v214, v215
v_mfma_f32_16x16x32_bf16 a[152:155], v[144:147], v[128:131], a[152:155]
v_accvgpr_read_b32 v208, a24
v_accvgpr_read_b32 v209, a25
v_mfma_f32_16x16x32_bf16 a[152:155], v[148:151], v[132:135], a[152:155]
v_accvgpr_read_b32 v210, a26
v_accvgpr_read_b32 v211, a27
v_mfma_f32_16x16x32_bf16 a[156:159], v[144:147], v[136:139], a[156:159]
v_accvgpr_read_b32 v212, a56
v_accvgpr_read_b32 v213, a57
v_mfma_f32_16x16x32_bf16 a[156:159], v[148:151], v[140:143], a[156:159]
v_accvgpr_read_b32 v214, a58
v_accvgpr_read_b32 v215, a59
v_mfma_f32_16x16x32_bf16 a[160:163], v[80:83], v[152:155], a[160:163]
buffer_load_dwordx4 v[52:55], v4, s[52:55], s86 offen offset:1024 sc1
v_mfma_f32_16x16x32_bf16 a[160:163], v[84:87], v[156:159], a[160:163]
v_mfma_f32_16x16x32_bf16 a[164:167], v[88:91], v[152:155], a[164:167]
v_mfma_f32_16x16x32_bf16 a[164:167], v[92:95], v[156:159], a[164:167]
v_mfma_f32_16x16x32_bf16 a[168:171], v[96:99], v[152:155], a[168:171]
buffer_load_dwordx4 v8, s[16:19], s81 offen offset:0 lds
v_mfma_f32_16x16x32_bf16 a[168:171], v[100:103], v[156:159], a[168:171]
v_mfma_f32_16x16x32_bf16 a[172:175], v[104:107], v[152:155], a[172:175]
v_mfma_f32_16x16x32_bf16 a[172:175], v[108:111], v[156:159], a[172:175]
v_mfma_f32_16x16x32_bf16 a[176:179], v[152:155], v[112:115], a[176:179]
buffer_load_dwordx4 v[56:59], v4, s[52:55], s87 offen offset:0 sc1
v_mfma_f32_16x16x32_bf16 a[176:179], v[156:159], v[116:119], a[176:179]
v_mfma_f32_16x16x32_bf16 a[180:183], v[152:155], v[120:123], a[180:183]
v_mfma_f32_16x16x32_bf16 a[180:183], v[156:159], v[124:127], a[180:183]
v_mfma_f32_16x16x32_bf16 a[184:187], v[152:155], v[128:131], a[184:187]
buffer_load_dwordx4 v[60:63], v4, s[52:55], s87 offen offset:1024 sc1
v_mfma_f32_16x16x32_bf16 a[184:187], v[156:159], v[132:135], a[184:187]
v_mfma_f32_16x16x32_bf16 a[188:191], v[152:155], v[136:139], a[188:191]
v_mfma_f32_16x16x32_bf16 a[188:191], v[156:159], v[140:143], a[188:191]
v_mfma_f32_16x16x32_bf16 a[192:195], v[80:83], v[160:163], a[192:195]
buffer_load_dwordx4 v9, s[16:19], s81 offen offset:1024 lds
v_mfma_f32_16x16x32_bf16 a[192:195], v[84:87], v[164:167], a[192:195]
v_mfma_f32_16x16x32_bf16 a[196:199], v[88:91], v[160:163], a[196:199]
v_mfma_f32_16x16x32_bf16 a[196:199], v[92:95], v[164:167], a[196:199]
v_mfma_f32_16x16x32_bf16 a[200:203], v[96:99], v[160:163], a[200:203]
buffer_store_dwordx4 v[224:227], v1, s[52:55], s1 offen offset:0 sc1
v_mfma_f32_16x16x32_bf16 a[200:203], v[100:103], v[164:167], a[200:203]
s_nop 0
v_cvt_pk_bf16_f32 v224, v208, v209
v_cvt_pk_bf16_f32 v225, v210, v211
v_mfma_f32_16x16x32_bf16 a[204:207], v[104:107], v[160:163], a[204:207]
v_accvgpr_read_b32 v208, a28
v_accvgpr_read_b32 v209, a29
v_mfma_f32_16x16x32_bf16 a[204:207], v[108:111], v[164:167], a[204:207]
v_accvgpr_read_b32 v210, a30
v_accvgpr_read_b32 v211, a31
v_mfma_f32_16x16x32_bf16 a[208:211], v[160:163], v[112:115], a[208:211]
v_cvt_pk_bf16_f32 v226, v212, v213
v_cvt_pk_bf16_f32 v227, v214, v215
v_mfma_f32_16x16x32_bf16 a[208:211], v[164:167], v[116:119], a[208:211]
v_accvgpr_read_b32 v212, a60
v_accvgpr_read_b32 v213, a61
v_mfma_f32_16x16x32_bf16 a[212:215], v[160:163], v[120:123], a[212:215]
v_accvgpr_read_b32 v214, a62
v_accvgpr_read_b32 v215, a63
v_mfma_f32_16x16x32_bf16 a[212:215], v[164:167], v[124:127], a[212:215]
buffer_load_dwordx4 v[64:67], v4, s[52:55], s88 offen offset:0 sc1
v_mfma_f32_16x16x32_bf16 a[216:219], v[160:163], v[128:131], a[216:219]
v_mfma_f32_16x16x32_bf16 a[216:219], v[164:167], v[132:135], a[216:219]
v_mfma_f32_16x16x32_bf16 a[220:223], v[160:163], v[136:139], a[220:223]
v_mfma_f32_16x16x32_bf16 a[220:223], v[164:167], v[140:143], a[220:223]
buffer_load_dwordx4 v[68:71], v4, s[52:55], s88 offen offset:1024 sc1
v_mfma_f32_16x16x32_bf16 a[224:227], v[80:83], v[168:171], a[224:227]
v_mfma_f32_16x16x32_bf16 a[224:227], v[84:87], v[172:175], a[224:227]
v_mfma_f32_16x16x32_bf16 a[228:231], v[88:91], v[168:171], a[228:231]
v_mfma_f32_16x16x32_bf16 a[228:231], v[92:95], v[172:175], a[228:231]
buffer_load_dwordx4 v10, s[16:19], s81 offen offset:2048 lds
buffer_load_dwordx4 v11, s[16:19], s81 offen offset:3072 lds
v_mfma_f32_16x16x32_bf16 a[232:235], v[96:99], v[168:171], a[232:235]
s_barrier
ds_read_b64_tr_b16 v[232:233], v0 offset:0
v_mov_b32 v10, s64
v_mfma_f32_16x16x32_bf16 a[232:235], v[100:103], v[172:175], a[232:235]
ds_read_b64_tr_b16 v[234:235], v0 offset:256
v_mov_b32 v11, s65
v_mfma_f32_16x16x32_bf16 a[236:239], v[104:107], v[168:171], a[236:239]
ds_read_b64_tr_b16 v[236:237], v0 offset:4096
v_mfma_f32_16x16x32_bf16 a[236:239], v[108:111], v[172:175], a[236:239]
buffer_load_dwordx4 v[72:75], v4, s[52:55], s89 offen offset:0 sc1
v_mfma_f32_16x16x32_bf16 a[240:243], v[168:171], v[112:115], a[240:243]
ds_read_b64_tr_b16 v[238:239], v0 offset:4352
v_mfma_f32_16x16x32_bf16 a[240:243], v[172:175], v[116:119], a[240:243]
ds_read_b64_tr_b16 v[240:241], v3 offset:0
v_mfma_f32_16x16x32_bf16 a[244:247], v[168:171], v[120:123], a[244:247]
ds_read_b64_tr_b16 v[242:243], v3 offset:256
v_mfma_f32_16x16x32_bf16 a[244:247], v[172:175], v[124:127], a[244:247]
buffer_store_dwordx4 v[228:231], v1, s[52:55], s1 offen offset:1024 sc1
v_mfma_f32_16x16x32_bf16 a[248:251], v[168:171], v[128:131], a[248:251]
s_add_u32 s1, s4, 307200
ds_read_b64_tr_b16 v[244:245], v3 offset:4096
v_cvt_pk_bf16_f32 v228, v208, v209
v_mfma_f32_16x16x32_bf16 a[248:251], v[172:175], v[132:135], a[248:251]
ds_read_b64_tr_b16 v[246:247], v3 offset:4352
v_cvt_pk_bf16_f32 v229, v210, v211
v_mfma_f32_16x16x32_bf16 a[252:255], v[168:171], v[136:139], a[252:255]
ds_read_b64_tr_b16 v[248:249], v0 offset:512
v_cvt_pk_bf16_f32 v230, v212, v213
v_mfma_f32_16x16x32_bf16 a[252:255], v[172:175], v[140:143], a[252:255]
s_waitcnt vmcnt(29) lgkmcnt(3)
ds_read_b64_tr_b16 v[250:251], v0 offset:768
v_cvt_pk_bf16_f32 v231, v214, v215
v_mfma_f32_16x16x32_bf16 v[192:195], v[232:235], v[16:19], 0
s_waitcnt vmcnt(27)
ds_read_b64_tr_b16 v[252:253], v0 offset:4608
v_accvgpr_read_b32 v208, a192
v_mfma_f32_16x16x32_bf16 v[192:195], v[236:239], v[20:23], v[192:195]
s_waitcnt vmcnt(23)
ds_read_b64_tr_b16 v[254:255], v0 offset:4864
v_accvgpr_read_b32 v209, a193
v_mfma_f32_16x16x32_bf16 v[196:199], v[232:235], v[24:27], 0
buffer_load_dwordx4 v[76:79], v4, s[52:55], s89 offen offset:1024 sc1
v_mfma_f32_16x16x32_bf16 v[196:199], v[236:239], v[28:31], v[196:199]
s_waitcnt vmcnt(17)
v_add_u32 v4, s94, v1
v_accvgpr_read_b32 v210, a194
v_mfma_f32_16x16x32_bf16 v[200:203], v[232:235], v[32:35], 0
s_add_u32 s94, s62, 3
s_sub_u32 s5, s94, 10
s_min_u32 s94, s94, s5
v_mfma_f32_16x16x32_bf16 v[200:203], v[236:239], v[36:39], v[200:203]
s_lshl_b32 s94, s94, 11
v_accvgpr_read_b32 v211, a195
v_cvt_pk_bf16_f32 v208, v208, v209
L_pad_a_0_1:
s_cbranch_execz L_pad_a_0_2
v_mfma_f32_16x16x32_bf16 v[204:207], v[232:235], v[40:43], 0
s_waitcnt vmcnt(15)
buffer_load_dwordx4 a[0:3], v4, s[52:55], s86 offen offset:0 sc1
v_mfma_f32_16x16x32_bf16 v[204:207], v[236:239], v[44:47], v[204:207]
ds_read_b64_tr_b16 v[232:233], v3 offset:512
v_accvgpr_read_b32 v212, a196
v_mfma_f32_16x16x32_bf16 v[80:83], v[240:243], v[16:19], 0
s_waitcnt lgkmcnt(1)
ds_read_b64_tr_b16 v[234:235], v3 offset:768
v_accvgpr_read_b32 v213, a197
v_mfma_f32_16x16x32_bf16 v[80:83], v[244:247], v[20:23], v[80:83]
ds_read_b64_tr_b16 v[236:237], v3 offset:4608
v_accvgpr_read_b32 v214, a198
v_mfma_f32_16x16x32_bf16 v[84:87], v[240:243], v[24:27], 0
buffer_load_dwordx4 a[4:7], v4, s[52:55], s86 offen offset:1024 sc1
v_mfma_f32_16x16x32_bf16 v[84:87], v[244:247], v[28:31], v[84:87]
ds_read_b64_tr_b16 v[238:239], v3 offset:4864
v_accvgpr_read_b32 v215, a199
v_mfma_f32_16x16x32_bf16 v[88:91], v[240:243], v[32:35], 0
v_cvt_pk_bf16_f32 v209, v210, v211
v_cvt_pk_bf16_f32 v210, v212, v213
v_mfma_f32_16x16x32_bf16 v[88:91], v[244:247], v[36:39], v[88:91]
v_cvt_pk_bf16_f32 v211, v214, v215
v_accvgpr_write_b32 a192, v208
v_mfma_f32_16x16x32_bf16 v[92:95], v[240:243], v[40:43], 0
buffer_store_dwordx4 v[216:219], v1, s[52:55], s2 offen offset:0 sc1
v_mfma_f32_16x16x32_bf16 v[92:95], v[244:247], v[44:47], v[92:95]
ds_read_b64_tr_b16 v[240:241], v0 offset:8192
v_accvgpr_write_b32 a193, v209
v_mfma_f32_16x16x32_bf16 v[96:99], v[248:251], v[16:19], 0
ds_read_b64_tr_b16 v[242:243], v0 offset:8448
v_accvgpr_write_b32 a194, v210
v_mfma_f32_16x16x32_bf16 v[96:99], v[252:255], v[20:23], v[96:99]
ds_read_b64_tr_b16 v[244:245], v0 offset:12288
v_accvgpr_write_b32 a195, v211
v_mfma_f32_16x16x32_bf16 v[100:103], v[248:251], v[24:27], 0
ds_read_b64_tr_b16 v[246:247], v0 offset:12544
v_accvgpr_read_b32 v208, a64
v_mfma_f32_16x16x32_bf16 v[100:103], v[252:255], v[28:31], v[100:103]
v_accvgpr_read_b32 v209, a65
v_accvgpr_read_b32 v210, a66
v_mfma_f32_16x16x32_bf16 v[104:107], v[248:251], v[32:35], 0
v_accvgpr_read_b32 v211, a67
v_accvgpr_read_b32 v212, a68
v_mfma_f32_16x16x32_bf16 v[104:107], v[252:255], v[36:39], v[104:107]
buffer_load_dwordx4 a[8:11], v4, s[52:55], s87 offen offset:0 sc1
v_mfma_f32_16x16x32_bf16 v[108:111], v[248:251], v[40:43], 0
ds_read_b64_tr_b16 v[248:249], v3 offset:8192
v_accvgpr_read_b32 v213, a69
v_mfma_f32_16x16x32_bf16 v[108:111], v[252:255], v[44:47], v[108:111]
s_waitcnt lgkmcnt(1)
ds_read_b64_tr_b16 v[250:251], v3 offset:8448
v_accvgpr_read_b32 v214, a70
v_mfma_f32_16x16x32_bf16 v[112:115], v[232:235], v[16:19], 0
ds_read_b64_tr_b16 v[252:253], v3 offset:12288
v_accvgpr_read_b32 v215, a71
v_mfma_f32_16x16x32_bf16 v[112:115], v[236:239], v[20:23], v[112:115]
buffer_load_dwordx4 a[12:15], v4, s[52:55], s87 offen offset:1024 sc1
v_mfma_f32_16x16x32_bf16 v[116:119], v[232:235], v[24:27], 0
ds_read_b64_tr_b16 v[254:255], v3 offset:12544
v_cvt_pk_bf16_f32 v216, v208, v209
v_mfma_f32_16x16x32_bf16 v[116:119], v[236:239], v[28:31], v[116:119]
v_cvt_pk_bf16_f32 v217, v210, v211
v_cvt_pk_bf16_f32 v218, v212, v213
v_mfma_f32_16x16x32_bf16 v[120:123], v[232:235], v[32:35], 0
v_cvt_pk_bf16_f32 v219, v214, v215
v_accvgpr_read_b32 v208, a200
v_mfma_f32_16x16x32_bf16 v[120:123], v[236:239], v[36:39], v[120:123]
buffer_load_dwordx4 a[16:19], v4, s[52:55], s88 offen offset:0 sc1
v_mfma_f32_16x16x32_bf16 v[124:127], v[232:235], v[40:43], 0
ds_read_b64_tr_b16 v[232:233], v0 offset:8704
v_accvgpr_read_b32 v209, a201
v_mfma_f32_16x16x32_bf16 v[124:127], v[236:239], v[44:47], v[124:127]
ds_read_b64_tr_b16 v[234:235], v0 offset:8960
v_accvgpr_read_b32 v210, a202
v_mfma_f32_16x16x32_bf16 v[128:131], v[240:243], v[16:19], 0
ds_read_b64_tr_b16 v[236:237], v0 offset:12800
v_accvgpr_read_b32 v211, a203
v_mfma_f32_16x16x32_bf16 v[128:131], v[244:247], v[20:23], v[128:131]
buffer_load_dwordx4 a[20:23], v4, s[52:55], s88 offen offset:1024 sc1
v_mfma_f32_16x16x32_bf16 v[132:135], v[240:243], v[24:27], 0
ds_read_b64_tr_b16 v[238:239], v0 offset:13056
v_add_u32 v0, 0xfffe8000, v0
v_mfma_f32_16x16x32_bf16 v[132:135], v[244:247], v[28:31], v[132:135]
v_accvgpr_read_b32 v212, a204
v_cvt_pk_bf16_f32 v208, v208, v209
v_mfma_f32_16x16x32_bf16 v[136:139], v[240:243], v[32:35], 0
v_accvgpr_read_b32 v213, a205
v_accvgpr_read_b32 v214, a206
v_mfma_f32_16x16x32_bf16 v[136:139], v[244:247], v[36:39], v[136:139]
buffer_store_dwordx4 v[220:223], v1, s[52:55], s2 offen offset:2048 sc1
v_mfma_f32_16x16x32_bf16 v[140:143], v[240:243], v[40:43], 0
s_add_u32 s2, s82, 303104
ds_read_b64_tr_b16 v[240:241], v3 offset:8704
v_accvgpr_read_b32 v215, a207
v_mfma_f32_16x16x32_bf16 v[140:143], v[244:247], v[44:47], v[140:143]
s_waitcnt lgkmcnt(1)
ds_read_b64_tr_b16 v[242:243], v3 offset:8960
v_cvt_pk_bf16_f32 v209, v210, v211
v_mfma_f32_16x16x32_bf16 v[144:147], v[248:251], v[16:19], 0
ds_read_b64_tr_b16 v[244:245], v3 offset:12800
v_cvt_pk_bf16_f32 v210, v212, v213
v_mfma_f32_16x16x32_bf16 v[144:147], v[252:255], v[20:23], v[144:147]
ds_read_b64_tr_b16 v[246:247], v3 offset:13056
v_add_u32 v3, 0xfffe8000, v3
v_mfma_f32_16x16x32_bf16 v[148:151], v[248:251], v[24:27], 0
v_cvt_pk_bf16_f32 v211, v214, v215
v_accvgpr_write_b32 a200, v208
v_mfma_f32_16x16x32_bf16 v[148:151], v[252:255], v[28:31], v[148:151]
v_accvgpr_write_b32 a201, v209
v_accvgpr_write_b32 a202, v210
v_mfma_f32_16x16x32_bf16 v[152:155], v[248:251], v[32:35], 0
buffer_load_dwordx4 a[24:27], v4, s[52:55], s89 offen offset:0 sc1
v_mfma_f32_16x16x32_bf16 v[152:155], v[252:255], v[36:39], v[152:155]
v_accvgpr_write_b32 a203, v211
v_accvgpr_read_b32 v208, a72
v_mfma_f32_16x16x32_bf16 v[156:159], v[248:251], v[40:43], 0
v_accvgpr_read_b32 v209, a73
v_accvgpr_read_b32 v210, a74
v_mfma_f32_16x16x32_bf16 v[156:159], v[252:255], v[44:47], v[156:159]
v_accvgpr_read_b32 v211, a75
v_accvgpr_read_b32 v212, a76
v_mfma_f32_16x16x32_bf16 v[160:163], v[232:235], v[16:19], 0
buffer_load_dwordx4 a[28:31], v4, s[52:55], s89 offen offset:1024 sc1
v_mfma_f32_16x16x32_bf16 v[160:163], v[236:239], v[20:23], v[160:163]
v_add_u32 v4, s94, v1
v_accvgpr_read_b32 v213, a77
v_mfma_f32_16x16x32_bf16 v[164:167], v[232:235], v[24:27], 0
s_add_u32 s94, s62, 4
s_sub_u32 s5, s94, 10
s_min_u32 s94, s94, s5
v_mfma_f32_16x16x32_bf16 v[164:167], v[236:239], v[28:31], v[164:167]
s_lshl_b32 s94, s94, 11
v_accvgpr_read_b32 v214, a78
v_accvgpr_read_b32 v215, a79
v_mfma_f32_16x16x32_bf16 v[168:171], v[232:235], v[32:35], 0
s_waitcnt lgkmcnt(0)
s_barrier
ds_read_b64_tr_b16 v[248:249], v0 offset:0
v_cvt_pk_bf16_f32 v220, v208, v209
v_mfma_f32_16x16x32_bf16 v[168:171], v[236:239], v[36:39], v[168:171]
buffer_load_dwordx4 a[32:35], v4, s[52:55], s86 offen offset:0 sc1
v_mfma_f32_16x16x32_bf16 v[172:175], v[232:235], v[40:43], 0
ds_read_b64_tr_b16 v[250:251], v0 offset:256
v_cvt_pk_bf16_f32 v221, v210, v211
v_mfma_f32_16x16x32_bf16 v[172:175], v[236:239], v[44:47], v[172:175]
ds_read_b64_tr_b16 v[252:253], v0 offset:4096
v_cvt_pk_bf16_f32 v222, v212, v213
v_mfma_f32_16x16x32_bf16 v[176:179], v[240:243], v[16:19], 0
ds_read_b64_tr_b16 v[254:255], v0 offset:4352
v_cvt_pk_bf16_f32 v223, v214, v215
v_mfma_f32_16x16x32_bf16 v[176:179], v[244:247], v[20:23], v[176:179]
buffer_store_dwordx4 v[224:227], v1, s[52:55], s3 offen offset:0 sc1
v_mfma_f32_16x16x32_bf16 v[180:183], v[240:243], v[24:27], 0
)ASM"
R"ASM(
ds_read_b64_tr_b16 v[232:233], v3 offset:0
v_accvgpr_read_b32 v208, a224
v_mfma_f32_16x16x32_bf16 v[180:183], v[244:247], v[28:31], v[180:183]
ds_read_b64_tr_b16 v[234:235], v3 offset:256
v_accvgpr_read_b32 v209, a225
v_mfma_f32_16x16x32_bf16 v[184:187], v[240:243], v[32:35], 0
ds_read_b64_tr_b16 v[236:237], v3 offset:4096
v_accvgpr_read_b32 v210, a226
v_mfma_f32_16x16x32_bf16 v[184:187], v[244:247], v[36:39], v[184:187]
ds_read_b64_tr_b16 v[238:239], v3 offset:4352
v_accvgpr_read_b32 v211, a227
v_mfma_f32_16x16x32_bf16 v[188:191], v[240:243], v[40:43], 0
ds_read_b64_tr_b16 v[240:241], v0 offset:512
v_cvt_pk_bf16_f32 v208, v208, v209
v_mfma_f32_16x16x32_bf16 v[188:191], v[244:247], v[44:47], v[188:191]
s_waitcnt vmcnt(24) lgkmcnt(4)
ds_read_b64_tr_b16 v[242:243], v0 offset:768
v_accvgpr_read_b32 v212, a228
v_mfma_f32_16x16x32_bf16 v[192:195], v[248:251], v[48:51], v[192:195]
buffer_load_dwordx4 a[36:39], v4, s[52:55], s86 offen offset:1024 sc1
v_mfma_f32_16x16x32_bf16 v[192:195], v[252:255], v[52:55], v[192:195]
s_waitcnt vmcnt(23)
ds_read_b64_tr_b16 v[244:245], v0 offset:4608
v_accvgpr_read_b32 v213, a229
v_mfma_f32_16x16x32_bf16 v[196:199], v[248:251], v[56:59], v[196:199]
s_waitcnt vmcnt(22)
ds_read_b64_tr_b16 v[246:247], v0 offset:4864
v_accvgpr_read_b32 v214, a230
v_mfma_f32_16x16x32_bf16 v[196:199], v[252:255], v[60:63], v[196:199]
s_waitcnt vmcnt(18)
v_accvgpr_read_b32 v215, a231
v_cvt_pk_bf16_f32 v209, v210, v211
v_mfma_f32_16x16x32_bf16 v[200:203], v[248:251], v[64:67], v[200:203]
buffer_load_dwordx4 a[40:43], v4, s[52:55], s87 offen offset:0 sc1
v_mfma_f32_16x16x32_bf16 v[200:203], v[252:255], v[68:71], v[200:203]
s_waitcnt vmcnt(16)
v_cvt_pk_bf16_f32 v210, v212, v213
v_cvt_pk_bf16_f32 v211, v214, v215
v_mfma_f32_16x16x32_bf16 v[204:207], v[248:251], v[72:75], v[204:207]
s_waitcnt vmcnt(14)
ds_read_b64_tr_b16 v[248:249], v3 offset:512
v_accvgpr_write_b32 a224, v208
v_mfma_f32_16x16x32_bf16 v[204:207], v[252:255], v[76:79], v[204:207]
s_waitcnt lgkmcnt(1)
ds_read_b64_tr_b16 v[250:251], v3 offset:768
v_accvgpr_write_b32 a225, v209
v_mfma_f32_16x16x32_bf16 v[80:83], v[232:235], v[48:51], v[80:83]
buffer_load_dwordx4 a[44:47], v4, s[52:55], s87 offen offset:1024 sc1
v_mfma_f32_16x16x32_bf16 v[80:83], v[236:239], v[52:55], v[80:83]
ds_read_b64_tr_b16 v[252:253], v3 offset:4608
v_accvgpr_write_b32 a226, v210
v_mfma_f32_16x16x32_bf16 v[84:87], v[232:235], v[56:59], v[84:87]
ds_read_b64_tr_b16 v[254:255], v3 offset:4864
v_accvgpr_write_b32 a227, v211
v_mfma_f32_16x16x32_bf16 v[84:87], v[236:239], v[60:63], v[84:87]
v_accvgpr_read_b32 v208, a96
v_accvgpr_read_b32 v209, a97
v_mfma_f32_16x16x32_bf16 v[88:91], v[232:235], v[64:67], v[88:91]
buffer_store_dwordx4 v[228:231], v1, s[52:55], s3 offen offset:2048 sc1
v_mfma_f32_16x16x32_bf16 v[88:91], v[236:239], v[68:71], v[88:91]
s_add_u32 s3, s82, 307200
v_accvgpr_read_b32 v210, a98
v_accvgpr_read_b32 v211, a99
v_mfma_f32_16x16x32_bf16 v[92:95], v[232:235], v[72:75], v[92:95]
ds_read_b64_tr_b16 v[232:233], v0 offset:8192
v_accvgpr_read_b32 v212, a100
v_mfma_f32_16x16x32_bf16 v[92:95], v[236:239], v[76:79], v[92:95]
ds_read_b64_tr_b16 v[234:235], v0 offset:8448
v_accvgpr_read_b32 v213, a101
v_mfma_f32_16x16x32_bf16 v[96:99], v[240:243], v[48:51], v[96:99]
ds_read_b64_tr_b16 v[236:237], v0 offset:12288
v_accvgpr_read_b32 v214, a102
v_mfma_f32_16x16x32_bf16 v[96:99], v[244:247], v[52:55], v[96:99]
ds_read_b64_tr_b16 v[238:239], v0 offset:12544
v_accvgpr_read_b32 v215, a103
v_mfma_f32_16x16x32_bf16 v[100:103], v[240:243], v[56:59], v[100:103]
v_cvt_pk_bf16_f32 v224, v208, v209
v_cvt_pk_bf16_f32 v225, v210, v211
v_mfma_f32_16x16x32_bf16 v[100:103], v[244:247], v[60:63], v[100:103]
buffer_load_dwordx4 a[48:51], v4, s[52:55], s88 offen offset:0 sc1
v_mfma_f32_16x16x32_bf16 v[104:107], v[240:243], v[64:67], v[104:107]
v_cvt_pk_bf16_f32 v226, v212, v213
v_cvt_pk_bf16_f32 v227, v214, v215
v_mfma_f32_16x16x32_bf16 v[104:107], v[244:247], v[68:71], v[104:107]
v_accvgpr_read_b32 v208, a232
v_accvgpr_read_b32 v209, a233
v_mfma_f32_16x16x32_bf16 v[108:111], v[240:243], v[72:75], v[108:111]
s_waitcnt lgkmcnt(0)
ds_read_b64_tr_b16 v[240:241], v3 offset:8192
v_accvgpr_read_b32 v210, a234
v_mfma_f32_16x16x32_bf16 v[108:111], v[244:247], v[76:79], v[108:111]
buffer_load_dwordx4 a[52:55], v4, s[52:55], s88 offen offset:1024 sc1
v_mfma_f32_16x16x32_bf16 v[112:115], v[248:251], v[48:51], v[112:115]
ds_read_b64_tr_b16 v[242:243], v3 offset:8448
v_accvgpr_read_b32 v211, a235
v_mfma_f32_16x16x32_bf16 v[112:115], v[252:255], v[52:55], v[112:115]
ds_read_b64_tr_b16 v[244:245], v3 offset:12288
v_cvt_pk_bf16_f32 v208, v208, v209
v_mfma_f32_16x16x32_bf16 v[116:119], v[248:251], v[56:59], v[116:119]
ds_read_b64_tr_b16 v[246:247], v3 offset:12544
v_accvgpr_read_b32 v212, a236
v_mfma_f32_16x16x32_bf16 v[116:119], v[252:255], v[60:63], v[116:119]
buffer_load_dwordx4 a[56:59], v4, s[52:55], s89 offen offset:0 sc1
v_mfma_f32_16x16x32_bf16 v[120:123], v[248:251], v[64:67], v[120:123]
v_accvgpr_read_b32 v213, a237
v_accvgpr_read_b32 v214, a238
v_mfma_f32_16x16x32_bf16 v[120:123], v[252:255], v[68:71], v[120:123]
v_accvgpr_read_b32 v215, a239
v_cvt_pk_bf16_f32 v209, v210, v211
v_mfma_f32_16x16x32_bf16 v[124:127], v[248:251], v[72:75], v[124:127]
ds_read_b64_tr_b16 v[248:249], v0 offset:8704
v_cvt_pk_bf16_f32 v210, v212, v213
v_mfma_f32_16x16x32_bf16 v[124:127], v[252:255], v[76:79], v[124:127]
buffer_load_dwordx4 a[60:63], v4, s[52:55], s89 offen offset:1024 sc1
v_mfma_f32_16x16x32_bf16 v[128:131], v[232:235], v[48:51], v[128:131]
ds_read_b64_tr_b16 v[250:251], v0 offset:8960
v_add_u32 v4, s94, v1
v_mfma_f32_16x16x32_bf16 v[128:131], v[236:239], v[52:55], v[128:131]
s_add_u32 s94, s62, 5
s_sub_u32 s5, s94, 10
ds_read_b64_tr_b16 v[252:253], v0 offset:12800
v_mfma_f32_16x16x32_bf16 v[132:135], v[232:235], v[56:59], v[132:135]
s_min_u32 s94, s94, s5
s_lshl_b32 s94, s94, 11
ds_read_b64_tr_b16 v[254:255], v0 offset:13056
v_mfma_f32_16x16x32_bf16 v[132:135], v[236:239], v[60:63], v[132:135]
buffer_store_dwordx4 v[216:219], v1, s[52:55], s0 offen offset:0 sc1
v_mfma_f32_16x16x32_bf16 v[136:139], v[232:235], v[64:67], v[136:139]
v_cvt_pk_bf16_f32 v211, v214, v215
v_accvgpr_write_b32 a232, v208
v_mfma_f32_16x16x32_bf16 v[136:139], v[236:239], v[68:71], v[136:139]
v_accvgpr_write_b32 a233, v209
v_accvgpr_write_b32 a234, v210
v_mfma_f32_16x16x32_bf16 v[140:143], v[232:235], v[72:75], v[140:143]
ds_read_b64_tr_b16 v[232:233], v3 offset:8704
v_accvgpr_write_b32 a235, v211
v_mfma_f32_16x16x32_bf16 v[140:143], v[236:239], v[76:79], v[140:143]
s_waitcnt lgkmcnt(1)
ds_read_b64_tr_b16 v[234:235], v3 offset:8960
v_accvgpr_read_b32 v208, a104
v_mfma_f32_16x16x32_bf16 v[144:147], v[240:243], v[48:51], v[144:147]
ds_read_b64_tr_b16 v[236:237], v3 offset:12800
v_accvgpr_read_b32 v209, a105
v_mfma_f32_16x16x32_bf16 v[144:147], v[244:247], v[52:55], v[144:147]
ds_read_b64_tr_b16 v[238:239], v3 offset:13056
v_accvgpr_read_b32 v210, a106
v_mfma_f32_16x16x32_bf16 v[148:151], v[240:243], v[56:59], v[148:151]
buffer_load_dwordx4 v[16:19], v4, s[52:55], s86 offen offset:0 sc1
v_mfma_f32_16x16x32_bf16 v[148:151], v[244:247], v[60:63], v[148:151]
v_accvgpr_read_b32 v211, a107
v_accvgpr_read_b32 v212, a108
v_mfma_f32_16x16x32_bf16 v[152:155], v[240:243], v[64:67], v[152:155]
v_accvgpr_read_b32 v213, a109
v_accvgpr_read_b32 v214, a110
v_mfma_f32_16x16x32_bf16 v[152:155], v[244:247], v[68:71], v[152:155]
v_accvgpr_read_b32 v215, a111
v_cvt_pk_bf16_f32 v228, v208, v209
v_mfma_f32_16x16x32_bf16 v[156:159], v[240:243], v[72:75], v[156:159]
buffer_load_dwordx4 v[20:23], v4, s[52:55], s86 offen offset:1024 sc1
v_mfma_f32_16x16x32_bf16 v[156:159], v[244:247], v[76:79], v[156:159]
v_cvt_pk_bf16_f32 v229, v210, v211
v_cvt_pk_bf16_f32 v230, v212, v213
v_mfma_f32_16x16x32_bf16 v[160:163], v[248:251], v[48:51], v[160:163]
v_cvt_pk_bf16_f32 v231, v214, v215
v_accvgpr_read_b32 v208, a208
v_mfma_f32_16x16x32_bf16 v[160:163], v[252:255], v[52:55], v[160:163]
v_accvgpr_read_b32 v209, a209
v_accvgpr_read_b32 v210, a210
v_mfma_f32_16x16x32_bf16 v[164:167], v[248:251], v[56:59], v[164:167]
buffer_load_dwordx4 v[24:27], v4, s[52:55], s87 offen offset:0 sc1
v_mfma_f32_16x16x32_bf16 v[164:167], v[252:255], v[60:63], v[164:167]
s_waitcnt lgkmcnt(0)
v_accvgpr_read_b32 v211, a211
v_cvt_pk_bf16_f32 v208, v208, v209
v_mfma_f32_16x16x32_bf16 v[168:171], v[248:251], v[64:67], v[168:171]
s_barrier
ds_read_b64_tr_b16 v[240:241], v0 offset:32768
v_accvgpr_read_b32 v212, a240
v_mfma_f32_16x16x32_bf16 v[168:171], v[252:255], v[68:71], v[168:171]
ds_read_b64_tr_b16 v[242:243], v0 offset:33024
v_accvgpr_read_b32 v213, a241
v_mfma_f32_16x16x32_bf16 v[172:175], v[248:251], v[72:75], v[172:175]
buffer_store_dwordx4 v[220:223], v1, s[52:55], s0 offen offset:1024 sc1
v_mfma_f32_16x16x32_bf16 v[172:175], v[252:255], v[76:79], v[172:175]
s_add_u32 s0, s4, 606208
ds_read_b64_tr_b16 v[244:245], v0 offset:36864
v_accvgpr_read_b32 v214, a242
v_mfma_f32_16x16x32_bf16 v[176:179], v[232:235], v[48:51], v[176:179]
ds_read_b64_tr_b16 v[246:247], v0 offset:37120
v_accvgpr_read_b32 v215, a243
v_mfma_f32_16x16x32_bf16 v[176:179], v[236:239], v[52:55], v[176:179]
ds_read_b64_tr_b16 v[248:249], v3 offset:32768
v_cvt_pk_bf16_f32 v209, v210, v211
v_mfma_f32_16x16x32_bf16 v[180:183], v[232:235], v[56:59], v[180:183]
ds_read_b64_tr_b16 v[250:251], v3 offset:33024
v_cvt_pk_bf16_f32 v210, v212, v213
v_mfma_f32_16x16x32_bf16 v[180:183], v[236:239], v[60:63], v[180:183]
ds_read_b64_tr_b16 v[252:253], v3 offset:36864
v_cvt_pk_bf16_f32 v211, v214, v215
v_mfma_f32_16x16x32_bf16 v[184:187], v[232:235], v[64:67], v[184:187]
ds_read_b64_tr_b16 v[254:255], v3 offset:37120
v_accvgpr_write_b32 a208, v208
v_mfma_f32_16x16x32_bf16 v[184:187], v[236:239], v[68:71], v[184:187]
buffer_load_dwordx4 v[28:31], v4, s[52:55], s87 offen offset:1024 sc1
v_mfma_f32_16x16x32_bf16 v[188:191], v[232:235], v[72:75], v[188:191]
ds_read_b64_tr_b16 v[232:233], v0 offset:33280
v_accvgpr_write_b32 a209, v209
v_mfma_f32_16x16x32_bf16 v[188:191], v[236:239], v[76:79], v[188:191]
s_waitcnt vmcnt(25) lgkmcnt(3)
ds_read_b64_tr_b16 v[234:235], v0 offset:33536
v_accvgpr_write_b32 a210, v210
v_mfma_f32_16x16x32_bf16 v[192:195], v[240:243], a[0:3], v[192:195]
s_waitcnt vmcnt(22)
ds_read_b64_tr_b16 v[236:237], v0 offset:37376
v_accvgpr_write_b32 a211, v211
v_mfma_f32_16x16x32_bf16 v[192:195], v[244:247], a[4:7], v[192:195]
buffer_load_dwordx4 v[32:35], v4, s[52:55], s88 offen offset:0 sc1
v_mfma_f32_16x16x32_bf16 v[196:199], v[240:243], a[8:11], v[196:199]
s_waitcnt vmcnt(22)
ds_read_b64_tr_b16 v[238:239], v0 offset:37632
v_accvgpr_read_b32 v208, a80
v_mfma_f32_16x16x32_bf16 v[196:199], v[244:247], a[12:15], v[196:199]
s_waitcnt vmcnt(21)
v_accvgpr_read_b32 v209, a81
v_accvgpr_read_b32 v210, a82
v_mfma_f32_16x16x32_bf16 v[200:203], v[240:243], a[16:19], v[200:203]
s_waitcnt vmcnt(18)
v_accvgpr_read_b32 v211, a83
v_accvgpr_read_b32 v212, a112
v_mfma_f32_16x16x32_bf16 v[200:203], v[244:247], a[20:23], v[200:203]
buffer_load_dwordx4 v[36:39], v4, s[52:55], s88 offen offset:1024 sc1
v_mfma_f32_16x16x32_bf16 v[204:207], v[240:243], a[24:27], v[204:207]
s_waitcnt vmcnt(18)
ds_read_b64_tr_b16 v[240:241], v3 offset:33280
v_accvgpr_read_b32 v213, a113
v_mfma_f32_16x16x32_bf16 v[204:207], v[244:247], a[28:31], v[204:207]
ds_read_b64_tr_b16 v[242:243], v3 offset:33536
v_accvgpr_read_b32 v214, a114
v_mfma_f32_16x16x32_bf16 v[80:83], v[248:251], a[0:3], v[80:83]
s_waitcnt lgkmcnt(2)
ds_read_b64_tr_b16 v[244:245], v3 offset:37376
v_accvgpr_read_b32 v215, a115
v_mfma_f32_16x16x32_bf16 v[80:83], v[252:255], a[4:7], v[80:83]
ds_read_b64_tr_b16 v[246:247], v3 offset:37632
v_cvt_pk_bf16_f32 v216, v208, v209
v_mfma_f32_16x16x32_bf16 v[84:87], v[248:251], a[8:11], v[84:87]
buffer_store_dwordx4 v[224:227], v1, s[52:55], s1 offen offset:0 sc1
v_mfma_f32_16x16x32_bf16 v[84:87], v[252:255], a[12:15], v[84:87]
v_cvt_pk_bf16_f32 v217, v210, v211
v_cvt_pk_bf16_f32 v218, v212, v213
v_mfma_f32_16x16x32_bf16 v[88:91], v[248:251], a[16:19], v[88:91]
v_cvt_pk_bf16_f32 v219, v214, v215
v_accvgpr_read_b32 v208, a212
v_mfma_f32_16x16x32_bf16 v[88:91], v[252:255], a[20:23], v[88:91]
v_accvgpr_read_b32 v209, a213
v_accvgpr_read_b32 v210, a214
v_mfma_f32_16x16x32_bf16 v[92:95], v[248:251], a[24:27], v[92:95]
ds_read_b64_tr_b16 v[248:249], v0 offset:40960
v_accvgpr_read_b32 v211, a215
v_mfma_f32_16x16x32_bf16 v[92:95], v[252:255], a[28:31], v[92:95]
ds_read_b64_tr_b16 v[250:251], v0 offset:41216
v_cvt_pk_bf16_f32 v208, v208, v209
v_mfma_f32_16x16x32_bf16 v[96:99], v[232:235], a[0:3], v[96:99]
ds_read_b64_tr_b16 v[252:253], v0 offset:45056
v_accvgpr_read_b32 v212, a244
v_mfma_f32_16x16x32_bf16 v[96:99], v[236:239], a[4:7], v[96:99]
buffer_load_dwordx4 v[40:43], v4, s[52:55], s89 offen offset:0 sc1
v_mfma_f32_16x16x32_bf16 v[100:103], v[232:235], a[8:11], v[100:103]
ds_read_b64_tr_b16 v[254:255], v0 offset:45312
v_accvgpr_read_b32 v213, a245
v_mfma_f32_16x16x32_bf16 v[100:103], v[236:239], a[12:15], v[100:103]
v_accvgpr_read_b32 v214, a246
v_accvgpr_read_b32 v215, a247
v_mfma_f32_16x16x32_bf16 v[104:107], v[232:235], a[16:19], v[104:107]
v_cvt_pk_bf16_f32 v209, v210, v211
v_cvt_pk_bf16_f32 v210, v212, v213
v_mfma_f32_16x16x32_bf16 v[104:107], v[236:239], a[20:23], v[104:107]
buffer_load_dwordx4 v[44:47], v4, s[52:55], s89 offen offset:1024 sc1
v_mfma_f32_16x16x32_bf16 v[108:111], v[232:235], a[24:27], v[108:111]
s_waitcnt lgkmcnt(0)
ds_read_b64_tr_b16 v[232:233], v3 offset:40960
v_add_u32 v4, s94, v1
v_mfma_f32_16x16x32_bf16 v[108:111], v[236:239], a[28:31], v[108:111]
s_add_u32 s94, s62, 6
ds_read_b64_tr_b16 v[234:235], v3 offset:41216
v_cvt_pk_bf16_f32 v211, v214, v215
v_mfma_f32_16x16x32_bf16 v[112:115], v[240:243], a[0:3], v[112:115]
s_sub_u32 s5, s94, 10
ds_read_b64_tr_b16 v[236:237], v3 offset:45056
v_accvgpr_write_b32 a212, v208
v_mfma_f32_16x16x32_bf16 v[112:115], v[244:247], a[4:7], v[112:115]
s_min_u32 s94, s94, s5
ds_read_b64_tr_b16 v[238:239], v3 offset:45312
v_accvgpr_write_b32 a213, v209
v_mfma_f32_16x16x32_bf16 v[116:119], v[240:243], a[8:11], v[116:119]
buffer_load_dwordx4 v[48:51], v4, s[52:55], s86 offen offset:0 sc1
v_mfma_f32_16x16x32_bf16 v[116:119], v[244:247], a[12:15], v[116:119]
s_lshl_b32 s94, s94, 11
v_accvgpr_write_b32 a214, v210
v_accvgpr_write_b32 a215, v211
v_mfma_f32_16x16x32_bf16 v[120:123], v[240:243], a[16:19], v[120:123]
v_accvgpr_read_b32 v208, a84
v_accvgpr_read_b32 v209, a85
v_mfma_f32_16x16x32_bf16 v[120:123], v[244:247], a[20:23], v[120:123]
v_accvgpr_read_b32 v210, a86
v_accvgpr_read_b32 v211, a87
v_mfma_f32_16x16x32_bf16 v[124:127], v[240:243], a[24:27], v[124:127]
ds_read_b64_tr_b16 v[240:241], v0 offset:41472
v_accvgpr_read_b32 v212, a116
v_mfma_f32_16x16x32_bf16 v[124:127], v[244:247], a[28:31], v[124:127]
ds_read_b64_tr_b16 v[242:243], v0 offset:41728
v_accvgpr_read_b32 v213, a117
v_mfma_f32_16x16x32_bf16 v[128:131], v[248:251], a[0:3], v[128:131]
ds_read_b64_tr_b16 v[244:245], v0 offset:45568
v_accvgpr_read_b32 v214, a118
v_mfma_f32_16x16x32_bf16 v[128:131], v[252:255], a[4:7], v[128:131]
buffer_store_dwordx4 v[228:231], v1, s[52:55], s1 offen offset:1024 sc1
v_mfma_f32_16x16x32_bf16 v[132:135], v[248:251], a[8:11], v[132:135]
s_add_u32 s1, s4, 610304
ds_read_b64_tr_b16 v[246:247], v0 offset:45824
v_accvgpr_read_b32 v215, a119
v_mfma_f32_16x16x32_bf16 v[132:135], v[252:255], a[12:15], v[132:135]
v_cvt_pk_bf16_f32 v220, v208, v209
v_cvt_pk_bf16_f32 v221, v210, v211
v_mfma_f32_16x16x32_bf16 v[136:139], v[248:251], a[16:19], v[136:139]
v_cvt_pk_bf16_f32 v222, v212, v213
v_cvt_pk_bf16_f32 v223, v214, v215
v_mfma_f32_16x16x32_bf16 v[136:139], v[252:255], a[20:23], v[136:139]
v_accvgpr_read_b32 v208, a216
v_accvgpr_read_b32 v209, a217
v_mfma_f32_16x16x32_bf16 v[140:143], v[248:251], a[24:27], v[140:143]
ds_read_b64_tr_b16 v[248:249], v3 offset:41472
v_accvgpr_read_b32 v210, a218
v_mfma_f32_16x16x32_bf16 v[140:143], v[252:255], a[28:31], v[140:143]
s_waitcnt lgkmcnt(1)
ds_read_b64_tr_b16 v[250:251], v3 offset:41728
v_accvgpr_read_b32 v211, a219
v_mfma_f32_16x16x32_bf16 v[144:147], v[232:235], a[0:3], v[144:147]
buffer_load_dwordx4 v[52:55], v4, s[52:55], s86 offen offset:1024 sc1
v_mfma_f32_16x16x32_bf16 v[144:147], v[236:239], a[4:7], v[144:147]
ds_read_b64_tr_b16 v[252:253], v3 offset:45568
v_cvt_pk_bf16_f32 v208, v208, v209
v_mfma_f32_16x16x32_bf16 v[148:151], v[232:235], a[8:11], v[148:151]
ds_read_b64_tr_b16 v[254:255], v3 offset:45824
v_accvgpr_read_b32 v212, a248
v_mfma_f32_16x16x32_bf16 v[148:151], v[236:239], a[12:15], v[148:151]
v_accvgpr_read_b32 v213, a249
v_accvgpr_read_b32 v214, a250
v_mfma_f32_16x16x32_bf16 v[152:155], v[232:235], a[16:19], v[152:155]
buffer_load_dwordx4 v[56:59], v4, s[52:55], s87 offen offset:0 sc1
v_mfma_f32_16x16x32_bf16 v[152:155], v[236:239], a[20:23], v[152:155]
v_accvgpr_read_b32 v215, a251
v_cvt_pk_bf16_f32 v209, v210, v211
v_mfma_f32_16x16x32_bf16 v[156:159], v[232:235], a[24:27], v[156:159]
v_cvt_pk_bf16_f32 v210, v212, v213
v_cvt_pk_bf16_f32 v211, v214, v215
v_mfma_f32_16x16x32_bf16 v[156:159], v[236:239], a[28:31], v[156:159]
v_accvgpr_write_b32 a216, v208
v_accvgpr_write_b32 a217, v209
v_mfma_f32_16x16x32_bf16 v[160:163], v[240:243], a[0:3], v[160:163]
buffer_load_dwordx4 v[60:63], v4, s[52:55], s87 offen offset:1024 sc1
v_mfma_f32_16x16x32_bf16 v[160:163], v[244:247], a[4:7], v[160:163]
v_accvgpr_write_b32 a218, v210
v_accvgpr_write_b32 a219, v211
v_mfma_f32_16x16x32_bf16 v[164:167], v[240:243], a[8:11], v[164:167]
v_accvgpr_read_b32 v208, a88
v_accvgpr_read_b32 v209, a89
v_mfma_f32_16x16x32_bf16 v[164:167], v[244:247], a[12:15], v[164:167]
s_waitcnt vmcnt(20) lgkmcnt(0)
v_accvgpr_read_b32 v210, a90
v_accvgpr_read_b32 v211, a91
v_mfma_f32_16x16x32_bf16 v[168:171], v[240:243], a[16:19], v[168:171]
s_barrier
ds_read_b64_tr_b16 v[232:233], v0 offset:49152
v_accvgpr_read_b32 v212, a120
v_mfma_f32_16x16x32_bf16 v[168:171], v[244:247], a[20:23], v[168:171]
buffer_store_dwordx2 v[10:11], v5, s[52:55], s93 offen sc1
v_mfma_f32_16x16x32_bf16 v[172:175], v[240:243], a[24:27], v[172:175]
s_lshl_b32 s93, s61, 11
L_pad_a_1_1:
s_cbranch_execz L_pad_a_1_2
ds_read_b64_tr_b16 v[234:235], v0 offset:49408
v_accvgpr_read_b32 v213, a121
v_mfma_f32_16x16x32_bf16 v[172:175], v[244:247], a[28:31], v[172:175]
s_add_u32 s93, s93, 196608
ds_read_b64_tr_b16 v[236:237], v0 offset:53248
v_accvgpr_read_b32 v214, a122
v_mfma_f32_16x16x32_bf16 v[176:179], v[248:251], a[0:3], v[176:179]
ds_read_b64_tr_b16 v[238:239], v0 offset:53504
v_accvgpr_read_b32 v215, a123
v_mfma_f32_16x16x32_bf16 v[176:179], v[252:255], a[4:7], v[176:179]
buffer_load_dwordx4 v[64:67], v4, s[52:55], s88 offen offset:0 sc1
v_mfma_f32_16x16x32_bf16 v[180:183], v[248:251], a[8:11], v[180:183]
ds_read_b64_tr_b16 v[240:241], v3 offset:49152
v_cvt_pk_bf16_f32 v224, v208, v209
v_mfma_f32_16x16x32_bf16 v[180:183], v[252:255], a[12:15], v[180:183]
ds_read_b64_tr_b16 v[242:243], v3 offset:49408
v_cvt_pk_bf16_f32 v225, v210, v211
v_mfma_f32_16x16x32_bf16 v[184:187], v[248:251], a[16:19], v[184:187]
ds_read_b64_tr_b16 v[244:245], v3 offset:53248
)ASM"
R"ASM(
v_cvt_pk_bf16_f32 v226, v212, v213
v_mfma_f32_16x16x32_bf16 v[184:187], v[252:255], a[20:23], v[184:187]
buffer_store_dwordx4 v[216:219], v1, s[52:55], s2 offen offset:0 sc1
v_mfma_f32_16x16x32_bf16 v[188:191], v[248:251], a[24:27], v[188:191]
ds_read_b64_tr_b16 v[246:247], v3 offset:53504
v_cvt_pk_bf16_f32 v227, v214, v215
v_mfma_f32_16x16x32_bf16 v[188:191], v[252:255], a[28:31], v[188:191]
s_waitcnt lgkmcnt(3)
ds_read_b64_tr_b16 v[248:249], v0 offset:49664
v_accvgpr_read_b32 v208, a220
v_mfma_f32_16x16x32_bf16 v[192:195], v[232:235], a[32:35], v[192:195]
ds_read_b64_tr_b16 v[250:251], v0 offset:49920
v_accvgpr_read_b32 v209, a221
v_mfma_f32_16x16x32_bf16 v[192:195], v[236:239], a[36:39], v[192:195]
ds_read_b64_tr_b16 v[252:253], v0 offset:53760
v_accvgpr_read_b32 v210, a222
v_mfma_f32_16x16x32_bf16 v[196:199], v[232:235], a[40:43], v[196:199]
ds_read_b64_tr_b16 v[254:255], v0 offset:54016
v_accvgpr_read_b32 v211, a223
v_mfma_f32_16x16x32_bf16 v[196:199], v[236:239], a[44:47], v[196:199]
s_waitcnt vmcnt(21)
v_accvgpr_read_b32 v212, a252
v_cvt_pk_bf16_f32 v208, v208, v209
v_mfma_f32_16x16x32_bf16 v[200:203], v[232:235], a[48:51], v[200:203]
buffer_load_dwordx4 v[68:71], v4, s[52:55], s88 offen offset:1024 sc1
v_mfma_f32_16x16x32_bf16 v[200:203], v[236:239], a[52:55], v[200:203]
s_waitcnt vmcnt(21)
v_accvgpr_read_b32 v213, a253
v_accvgpr_read_b32 v214, a254
v_mfma_f32_16x16x32_bf16 v[204:207], v[232:235], a[56:59], v[204:207]
s_waitcnt vmcnt(20)
ds_read_b64_tr_b16 v[232:233], v3 offset:49664
v_accvgpr_read_b32 v215, a255
v_mfma_f32_16x16x32_bf16 v[204:207], v[236:239], a[60:63], v[204:207]
s_waitcnt lgkmcnt(1)
ds_read_b64_tr_b16 v[234:235], v3 offset:49920
v_cvt_pk_bf16_f32 v209, v210, v211
v_mfma_f32_16x16x32_bf16 v[80:83], v[240:243], a[32:35], v[80:83]
buffer_load_dwordx4 v[72:75], v4, s[52:55], s89 offen offset:0 sc1
v_mfma_f32_16x16x32_bf16 v[80:83], v[244:247], a[36:39], v[80:83]
ds_read_b64_tr_b16 v[236:237], v3 offset:53760
v_cvt_pk_bf16_f32 v210, v212, v213
v_mfma_f32_16x16x32_bf16 v[84:87], v[240:243], a[40:43], v[84:87]
ds_read_b64_tr_b16 v[238:239], v3 offset:54016
v_cvt_pk_bf16_f32 v211, v214, v215
v_mfma_f32_16x16x32_bf16 v[84:87], v[244:247], a[44:47], v[84:87]
v_accvgpr_write_b32 a220, v208
v_accvgpr_write_b32 a221, v209
v_mfma_f32_16x16x32_bf16 v[88:91], v[240:243], a[48:51], v[88:91]
buffer_load_dwordx4 v[76:79], v4, s[52:55], s89 offen offset:1024 sc1
v_mfma_f32_16x16x32_bf16 v[88:91], v[244:247], a[52:55], v[88:91]
v_accvgpr_write_b32 a222, v210
v_add_u32 v4, s94, v1
v_mfma_f32_16x16x32_bf16 v[92:95], v[240:243], a[56:59], v[92:95]
s_add_u32 s94, s62, 7
ds_read_b64_tr_b16 v[240:241], v0 offset:57344
v_accvgpr_write_b32 a223, v211
v_mfma_f32_16x16x32_bf16 v[92:95], v[244:247], a[60:63], v[92:95]
s_sub_u32 s5, s94, 10
ds_read_b64_tr_b16 v[242:243], v0 offset:57600
v_accvgpr_read_b32 v208, a92
v_mfma_f32_16x16x32_bf16 v[96:99], v[248:251], a[32:35], v[96:99]
buffer_store_dwordx4 v[220:223], v1, s[52:55], s2 offen offset:2048 sc1
v_mfma_f32_16x16x32_bf16 v[96:99], v[252:255], a[36:39], v[96:99]
s_min_u32 s94, s94, s5
ds_read_b64_tr_b16 v[244:245], v0 offset:61440
v_accvgpr_read_b32 v209, a93
v_mfma_f32_16x16x32_bf16 v[100:103], v[248:251], a[40:43], v[100:103]
s_lshl_b32 s94, s94, 11
ds_read_b64_tr_b16 v[246:247], v0 offset:61696
v_accvgpr_read_b32 v210, a94
v_mfma_f32_16x16x32_bf16 v[100:103], v[252:255], a[44:47], v[100:103]
s_add_u32 s2, s82, 606208
v_accvgpr_read_b32 v211, a95
v_accvgpr_read_b32 v212, a124
v_mfma_f32_16x16x32_bf16 v[104:107], v[248:251], a[48:51], v[104:107]
v_accvgpr_read_b32 v213, a125
v_accvgpr_read_b32 v214, a126
v_mfma_f32_16x16x32_bf16 v[104:107], v[252:255], a[52:55], v[104:107]
v_accvgpr_read_b32 v215, a127
v_cvt_pk_bf16_f32 v228, v208, v209
v_mfma_f32_16x16x32_bf16 v[108:111], v[248:251], a[56:59], v[108:111]
s_waitcnt lgkmcnt(0)
ds_read_b64_tr_b16 v[248:249], v3 offset:57344
v_cvt_pk_bf16_f32 v229, v210, v211
v_mfma_f32_16x16x32_bf16 v[108:111], v[252:255], a[60:63], v[108:111]
buffer_load_dwordx4 a[64:67], v4, s[52:55], s86 offen offset:0 sc1
v_mfma_f32_16x16x32_bf16 v[112:115], v[232:235], a[32:35], v[112:115]
ds_read_b64_tr_b16 v[250:251], v3 offset:57600
v_cvt_pk_bf16_f32 v230, v212, v213
v_mfma_f32_16x16x32_bf16 v[112:115], v[236:239], a[36:39], v[112:115]
ds_read_b64_tr_b16 v[252:253], v3 offset:61440
v_cvt_pk_bf16_f32 v231, v214, v215
v_mfma_f32_16x16x32_bf16 v[116:119], v[232:235], a[40:43], v[116:119]
ds_read_b64_tr_b16 v[254:255], v3 offset:61696
v_accvgpr_read_b32 v208, a128
v_mfma_f32_16x16x32_bf16 v[116:119], v[236:239], a[44:47], v[116:119]
buffer_load_dwordx4 a[68:71], v4, s[52:55], s86 offen offset:1024 sc1
v_mfma_f32_16x16x32_bf16 v[120:123], v[232:235], a[48:51], v[120:123]
v_accvgpr_read_b32 v209, a129
v_accvgpr_read_b32 v210, a130
v_mfma_f32_16x16x32_bf16 v[120:123], v[236:239], a[52:55], v[120:123]
v_accvgpr_read_b32 v211, a131
v_accvgpr_read_b32 v212, a132
v_mfma_f32_16x16x32_bf16 v[124:127], v[232:235], a[56:59], v[124:127]
ds_read_b64_tr_b16 v[232:233], v0 offset:57856
v_accvgpr_read_b32 v213, a133
v_mfma_f32_16x16x32_bf16 v[124:127], v[236:239], a[60:63], v[124:127]
ds_read_b64_tr_b16 v[234:235], v0 offset:58112
v_accvgpr_read_b32 v214, a134
v_mfma_f32_16x16x32_bf16 v[128:131], v[240:243], a[32:35], v[128:131]
buffer_load_dwordx4 a[72:75], v4, s[52:55], s87 offen offset:0 sc1
v_mfma_f32_16x16x32_bf16 v[128:131], v[244:247], a[36:39], v[128:131]
ds_read_b64_tr_b16 v[236:237], v0 offset:61952
v_accvgpr_read_b32 v215, a135
v_mfma_f32_16x16x32_bf16 v[132:135], v[240:243], a[40:43], v[132:135]
ds_read_b64_tr_b16 v[238:239], v0 offset:62208
v_add_u32 v0, 0x10000, v0
v_mfma_f32_16x16x32_bf16 v[132:135], v[244:247], a[44:47], v[132:135]
v_cvt_pk_bf16_f32 v216, v208, v209
v_cvt_pk_bf16_f32 v217, v210, v211
v_mfma_f32_16x16x32_bf16 v[136:139], v[240:243], a[48:51], v[136:139]
v_cvt_pk_bf16_f32 v218, v212, v213
v_cvt_pk_bf16_f32 v219, v214, v215
v_mfma_f32_16x16x32_bf16 v[136:139], v[244:247], a[52:55], v[136:139]
v_accvgpr_read_b32 v208, a136
v_accvgpr_read_b32 v209, a137
v_mfma_f32_16x16x32_bf16 v[140:143], v[240:243], a[56:59], v[140:143]
buffer_store_dwordx4 v[224:227], v1, s[52:55], s3 offen offset:0 sc1
v_mfma_f32_16x16x32_bf16 v[140:143], v[244:247], a[60:63], v[140:143]
s_waitcnt lgkmcnt(0)
ds_read_b64_tr_b16 v[240:241], v3 offset:57856
v_accvgpr_read_b32 v210, a138
v_mfma_f32_16x16x32_bf16 v[144:147], v[248:251], a[32:35], v[144:147]
ds_read_b64_tr_b16 v[242:243], v3 offset:58112
v_accvgpr_read_b32 v211, a139
v_mfma_f32_16x16x32_bf16 v[144:147], v[252:255], a[36:39], v[144:147]
ds_read_b64_tr_b16 v[244:245], v3 offset:61952
v_accvgpr_read_b32 v212, a140
v_mfma_f32_16x16x32_bf16 v[148:151], v[248:251], a[40:43], v[148:151]
ds_read_b64_tr_b16 v[246:247], v3 offset:62208
v_add_u32 v3, 0x10000, v3
v_mfma_f32_16x16x32_bf16 v[148:151], v[252:255], a[44:47], v[148:151]
v_accvgpr_read_b32 v213, a141
v_accvgpr_read_b32 v214, a142
v_mfma_f32_16x16x32_bf16 v[152:155], v[248:251], a[48:51], v[152:155]
v_accvgpr_read_b32 v215, a143
v_cvt_pk_bf16_f32 v220, v208, v209
v_mfma_f32_16x16x32_bf16 v[152:155], v[252:255], a[52:55], v[152:155]
buffer_load_dwordx4 a[76:79], v4, s[52:55], s87 offen offset:1024 sc1
v_mfma_f32_16x16x32_bf16 v[156:159], v[248:251], a[56:59], v[156:159]
v_cvt_pk_bf16_f32 v221, v210, v211
v_cvt_pk_bf16_f32 v222, v212, v213
v_mfma_f32_16x16x32_bf16 v[156:159], v[252:255], a[60:63], v[156:159]
v_cvt_pk_bf16_f32 v223, v214, v215
v_add_u32 v6, s93, v6
v_mfma_f32_16x16x32_bf16 v[160:163], v[232:235], a[32:35], v[160:163]
s_lshl_b32 s93, s61, 3
v_accvgpr_read_b32 v208, a160
v_accvgpr_read_b32 v209, a161
v_mfma_f32_16x16x32_bf16 v[160:163], v[236:239], a[36:39], v[160:163]
buffer_load_dwordx4 a[80:83], v4, s[52:55], s88 offen offset:0 sc1
v_mfma_f32_16x16x32_bf16 v[164:167], v[232:235], a[40:43], v[164:167]
s_add_u32 s93, s93, s62
v_accvgpr_read_b32 v210, a162
v_accvgpr_read_b32 v211, a163
v_mfma_f32_16x16x32_bf16 v[164:167], v[236:239], a[44:47], v[164:167]
s_lshl_b32 s93, s93, 8
v_accvgpr_read_b32 v212, a164
v_accvgpr_read_b32 v213, a165
v_mfma_f32_16x16x32_bf16 v[168:171], v[232:235], a[48:51], v[168:171]
s_waitcnt lgkmcnt(0)
s_barrier
ds_read_b64_tr_b16 v[248:249], v0 offset:0
v_accvgpr_read_b32 v214, a166
v_mfma_f32_16x16x32_bf16 v[168:171], v[236:239], a[52:55], v[168:171]
buffer_load_dwordx4 a[84:87], v4, s[52:55], s88 offen offset:1024 sc1
v_mfma_f32_16x16x32_bf16 v[172:175], v[232:235], a[56:59], v[172:175]
s_add_u32 s93, s93, 262144
ds_read_b64_tr_b16 v[250:251], v0 offset:256
v_accvgpr_read_b32 v215, a167
v_mfma_f32_16x16x32_bf16 v[172:175], v[236:239], a[60:63], v[172:175]
ds_read_b64_tr_b16 v[252:253], v0 offset:4096
v_cvt_pk_bf16_f32 v224, v208, v209
v_mfma_f32_16x16x32_bf16 v[176:179], v[240:243], a[32:35], v[176:179]
ds_read_b64_tr_b16 v[254:255], v0 offset:4352
v_cvt_pk_bf16_f32 v225, v210, v211
v_mfma_f32_16x16x32_bf16 v[176:179], v[244:247], a[36:39], v[176:179]
ds_read_b64_tr_b16 v[232:233], v3 offset:0
v_cvt_pk_bf16_f32 v226, v212, v213
v_mfma_f32_16x16x32_bf16 v[180:183], v[240:243], a[40:43], v[180:183]
ds_read_b64_tr_b16 v[234:235], v3 offset:256
v_cvt_pk_bf16_f32 v227, v214, v215
v_mfma_f32_16x16x32_bf16 v[180:183], v[244:247], a[44:47], v[180:183]
ds_read_b64_tr_b16 v[236:237], v3 offset:4096
v_accvgpr_read_b32 v208, a168
v_mfma_f32_16x16x32_bf16 v[184:187], v[240:243], a[48:51], v[184:187]
buffer_load_dwordx4 a[88:91], v4, s[52:55], s89 offen offset:0 sc1
v_mfma_f32_16x16x32_bf16 v[184:187], v[244:247], a[52:55], v[184:187]
ds_read_b64_tr_b16 v[238:239], v3 offset:4352
v_accvgpr_read_b32 v209, a169
v_mfma_f32_16x16x32_bf16 v[188:191], v[240:243], a[56:59], v[188:191]
ds_read_b64_tr_b16 v[240:241], v0 offset:512
v_accvgpr_read_b32 v210, a170
v_mfma_f32_16x16x32_bf16 v[188:191], v[244:247], a[60:63], v[188:191]
s_waitcnt vmcnt(27) lgkmcnt(3)
ds_read_b64_tr_b16 v[242:243], v0 offset:768
v_accvgpr_read_b32 v211, a171
v_mfma_f32_16x16x32_bf16 v[192:195], v[248:251], v[16:19], v[192:195]
buffer_store_dwordx4 v[228:231], v1, s[52:55], s3 offen offset:2048 sc1
v_mfma_f32_16x16x32_bf16 v[192:195], v[252:255], v[20:23], v[192:195]
s_add_u32 s3, s82, 610304
ds_read_b64_tr_b16 v[244:245], v0 offset:4608
v_accvgpr_read_b32 v212, a172
v_mfma_f32_16x16x32_bf16 v[196:199], v[248:251], v[24:27], v[196:199]
s_waitcnt vmcnt(26)
ds_read_b64_tr_b16 v[246:247], v0 offset:4864
v_accvgpr_read_b32 v213, a173
v_mfma_f32_16x16x32_bf16 v[196:199], v[252:255], v[28:31], v[196:199]
s_waitcnt vmcnt(25)
v_accvgpr_read_b32 v214, a174
v_accvgpr_read_b32 v215, a175
v_mfma_f32_16x16x32_bf16 v[200:203], v[248:251], v[32:35], v[200:203]
s_waitcnt vmcnt(24)
v_cvt_pk_bf16_f32 v228, v208, v209
v_cvt_pk_bf16_f32 v229, v210, v211
v_mfma_f32_16x16x32_bf16 v[200:203], v[252:255], v[36:39], v[200:203]
s_waitcnt vmcnt(22)
v_cvt_pk_bf16_f32 v230, v212, v213
v_cvt_pk_bf16_f32 v231, v214, v215
v_mfma_f32_16x16x32_bf16 v[204:207], v[248:251], v[40:43], v[204:207]
s_waitcnt vmcnt(21)
ds_read_b64_tr_b16 v[248:249], v3 offset:512
v_accvgpr_read_b32 v208, a144
v_mfma_f32_16x16x32_bf16 v[204:207], v[252:255], v[44:47], v[204:207]
buffer_load_dwordx4 a[92:95], v4, s[52:55], s89 offen offset:1024 sc1
v_mfma_f32_16x16x32_bf16 v[80:83], v[232:235], v[16:19], v[80:83]
s_waitcnt lgkmcnt(1)
ds_read_b64_tr_b16 v[250:251], v3 offset:768
v_add_u32 v4, s94, v1
v_mfma_f32_16x16x32_bf16 v[80:83], v[236:239], v[20:23], v[80:83]
s_add_u32 s94, s62, 8
s_sub_u32 s5, s94, 10
ds_read_b64_tr_b16 v[252:253], v3 offset:4608
v_mfma_f32_16x16x32_bf16 v[84:87], v[232:235], v[24:27], v[84:87]
s_min_u32 s94, s94, s5
s_lshl_b32 s94, s94, 11
ds_read_b64_tr_b16 v[254:255], v3 offset:4864
v_mfma_f32_16x16x32_bf16 v[84:87], v[236:239], v[28:31], v[84:87]
buffer_load_dwordx4 a[96:99], v4, s[52:55], s86 offen offset:0 sc1
v_mfma_f32_16x16x32_bf16 v[88:91], v[232:235], v[32:35], v[88:91]
v_accvgpr_read_b32 v209, a145
v_accvgpr_read_b32 v210, a146
v_mfma_f32_16x16x32_bf16 v[88:91], v[236:239], v[36:39], v[88:91]
v_accvgpr_read_b32 v211, a147
v_accvgpr_read_b32 v212, a176
v_mfma_f32_16x16x32_bf16 v[92:95], v[232:235], v[40:43], v[92:95]
ds_read_b64_tr_b16 v[232:233], v0 offset:8192
v_accvgpr_read_b32 v213, a177
v_mfma_f32_16x16x32_bf16 v[92:95], v[236:239], v[44:47], v[92:95]
ds_read_b64_tr_b16 v[234:235], v0 offset:8448
v_accvgpr_read_b32 v214, a178
v_mfma_f32_16x16x32_bf16 v[96:99], v[240:243], v[16:19], v[96:99]
ds_read_b64_tr_b16 v[236:237], v0 offset:12288
v_accvgpr_read_b32 v215, a179
v_mfma_f32_16x16x32_bf16 v[96:99], v[244:247], v[20:23], v[96:99]
buffer_load_dwordx4 a[100:103], v4, s[52:55], s86 offen offset:1024 sc1
v_mfma_f32_16x16x32_bf16 v[100:103], v[240:243], v[24:27], v[100:103]
ds_read_b64_tr_b16 v[238:239], v0 offset:12544
v_lshrrev_b32 v5, 4, v2
v_mfma_f32_16x16x32_bf16 v[100:103], v[244:247], v[28:31], v[100:103]
v_mov_b32 v10, s64
v_lshlrev_b32 v5, 28, v5
v_mfma_f32_16x16x32_bf16 v[104:107], v[240:243], v[32:35], v[104:107]
v_mov_b32 v11, s65
v_lshl_add_u32 v5, v2, 3, v5
v_mfma_f32_16x16x32_bf16 v[104:107], v[244:247], v[36:39], v[104:107]
buffer_store_dwordx4 v[216:219], v1, s[52:55], s0 offen offset:0 sc1
v_mfma_f32_16x16x32_bf16 v[108:111], v[240:243], v[40:43], v[108:111]
ds_read_b64_tr_b16 v[240:241], v3 offset:8192
v_cvt_pk_bf16_f32 v216, v208, v209
v_mfma_f32_16x16x32_bf16 v[108:111], v[244:247], v[44:47], v[108:111]
s_waitcnt lgkmcnt(1)
ds_read_b64_tr_b16 v[242:243], v3 offset:8448
v_cvt_pk_bf16_f32 v217, v210, v211
v_mfma_f32_16x16x32_bf16 v[112:115], v[248:251], v[16:19], v[112:115]
ds_read_b64_tr_b16 v[244:245], v3 offset:12288
v_accvgpr_read_b32 v208, a148
v_mfma_f32_16x16x32_bf16 v[112:115], v[252:255], v[20:23], v[112:115]
ds_read_b64_tr_b16 v[246:247], v3 offset:12544
v_accvgpr_read_b32 v209, a149
v_mfma_f32_16x16x32_bf16 v[116:119], v[248:251], v[24:27], v[116:119]
v_accvgpr_read_b32 v210, a150
v_accvgpr_read_b32 v211, a151
v_mfma_f32_16x16x32_bf16 v[116:119], v[252:255], v[28:31], v[116:119]
v_cvt_pk_bf16_f32 v218, v212, v213
v_cvt_pk_bf16_f32 v219, v214, v215
v_mfma_f32_16x16x32_bf16 v[120:123], v[248:251], v[32:35], v[120:123]
buffer_load_dwordx4 a[104:107], v4, s[52:55], s87 offen offset:0 sc1
v_mfma_f32_16x16x32_bf16 v[120:123], v[252:255], v[36:39], v[120:123]
v_accvgpr_read_b32 v212, a180
v_accvgpr_read_b32 v213, a181
v_mfma_f32_16x16x32_bf16 v[124:127], v[248:251], v[40:43], v[124:127]
ds_read_b64_tr_b16 v[248:249], v0 offset:8704
v_accvgpr_read_b32 v214, a182
v_mfma_f32_16x16x32_bf16 v[124:127], v[252:255], v[44:47], v[124:127]
ds_read_b64_tr_b16 v[250:251], v0 offset:8960
v_accvgpr_read_b32 v215, a183
v_mfma_f32_16x16x32_bf16 v[128:131], v[232:235], v[16:19], v[128:131]
buffer_load_dwordx4 a[108:111], v4, s[52:55], s87 offen offset:1024 sc1
v_mfma_f32_16x16x32_bf16 v[128:131], v[236:239], v[20:23], v[128:131]
s_cmp_eq_u32 s60, 3
s_cselect_b32 s93, s93, 1073741824
ds_read_b64_tr_b16 v[252:253], v0 offset:12800
v_mfma_f32_16x16x32_bf16 v[132:135], v[232:235], v[24:27], v[132:135]
ds_read_b64_tr_b16 v[254:255], v0 offset:13056
v_accvgpr_write_b32 a0, 0
v_mfma_f32_16x16x32_bf16 v[132:135], v[236:239], v[28:31], v[132:135]
v_accvgpr_write_b32 a1, 0
v_accvgpr_write_b32 a2, 0
v_mfma_f32_16x16x32_bf16 v[136:139], v[232:235], v[32:35], v[136:139]
v_accvgpr_write_b32 a3, 0
v_accvgpr_write_b32 a4, 0
v_mfma_f32_16x16x32_bf16 v[136:139], v[236:239], v[36:39], v[136:139]
v_accvgpr_write_b32 a5, 0
v_accvgpr_write_b32 a6, 0
v_mfma_f32_16x16x32_bf16 v[140:143], v[232:235], v[40:43], v[140:143]
buffer_load_dwordx4 a[112:115], v4, s[52:55], s88 offen offset:0 sc1
v_mfma_f32_16x16x32_bf16 v[140:143], v[236:239], v[44:47], v[140:143]
s_waitcnt lgkmcnt(0)
ds_read_b64_tr_b16 v[232:233], v3 offset:8704
v_accvgpr_write_b32 a7, 0
v_mfma_f32_16x16x32_bf16 v[144:147], v[240:243], v[16:19], v[144:147]
ds_read_b64_tr_b16 v[234:235], v3 offset:8960
v_accvgpr_write_b32 a8, 0
v_mfma_f32_16x16x32_bf16 v[144:147], v[244:247], v[20:23], v[144:147]
ds_read_b64_tr_b16 v[236:237], v3 offset:12800
v_accvgpr_write_b32 a9, 0
v_mfma_f32_16x16x32_bf16 v[148:151], v[240:243], v[24:27], v[148:151]
ds_read_b64_tr_b16 v[238:239], v3 offset:13056
v_accvgpr_write_b32 a10, 0
v_mfma_f32_16x16x32_bf16 v[148:151], v[244:247], v[28:31], v[148:151]
buffer_store_dwordx4 v[220:223], v1, s[52:55], s0 offen offset:1024 sc1
v_mfma_f32_16x16x32_bf16 v[152:155], v[240:243], v[32:35], v[152:155]
s_add_u32 s0, s4, 909312
v_cvt_pk_bf16_f32 v220, v208, v209
v_cvt_pk_bf16_f32 v221, v210, v211
v_mfma_f32_16x16x32_bf16 v[152:155], v[244:247], v[36:39], v[152:155]
v_accvgpr_read_b32 v208, a152
v_accvgpr_read_b32 v209, a153
v_mfma_f32_16x16x32_bf16 v[156:159], v[240:243], v[40:43], v[156:159]
v_accvgpr_read_b32 v210, a154
v_accvgpr_read_b32 v211, a155
v_mfma_f32_16x16x32_bf16 v[156:159], v[244:247], v[44:47], v[156:159]
v_cvt_pk_bf16_f32 v222, v212, v213
v_cvt_pk_bf16_f32 v223, v214, v215
v_mfma_f32_16x16x32_bf16 v[160:163], v[248:251], v[16:19], v[160:163]
v_accvgpr_read_b32 v212, a184
v_accvgpr_read_b32 v213, a185
v_mfma_f32_16x16x32_bf16 v[160:163], v[252:255], v[20:23], v[160:163]
v_accvgpr_read_b32 v214, a186
v_accvgpr_read_b32 v215, a187
v_mfma_f32_16x16x32_bf16 v[164:167], v[248:251], v[24:27], v[164:167]
buffer_load_dwordx4 a[116:119], v4, s[52:55], s88 offen offset:1024 sc1
v_mfma_f32_16x16x32_bf16 v[164:167], v[252:255], v[28:31], v[164:167]
s_waitcnt lgkmcnt(0)
v_accvgpr_write_b32 a11, 0
v_accvgpr_write_b32 a12, 0
v_mfma_f32_16x16x32_bf16 v[168:171], v[248:251], v[32:35], v[168:171]
s_barrier
ds_read_b64_tr_b16 v[240:241], v0 offset:16384
v_accvgpr_write_b32 a13, 0
v_mfma_f32_16x16x32_bf16 v[168:171], v[252:255], v[36:39], v[168:171]
ds_read_b64_tr_b16 v[242:243], v0 offset:16640
v_accvgpr_write_b32 a14, 0
v_mfma_f32_16x16x32_bf16 v[172:175], v[248:251], v[40:43], v[172:175]
buffer_load_dwordx4 a[120:123], v4, s[52:55], s89 offen offset:0 sc1
v_mfma_f32_16x16x32_bf16 v[172:175], v[252:255], v[44:47], v[172:175]
ds_read_b64_tr_b16 v[244:245], v0 offset:20480
v_accvgpr_write_b32 a15, 0
v_mfma_f32_16x16x32_bf16 v[176:179], v[232:235], v[16:19], v[176:179]
ds_read_b64_tr_b16 v[246:247], v0 offset:20736
v_mov_b32 v16, 0
v_mfma_f32_16x16x32_bf16 v[176:179], v[236:239], v[20:23], v[176:179]
ds_read_b64_tr_b16 v[248:249], v3 offset:16384
v_mov_b32 v17, 0
v_mfma_f32_16x16x32_bf16 v[180:183], v[232:235], v[24:27], v[180:183]
buffer_load_dwordx2 v[8:9], v6, s[52:55], 0 offen sc1
v_mfma_f32_16x16x32_bf16 v[180:183], v[236:239], v[28:31], v[180:183]
ds_read_b64_tr_b16 v[250:251], v3 offset:16640
v_mov_b32 v18, 0
v_mfma_f32_16x16x32_bf16 v[184:187], v[232:235], v[32:35], v[184:187]
ds_read_b64_tr_b16 v[252:253], v3 offset:20480
v_mov_b32 v19, 0
v_mfma_f32_16x16x32_bf16 v[184:187], v[236:239], v[36:39], v[184:187]
ds_read_b64_tr_b16 v[254:255], v3 offset:20736
v_mov_b32 v20, 0
v_mfma_f32_16x16x32_bf16 v[188:191], v[232:235], v[40:43], v[188:191]
buffer_load_dwordx4 a[124:127], v4, s[52:55], s89 offen offset:1024 sc1
v_mfma_f32_16x16x32_bf16 v[188:191], v[236:239], v[44:47], v[188:191]
)ASM"
R"ASM(
s_waitcnt vmcnt(29) lgkmcnt(3)
ds_read_b64_tr_b16 v[232:233], v0 offset:16896
v_add_u32 v4, s94, v1
v_mfma_f32_16x16x32_bf16 v[192:195], v[240:243], v[48:51], v[192:195]
s_add_u32 s94, s62, 9
s_sub_u32 s5, s94, 10
ds_read_b64_tr_b16 v[234:235], v0 offset:17152
v_mfma_f32_16x16x32_bf16 v[192:195], v[244:247], v[52:55], v[192:195]
s_min_u32 s94, s94, s5
s_lshl_b32 s94, s94, 11
ds_read_b64_tr_b16 v[236:237], v0 offset:20992
v_mfma_f32_16x16x32_bf16 v[196:199], v[240:243], v[56:59], v[196:199]
s_waitcnt vmcnt(28)
buffer_store_dwordx4 v[224:227], v1, s[52:55], s1 offen offset:0 sc1
v_mfma_f32_16x16x32_bf16 v[196:199], v[244:247], v[60:63], v[196:199]
s_waitcnt vmcnt(27)
ds_read_b64_tr_b16 v[238:239], v0 offset:21248
v_cvt_pk_bf16_f32 v224, v208, v209
v_mfma_f32_16x16x32_bf16 v[200:203], v[240:243], v[64:67], v[200:203]
s_waitcnt vmcnt(25)
v_cvt_pk_bf16_f32 v225, v210, v211
v_accvgpr_read_b32 v208, a156
v_mfma_f32_16x16x32_bf16 v[200:203], v[244:247], v[68:71], v[200:203]
s_waitcnt vmcnt(24)
v_accvgpr_read_b32 v209, a157
v_accvgpr_read_b32 v210, a158
v_mfma_f32_16x16x32_bf16 v[204:207], v[240:243], v[72:75], v[204:207]
s_waitcnt vmcnt(23)
ds_read_b64_tr_b16 v[240:241], v3 offset:16896
v_accvgpr_read_b32 v211, a159
v_mfma_f32_16x16x32_bf16 v[204:207], v[244:247], v[76:79], v[204:207]
s_waitcnt lgkmcnt(2)
ds_read_b64_tr_b16 v[242:243], v3 offset:17152
v_cvt_pk_bf16_f32 v226, v212, v213
v_mfma_f32_16x16x32_bf16 v[80:83], v[248:251], v[48:51], v[80:83]
ds_read_b64_tr_b16 v[244:245], v3 offset:20992
v_cvt_pk_bf16_f32 v227, v214, v215
v_mfma_f32_16x16x32_bf16 v[80:83], v[252:255], v[52:55], v[80:83]
buffer_load_dwordx4 a[128:131], v4, s[52:55], s86 offen offset:0 sc1
v_mfma_f32_16x16x32_bf16 v[84:87], v[248:251], v[56:59], v[84:87]
ds_read_b64_tr_b16 v[246:247], v3 offset:21248
v_accvgpr_read_b32 v212, a188
v_mfma_f32_16x16x32_bf16 v[84:87], v[252:255], v[60:63], v[84:87]
v_accvgpr_read_b32 v213, a189
v_accvgpr_read_b32 v214, a190
v_mfma_f32_16x16x32_bf16 v[88:91], v[248:251], v[64:67], v[88:91]
v_accvgpr_read_b32 v215, a191
v_mov_b32 v21, 0
v_mfma_f32_16x16x32_bf16 v[88:91], v[252:255], v[68:71], v[88:91]
buffer_load_dwordx4 a[132:135], v4, s[52:55], s86 offen offset:1024 sc1
v_mfma_f32_16x16x32_bf16 v[92:95], v[248:251], v[72:75], v[92:95]
ds_read_b64_tr_b16 v[248:249], v0 offset:24576
v_mov_b32 v22, 0
v_mfma_f32_16x16x32_bf16 v[92:95], v[252:255], v[76:79], v[92:95]
ds_read_b64_tr_b16 v[250:251], v0 offset:24832
v_mov_b32 v23, 0
v_mfma_f32_16x16x32_bf16 v[96:99], v[232:235], v[48:51], v[96:99]
s_waitcnt lgkmcnt(2)
ds_read_b64_tr_b16 v[252:253], v0 offset:28672
v_mov_b32 v24, 0
v_mfma_f32_16x16x32_bf16 v[96:99], v[236:239], v[52:55], v[96:99]
buffer_load_dwordx4 a[136:139], v4, s[52:55], s87 offen offset:0 sc1
v_mfma_f32_16x16x32_bf16 v[100:103], v[232:235], v[56:59], v[100:103]
ds_read_b64_tr_b16 v[254:255], v0 offset:28928
v_mov_b32 v25, 0
v_mfma_f32_16x16x32_bf16 v[100:103], v[236:239], v[60:63], v[100:103]
v_mov_b32 v26, 0
v_mov_b32 v27, 0
v_mfma_f32_16x16x32_bf16 v[104:107], v[232:235], v[64:67], v[104:107]
v_mov_b32 v28, 0
v_mov_b32 v29, 0
v_mfma_f32_16x16x32_bf16 v[104:107], v[236:239], v[68:71], v[104:107]
v_mov_b32 v30, 0
v_mov_b32 v31, 0
v_mfma_f32_16x16x32_bf16 v[108:111], v[232:235], v[72:75], v[108:111]
ds_read_b64_tr_b16 v[232:233], v3 offset:24576
v_mov_b32 v32, 0
v_mfma_f32_16x16x32_bf16 v[108:111], v[236:239], v[76:79], v[108:111]
buffer_load_dwordx4 a[140:143], v4, s[52:55], s87 offen offset:1024 sc1
v_mfma_f32_16x16x32_bf16 v[112:115], v[240:243], v[48:51], v[112:115]
ds_read_b64_tr_b16 v[234:235], v3 offset:24832
v_mov_b32 v33, 0
v_mfma_f32_16x16x32_bf16 v[112:115], v[244:247], v[52:55], v[112:115]
ds_read_b64_tr_b16 v[236:237], v3 offset:28672
v_mov_b32 v34, 0
v_mfma_f32_16x16x32_bf16 v[116:119], v[240:243], v[56:59], v[116:119]
ds_read_b64_tr_b16 v[238:239], v3 offset:28928
v_mov_b32 v35, 0
v_mfma_f32_16x16x32_bf16 v[116:119], v[244:247], v[60:63], v[116:119]
buffer_store_dwordx4 v[228:231], v1, s[52:55], s1 offen offset:1024 sc1
v_mfma_f32_16x16x32_bf16 v[120:123], v[240:243], v[64:67], v[120:123]
s_add_u32 s1, s4, 913408
v_cvt_pk_bf16_f32 v228, v208, v209
v_cvt_pk_bf16_f32 v229, v210, v211
v_mfma_f32_16x16x32_bf16 v[120:123], v[244:247], v[68:71], v[120:123]
v_cvt_pk_bf16_f32 v230, v212, v213
v_cvt_pk_bf16_f32 v231, v214, v215
v_mfma_f32_16x16x32_bf16 v[124:127], v[240:243], v[72:75], v[124:127]
ds_read_b64_tr_b16 v[240:241], v0 offset:25088
v_mov_b32 v36, 0
v_mfma_f32_16x16x32_bf16 v[124:127], v[244:247], v[76:79], v[124:127]
s_waitcnt lgkmcnt(1)
ds_read_b64_tr_b16 v[242:243], v0 offset:25344
v_mov_b32 v37, 0
v_mfma_f32_16x16x32_bf16 v[128:131], v[248:251], v[48:51], v[128:131]
ds_read_b64_tr_b16 v[244:245], v0 offset:29184
v_mov_b32 v38, 0
v_mfma_f32_16x16x32_bf16 v[128:131], v[252:255], v[52:55], v[128:131]
ds_read_b64_tr_b16 v[246:247], v0 offset:29440
v_add_u32 v0, 0xffff4000, v0
v_mfma_f32_16x16x32_bf16 v[132:135], v[248:251], v[56:59], v[132:135]
buffer_load_dwordx4 a[144:147], v4, s[52:55], s88 offen offset:0 sc1
v_mfma_f32_16x16x32_bf16 v[132:135], v[252:255], v[60:63], v[132:135]
v_mov_b32 v39, 0
v_mov_b32 v40, 0
v_mfma_f32_16x16x32_bf16 v[136:139], v[248:251], v[64:67], v[136:139]
v_mov_b32 v41, 0
v_mov_b32 v42, 0
v_mfma_f32_16x16x32_bf16 v[136:139], v[252:255], v[68:71], v[136:139]
v_mov_b32 v43, 0
v_mov_b32 v44, 0
v_mfma_f32_16x16x32_bf16 v[140:143], v[248:251], v[72:75], v[140:143]
buffer_load_dwordx4 a[148:151], v4, s[52:55], s88 offen offset:1024 sc1
v_mfma_f32_16x16x32_bf16 v[140:143], v[252:255], v[76:79], v[140:143]
ds_read_b64_tr_b16 v[248:249], v3 offset:25088
v_mov_b32 v45, 0
v_mfma_f32_16x16x32_bf16 v[144:147], v[232:235], v[48:51], v[144:147]
ds_read_b64_tr_b16 v[250:251], v3 offset:25344
v_mov_b32 v46, 0
v_mfma_f32_16x16x32_bf16 v[144:147], v[236:239], v[52:55], v[144:147]
L_pad_a_2_1:
s_cbranch_execz L_pad_a_2_2
ds_read_b64_tr_b16 v[252:253], v3 offset:29184
v_mov_b32 v47, 0
v_mfma_f32_16x16x32_bf16 v[148:151], v[232:235], v[56:59], v[148:151]
ds_read_b64_tr_b16 v[254:255], v3 offset:29440
v_add_u32 v3, 0xffff4000, v3
v_mfma_f32_16x16x32_bf16 v[148:151], v[236:239], v[60:63], v[148:151]
v_accvgpr_write_b32 a16, 0
v_accvgpr_write_b32 a17, 0
v_mfma_f32_16x16x32_bf16 v[152:155], v[232:235], v[64:67], v[152:155]
buffer_load_dwordx4 a[152:155], v4, s[52:55], s89 offen offset:0 sc1
v_mfma_f32_16x16x32_bf16 v[152:155], v[236:239], v[68:71], v[152:155]
v_accvgpr_write_b32 a18, 0
v_accvgpr_write_b32 a19, 0
v_mfma_f32_16x16x32_bf16 v[156:159], v[232:235], v[72:75], v[156:159]
v_accvgpr_write_b32 a20, 0
v_accvgpr_write_b32 a21, 0
v_mfma_f32_16x16x32_bf16 v[156:159], v[236:239], v[76:79], v[156:159]
s_waitcnt lgkmcnt(0)
v_accvgpr_write_b32 a22, 0
v_accvgpr_write_b32 a23, 0
v_mfma_f32_16x16x32_bf16 v[160:163], v[240:243], v[48:51], v[160:163]
buffer_store_dwordx4 v[216:219], v1, s[52:55], s2 offen offset:0 sc1
v_mfma_f32_16x16x32_bf16 v[160:163], v[244:247], v[52:55], v[160:163]
v_accvgpr_write_b32 a24, 0
v_accvgpr_write_b32 a25, 0
v_mfma_f32_16x16x32_bf16 v[164:167], v[240:243], v[56:59], v[164:167]
v_accvgpr_write_b32 a26, 0
v_accvgpr_write_b32 a27, 0
v_mfma_f32_16x16x32_bf16 v[164:167], v[244:247], v[60:63], v[164:167]
v_accvgpr_write_b32 a28, 0
v_accvgpr_write_b32 a29, 0
v_mfma_f32_16x16x32_bf16 v[168:171], v[240:243], v[64:67], v[168:171]
s_barrier
ds_read_b64_tr_b16 v[232:233], v0 offset:0
v_accvgpr_write_b32 a30, 0
v_mfma_f32_16x16x32_bf16 v[168:171], v[244:247], v[68:71], v[168:171]
ds_read_b64_tr_b16 v[234:235], v0 offset:256
v_accvgpr_write_b32 a31, 0
v_mfma_f32_16x16x32_bf16 v[172:175], v[240:243], v[72:75], v[172:175]
ds_read_b64_tr_b16 v[236:237], v0 offset:4096
v_accvgpr_write_b32 a32, 0
v_mfma_f32_16x16x32_bf16 v[172:175], v[244:247], v[76:79], v[172:175]
buffer_load_dwordx4 a[156:159], v4, s[52:55], s89 offen offset:1024 sc1
v_mfma_f32_16x16x32_bf16 v[176:179], v[248:251], v[48:51], v[176:179]
ds_read_b64_tr_b16 v[238:239], v0 offset:4352
v_add_u32 v4, s94, v1
v_mfma_f32_16x16x32_bf16 v[176:179], v[252:255], v[52:55], v[176:179]
ds_read_b64_tr_b16 v[240:241], v3 offset:0
v_mov_b32 v48, 0
v_mfma_f32_16x16x32_bf16 v[180:183], v[248:251], v[56:59], v[180:183]
ds_read_b64_tr_b16 v[242:243], v3 offset:256
v_mov_b32 v49, 0
v_mfma_f32_16x16x32_bf16 v[180:183], v[252:255], v[60:63], v[180:183]
buffer_load_dwordx4 a[160:163], v4, s[52:55], s86 offen offset:0 sc1
v_mfma_f32_16x16x32_bf16 v[184:187], v[248:251], v[64:67], v[184:187]
ds_read_b64_tr_b16 v[244:245], v3 offset:4096
v_mov_b32 v50, 0
v_mfma_f32_16x16x32_bf16 v[184:187], v[252:255], v[68:71], v[184:187]
ds_read_b64_tr_b16 v[246:247], v3 offset:4352
v_mov_b32 v51, 0
v_mfma_f32_16x16x32_bf16 v[188:191], v[248:251], v[72:75], v[188:191]
ds_read_b64_tr_b16 v[248:249], v0 offset:512
v_mov_b32 v52, 0
v_mfma_f32_16x16x32_bf16 v[188:191], v[252:255], v[76:79], v[188:191]
s_waitcnt vmcnt(32) lgkmcnt(3)
ds_read_b64_tr_b16 v[250:251], v0 offset:768
v_mov_b32 v53, 0
v_mfma_f32_16x16x32_bf16 v[192:195], v[232:235], a[64:67], v[192:195]
s_waitcnt vmcnt(28)
ds_read_b64_tr_b16 v[252:253], v0 offset:4608
v_mov_b32 v54, 0
v_mfma_f32_16x16x32_bf16 v[192:195], v[236:239], a[68:71], v[192:195]
buffer_load_dwordx4 a[164:167], v4, s[52:55], s86 offen offset:1024 sc1
v_mfma_f32_16x16x32_bf16 v[196:199], v[232:235], a[72:75], v[196:199]
s_mov_b32 s86, 3456
ds_read_b64_tr_b16 v[254:255], v0 offset:4864
v_mov_b32 v55, 0
v_mfma_f32_16x16x32_bf16 v[196:199], v[236:239], a[76:79], v[196:199]
s_waitcnt vmcnt(28)
v_mov_b32 v56, 0
v_mov_b32 v57, 0
v_mfma_f32_16x16x32_bf16 v[200:203], v[232:235], a[80:83], v[200:203]
s_waitcnt vmcnt(27)
v_mov_b32 v58, 0
v_mov_b32 v59, 0
v_mfma_f32_16x16x32_bf16 v[200:203], v[236:239], a[84:87], v[200:203]
s_waitcnt vmcnt(26)
v_mov_b32 v60, 0
v_mov_b32 v61, 0
v_mfma_f32_16x16x32_bf16 v[204:207], v[232:235], a[88:91], v[204:207]
s_waitcnt vmcnt(24) lgkmcnt(0)
ds_read_b64_tr_b16 v[232:233], v3 offset:512
v_mov_b32 v62, 0
v_mfma_f32_16x16x32_bf16 v[204:207], v[236:239], a[92:95], v[204:207]
buffer_store_dwordx4 v[220:223], v1, s[52:55], s2 offen offset:2048 sc1
v_mfma_f32_16x16x32_bf16 v[80:83], v[240:243], a[64:67], v[80:83]
s_add_u32 s2, s82, 909312
ds_read_b64_tr_b16 v[234:235], v3 offset:768
v_mov_b32 v63, 0
v_mfma_f32_16x16x32_bf16 v[80:83], v[244:247], a[68:71], v[80:83]
ds_read_b64_tr_b16 v[236:237], v3 offset:4608
v_mov_b32 v64, 0
v_mfma_f32_16x16x32_bf16 v[84:87], v[240:243], a[72:75], v[84:87]
ds_read_b64_tr_b16 v[238:239], v3 offset:4864
v_mov_b32 v65, 0
v_mfma_f32_16x16x32_bf16 v[84:87], v[244:247], a[76:79], v[84:87]
v_mov_b32 v66, 0
v_mov_b32 v67, 0
v_mfma_f32_16x16x32_bf16 v[88:91], v[240:243], a[80:83], v[88:91]
v_mov_b32 v68, 0
v_mov_b32 v69, 0
v_mfma_f32_16x16x32_bf16 v[88:91], v[244:247], a[84:87], v[88:91]
v_mov_b32 v70, 0
v_mov_b32 v71, 0
v_mfma_f32_16x16x32_bf16 v[92:95], v[240:243], a[88:91], v[92:95]
buffer_load_dwordx4 a[168:171], v4, s[52:55], s87 offen offset:0 sc1
v_mfma_f32_16x16x32_bf16 v[92:95], v[244:247], a[92:95], v[92:95]
ds_read_b64_tr_b16 v[240:241], v0 offset:8192
v_mov_b32 v72, 0
v_mfma_f32_16x16x32_bf16 v[96:99], v[248:251], a[64:67], v[96:99]
ds_read_b64_tr_b16 v[242:243], v0 offset:8448
v_mov_b32 v73, 0
v_mfma_f32_16x16x32_bf16 v[96:99], v[252:255], a[68:71], v[96:99]
ds_read_b64_tr_b16 v[244:245], v0 offset:12288
v_mov_b32 v74, 0
v_mfma_f32_16x16x32_bf16 v[100:103], v[248:251], a[72:75], v[100:103]
buffer_load_dwordx4 a[172:175], v4, s[52:55], s87 offen offset:1024 sc1
v_mfma_f32_16x16x32_bf16 v[100:103], v[252:255], a[76:79], v[100:103]
s_mov_b32 s87, 17825792
ds_read_b64_tr_b16 v[246:247], v0 offset:12544
v_mov_b32 v75, 0
v_mfma_f32_16x16x32_bf16 v[104:107], v[248:251], a[80:83], v[104:107]
v_mov_b32 v76, 0
v_mov_b32 v77, 0
v_mfma_f32_16x16x32_bf16 v[104:107], v[252:255], a[84:87], v[104:107]
v_mov_b32 v78, 0
v_mov_b32 v79, 0
v_mfma_f32_16x16x32_bf16 v[108:111], v[248:251], a[88:91], v[108:111]
buffer_load_dwordx4 a[176:179], v4, s[52:55], s88 offen offset:0 sc1
v_mfma_f32_16x16x32_bf16 v[108:111], v[252:255], a[92:95], v[108:111]
s_waitcnt lgkmcnt(1)
ds_read_b64_tr_b16 v[248:249], v3 offset:8192
v_accvgpr_write_b32 a33, 0
v_mfma_f32_16x16x32_bf16 v[112:115], v[232:235], a[64:67], v[112:115]
ds_read_b64_tr_b16 v[250:251], v3 offset:8448
v_accvgpr_write_b32 a34, 0
v_mfma_f32_16x16x32_bf16 v[112:115], v[236:239], a[68:71], v[112:115]
ds_read_b64_tr_b16 v[252:253], v3 offset:12288
v_accvgpr_write_b32 a35, 0
v_mfma_f32_16x16x32_bf16 v[116:119], v[232:235], a[72:75], v[116:119]
ds_read_b64_tr_b16 v[254:255], v3 offset:12544
v_accvgpr_write_b32 a36, 0
v_mfma_f32_16x16x32_bf16 v[116:119], v[236:239], a[76:79], v[116:119]
v_accvgpr_write_b32 a37, 0
v_accvgpr_write_b32 a38, 0
v_mfma_f32_16x16x32_bf16 v[120:123], v[232:235], a[80:83], v[120:123]
v_accvgpr_write_b32 a39, 0
v_accvgpr_write_b32 a40, 0
v_mfma_f32_16x16x32_bf16 v[120:123], v[236:239], a[84:87], v[120:123]
buffer_load_dwordx4 a[180:183], v4, s[52:55], s88 offen offset:1024 sc1
v_mfma_f32_16x16x32_bf16 v[124:127], v[232:235], a[88:91], v[124:127]
ds_read_b64_tr_b16 v[232:233], v0 offset:8704
v_accvgpr_write_b32 a41, 0
v_mfma_f32_16x16x32_bf16 v[124:127], v[236:239], a[92:95], v[124:127]
ds_read_b64_tr_b16 v[234:235], v0 offset:8960
v_accvgpr_write_b32 a42, 0
v_mfma_f32_16x16x32_bf16 v[128:131], v[240:243], a[64:67], v[128:131]
s_waitcnt lgkmcnt(2)
ds_read_b64_tr_b16 v[236:237], v0 offset:12800
v_accvgpr_write_b32 a43, 0
v_mfma_f32_16x16x32_bf16 v[128:131], v[244:247], a[68:71], v[128:131]
buffer_store_dwordx4 v[224:227], v1, s[52:55], s3 offen offset:0 sc1
v_mfma_f32_16x16x32_bf16 v[132:135], v[240:243], a[72:75], v[132:135]
ds_read_b64_tr_b16 v[238:239], v0 offset:13056
v_add_u32 v0, 0x18000, v0
v_mfma_f32_16x16x32_bf16 v[132:135], v[244:247], a[76:79], v[132:135]
v_accvgpr_write_b32 a44, 0
v_accvgpr_write_b32 a45, 0
v_mfma_f32_16x16x32_bf16 v[136:139], v[240:243], a[80:83], v[136:139]
s_waitcnt vmcnt(20)
v_cmp_ne_u64 vcc, s[64:65], v[8:9]
s_nop 0
s_cbranch_vccnz L_stub_kv0
L_ret_kv0:
v_mfma_f32_16x16x32_bf16 v[136:139], v[244:247], a[84:87], v[136:139]
v_lshrrev_b32 v8, 4, v2
v_lshlrev_b32 v8, 2, v8
v_mfma_f32_16x16x32_bf16 v[140:143], v[240:243], a[88:91], v[140:143]
ds_read_b64_tr_b16 v[240:241], v3 offset:8704
v_sub_u32 v8, v7, v8
v_mfma_f32_16x16x32_bf16 v[140:143], v[244:247], a[92:95], v[140:143]
ds_read_b64_tr_b16 v[242:243], v3 offset:8960
v_add_u32 v8, s77, v8
v_mfma_f32_16x16x32_bf16 v[144:147], v[248:251], a[64:67], v[144:147]
buffer_load_dwordx4 a[184:187], v4, s[52:55], s89 offen offset:0 sc1
v_mfma_f32_16x16x32_bf16 v[144:147], v[252:255], a[68:71], v[144:147]
ds_read_b64_tr_b16 v[244:245], v3 offset:12800
v_cvt_f32_i32 v8, v8
v_mfma_f32_16x16x32_bf16 v[148:151], v[248:251], a[72:75], v[148:151]
ds_read_b64_tr_b16 v[246:247], v3 offset:13056
v_add_u32 v3, 0x18000, v3
v_mfma_f32_16x16x32_bf16 v[148:151], v[252:255], a[76:79], v[148:151]
v_mul_f32 v8, 0x7149f2ca, v8
v_add_f32 v212, 0x70c9f2ca, v8
v_mfma_f32_16x16x32_bf16 v[152:155], v[248:251], a[80:83], v[152:155]
buffer_load_dwordx4 a[188:191], v4, s[52:55], s89 offen offset:1024 sc1
v_mfma_f32_16x16x32_bf16 v[152:155], v[252:255], a[84:87], v[152:155]
s_mov_b32 s89, 57344
v_add_f32 v213, 0xf0c9f2ca, v8
v_add_f32 v214, 0xf1977617, v8
v_mfma_f32_16x16x32_bf16 v[156:159], v[248:251], a[88:91], v[156:159]
v_min_f32 v212, 0, v212
v_add_f32 v215, 0xf1fc6f7c, v8
v_mfma_f32_16x16x32_bf16 v[156:159], v[252:255], a[92:95], v[156:159]
s_waitcnt lgkmcnt(0)
v_min_f32 v213, 0, v213
v_min_f32 v214, 0, v214
v_mfma_f32_16x16x32_bf16 v[160:163], v[232:235], a[64:67], v[160:163]
v_and_b32 v7, 3, v7
v_min_f32 v215, 0, v215
v_mfma_f32_16x16x32_bf16 v[160:163], v[236:239], a[68:71], v[160:163]
s_cmp_lt_u32 s78, 64
s_cselect_b32 s89, 136192, s89
s_cselect_b32 s87, 8388608, s87
v_mfma_f32_16x16x32_bf16 v[164:167], v[232:235], a[72:75], v[164:167]
s_mul_i32 s92, s92, s89
s_add_u32 s87, s87, s92
s_lshl_b32 s92, s100, 12
v_mfma_f32_16x16x32_bf16 v[164:167], v[236:239], a[76:79], v[164:167]
s_lshl_b32 s100, s100, 2
s_add_u32 s100, s100, s60
s_mul_i32 s100, s100, 40960
v_mfma_f32_16x16x32_bf16 v[168:171], v[232:235], a[80:83], v[168:171]
s_barrier
ds_read_b64_tr_b16 v[248:249], v0 offset:0
v_min_u32 v7, 1, v7
v_mfma_f32_16x16x32_bf16 v[168:171], v[236:239], a[84:87], v[168:171]
buffer_store_dwordx2 v[10:11], v5, s[52:55], s93 offen sc1
v_mfma_f32_16x16x32_bf16 v[172:175], v[232:235], a[88:91], v[172:175]
s_add_u32 m0, s66, 98304
s_add_u32 s67, s66, 0
ds_read_b64_tr_b16 v[250:251], v0 offset:256
v_mfma_f32_16x16x32_bf16 v[172:175], v[236:239], a[92:95], v[172:175]
s_add_u32 s84, s84, s100
ds_read_b64_tr_b16 v[252:253], v0 offset:4096
v_subrev_u32 v7, 1, v7
v_mfma_f32_16x16x32_bf16 v[176:179], v[240:243], a[64:67], v[176:179]
s_add_u32 s87, s87, s92
s_sub_u32 s100, s78, 92
ds_read_b64_tr_b16 v[254:255], v0 offset:4352
v_mfma_f32_16x16x32_bf16 v[176:179], v[244:247], a[68:71], v[176:179]
buffer_store_dwordx4 v[228:231], v1, s[52:55], s3 offen offset:2048 sc1
v_mfma_f32_16x16x32_bf16 v[180:183], v[240:243], a[72:75], v[180:183]
s_add_u32 s3, s82, 913408
s_mov_b32 s82, 2211840
ds_read_b64_tr_b16 v[232:233], v3 offset:0
v_mfma_f32_16x16x32_bf16 v[180:183], v[244:247], a[76:79], v[180:183]
s_lshl_b32 s88, s89, 2
s_mov_b32 s79, s89
ds_read_b64_tr_b16 v[234:235], v3 offset:256
v_mfma_f32_16x16x32_bf16 v[184:187], v[240:243], a[80:83], v[184:187]
s_lshl_b32 s92, s100, 7
ds_read_b64_tr_b16 v[236:237], v3 offset:4096
v_and_b32 v208, 0x3f803f80, v7
v_mfma_f32_16x16x32_bf16 v[184:187], v[244:247], a[84:87], v[184:187]
s_mul_i32 s89, s60, 110592
ds_read_b64_tr_b16 v[238:239], v3 offset:4352
v_lshrrev_b32 v4, 3, v2
)ASM"
R"ASM(
v_mfma_f32_16x16x32_bf16 v[188:191], v[240:243], a[88:91], v[188:191]
s_mul_i32 s100, s100, 81920
ds_read_b64_tr_b16 v[240:241], v0 offset:512
v_and_b32 v7, 7, v2
v_mfma_f32_16x16x32_bf16 v[188:191], v[244:247], a[92:95], v[188:191]
s_waitcnt lgkmcnt(4)
s_add_u32 s92, s92, s89
s_add_u32 s100, s100, 14680064
ds_read_b64_tr_b16 v[242:243], v0 offset:768
v_mfma_f32_16x16x32_bf16 v[192:195], v[248:251], a[96:99], v[192:195]
buffer_load_dwordx4 v1, s[56:59], s67 offen offset:0 sc1 lds
v_mfma_f32_16x16x32_bf16 v[192:195], v[252:255], a[100:103], v[192:195]
s_add_u32 s93, s93, 327680
ds_read_b64_tr_b16 v[244:245], v0 offset:4608
v_and_b32 v9, 3, v2
v_mfma_f32_16x16x32_bf16 v[196:199], v[248:251], a[104:107], v[196:199]
ds_read_b64_tr_b16 v[246:247], v0 offset:4864
v_add_f32 v216, 0xf343a333, v8
v_mfma_f32_16x16x32_bf16 v[196:199], v[252:255], a[108:111], v[196:199]
s_cmp_lt_u32 s78, 64
s_cselect_b32 s82, 5242880, s82
s_cselect_b64 s[80:81], s[28:29], s[36:37]
v_mfma_f32_16x16x32_bf16 v[200:203], v[248:251], a[112:115], v[200:203]
buffer_load_dwordx4 v1, s[56:59], s67 offen offset:1024 sc1 lds
v_mfma_f32_16x16x32_bf16 v[200:203], v[252:255], a[116:119], v[200:203]
s_cmp_lt_u32 s78, 92
s_cselect_b32 s86, 1280, s86
s_cselect_b32 s79, s79, 20480
s_cselect_b64 s[80:81], s[80:81], s[32:33]
v_mfma_f32_16x16x32_bf16 v[204:207], v[248:251], a[120:123], v[204:207]
s_waitcnt vmcnt(25) lgkmcnt(0)
ds_read_b64_tr_b16 v[248:249], v3 offset:512
v_mul_u32_u24 v4, s86, v4
v_mfma_f32_16x16x32_bf16 v[204:207], v[252:255], a[124:127], v[204:207]
s_lshl_b32 s86, s86, 3
ds_read_b64_tr_b16 v[250:251], v3 offset:768
v_lshl_add_u32 v4, v7, 4, v4
v_mfma_f32_16x16x32_bf16 v[80:83], v[232:235], a[96:99], v[80:83]
buffer_load_dwordx4 v1, s[56:59], s67 offen offset:2048 sc1 lds
v_mfma_f32_16x16x32_bf16 v[80:83], v[236:239], a[100:103], v[80:83]
s_sub_u32 s86, s86, 1024
ds_read_b64_tr_b16 v[252:253], v3 offset:4608
v_lshrrev_b32 v7, 2, v2
v_mfma_f32_16x16x32_bf16 v[84:87], v[232:235], a[104:107], v[84:87]
ds_read_b64_tr_b16 v[254:255], v3 offset:4864
v_lshlrev_b32 v7, 7, v7
v_mfma_f32_16x16x32_bf16 v[84:87], v[236:239], a[108:111], v[84:87]
s_cmp_lt_u32 s78, 92
s_cselect_b32 s84, s84, s92
s_cselect_b32 s87, s87, s100
v_mfma_f32_16x16x32_bf16 v[88:91], v[232:235], a[112:115], v[88:91]
buffer_store_dwordx4 a[192:195], v1, s[52:55], s0 offen offset:0 sc1
v_mfma_f32_16x16x32_bf16 v[88:91], v[236:239], a[116:119], v[88:91]
s_lshl_b32 s100, s60, 10
s_add_u32 s87, s87, s100
v_add_f32 v217, 0xf3504260, v8
v_mfma_f32_16x16x32_bf16 v[92:95], v[232:235], a[120:123], v[92:95]
s_cmp_lt_u32 s62, 4
s_cselect_b32 s100, 255, s78
ds_read_b64_tr_b16 v[232:233], v0 offset:8192
v_mfma_f32_16x16x32_bf16 v[92:95], v[236:239], a[124:127], v[92:95]
ds_read_b64_tr_b16 v[234:235], v0 offset:8448
v_add_f32 v218, 0xf35ce18d, v8
v_mfma_f32_16x16x32_bf16 v[96:99], v[240:243], a[96:99], v[96:99]
ds_read_b64_tr_b16 v[236:237], v0 offset:12288
v_add_f32 v219, 0xf36980b9, v8
v_mfma_f32_16x16x32_bf16 v[96:99], v[244:247], a[100:103], v[96:99]
ds_read_b64_tr_b16 v[238:239], v0 offset:12544
v_add_f32 v220, 0xf3c6caff, v8
v_mfma_f32_16x16x32_bf16 v[100:103], v[240:243], a[104:107], v[100:103]
s_cmp_lt_u32 s78, 92
s_cselect_b32 s85, 128, s85
s_cselect_b32 s88, s88, 4096
v_mfma_f32_16x16x32_bf16 v[100:103], v[244:247], a[108:111], v[100:103]
buffer_load_dwordx4 v1, s[56:59], s67 offen offset:3072 sc1 lds
v_mfma_f32_16x16x32_bf16 v[104:107], v[240:243], a[112:115], v[104:107]
s_add_u32 m0, s66, 0
s_add_u32 s67, s66, 37888
s_lshl_b32 s78, s78, 8
v_mfma_f32_16x16x32_bf16 v[104:107], v[244:247], a[116:119], v[104:107]
s_cmp_lt_u32 s100, 119
s_cselect_b32 s84, s84, 1073741824
s_cselect_b32 s87, s87, 1073741824
v_mfma_f32_16x16x32_bf16 v[108:111], v[240:243], a[120:123], v[108:111]
s_add_u32 s100, s90, 16384
ds_read_b64_tr_b16 v[240:241], v3 offset:8192
v_add_f32 v221, 0xf3cd1a95, v8
v_mfma_f32_16x16x32_bf16 v[108:111], v[244:247], a[124:127], v[108:111]
s_waitcnt lgkmcnt(1)
buffer_load_dwordx4 v1, s[56:59], s67 offen offset:0 sc1 lds
v_mfma_f32_16x16x32_bf16 v[112:115], v[248:251], a[96:99], v[112:115]
s_add_u32 s78, s78, 524288
ds_read_b64_tr_b16 v[242:243], v3 offset:8448
v_add_f32 v222, 0xf3d36a2b, v8
v_mfma_f32_16x16x32_bf16 v[112:115], v[252:255], a[100:103], v[112:115]
s_sub_u32 s87, s87, s88
ds_read_b64_tr_b16 v[244:245], v3 offset:12288
v_add_f32 v223, 0xf3d9b9c2, v8
v_mfma_f32_16x16x32_bf16 v[116:119], v[248:251], a[104:107], v[116:119]
ds_read_b64_tr_b16 v[246:247], v3 offset:12544
v_add_f32 v224, 0xf415e232, v8
v_mfma_f32_16x16x32_bf16 v[116:119], v[252:255], a[108:111], v[116:119]
buffer_load_dwordx4 v1, s[56:59], s67 offen offset:1024 sc1 lds
v_mfma_f32_16x16x32_bf16 v[120:123], v[248:251], a[112:115], v[120:123]
v_add_f32 v225, 0xf41909fd, v8
v_add_f32 v226, 0xf41c31c8, v8
v_mfma_f32_16x16x32_bf16 v[120:123], v[252:255], a[116:119], v[120:123]
v_add_f32 v227, 0xf41f5993, v8
v_lshl_add_u32 v7, v9, 3, v7
v_mfma_f32_16x16x32_bf16 v[124:127], v[248:251], a[120:123], v[124:127]
ds_read_b64_tr_b16 v[248:249], v0 offset:8704
v_add_u32 v6, 65536, v6
v_mfma_f32_16x16x32_bf16 v[124:127], v[252:255], a[124:127], v[124:127]
ds_read_b64_tr_b16 v[250:251], v0 offset:8960
v_accvgpr_write_b32 a46, 0
v_mfma_f32_16x16x32_bf16 v[128:131], v[232:235], a[96:99], v[128:131]
ds_read_b64_tr_b16 v[252:253], v0 offset:12800
v_accvgpr_write_b32 a47, 0
v_mfma_f32_16x16x32_bf16 v[128:131], v[236:239], a[100:103], v[128:131]
ds_read_b64_tr_b16 v[254:255], v0 offset:13056
v_accvgpr_write_b32 a48, 0
v_mfma_f32_16x16x32_bf16 v[132:135], v[232:235], a[104:107], v[132:135]
buffer_load_dwordx4 v1, s[56:59], s67 offen offset:2048 sc1 lds
v_mfma_f32_16x16x32_bf16 v[132:135], v[236:239], a[108:111], v[132:135]
v_accvgpr_write_b32 a49, 0
v_accvgpr_write_b32 a50, 0
v_mfma_f32_16x16x32_bf16 v[136:139], v[232:235], a[112:115], v[136:139]
v_accvgpr_write_b32 a51, 0
v_accvgpr_write_b32 a52, 0
v_mfma_f32_16x16x32_bf16 v[136:139], v[236:239], a[116:119], v[136:139]
v_accvgpr_write_b32 a53, 0
v_accvgpr_write_b32 a54, 0
v_mfma_f32_16x16x32_bf16 v[140:143], v[232:235], a[120:123], v[140:143]
buffer_store_dwordx4 a[200:203], v1, s[52:55], s0 offen offset:1024 sc1
v_mfma_f32_16x16x32_bf16 v[140:143], v[236:239], a[124:127], v[140:143]
s_waitcnt lgkmcnt(0)
ds_read_b64_tr_b16 v[232:233], v3 offset:8704
v_accvgpr_write_b32 a55, 0
v_mfma_f32_16x16x32_bf16 v[144:147], v[240:243], a[96:99], v[144:147]
ds_read_b64_tr_b16 v[234:235], v3 offset:8960
v_accvgpr_write_b32 a56, 0
v_mfma_f32_16x16x32_bf16 v[144:147], v[244:247], a[100:103], v[144:147]
ds_read_b64_tr_b16 v[236:237], v3 offset:12800
v_accvgpr_write_b32 a57, 0
v_mfma_f32_16x16x32_bf16 v[148:151], v[240:243], a[104:107], v[148:151]
ds_read_b64_tr_b16 v[238:239], v3 offset:13056
v_accvgpr_write_b32 a58, 0
v_mfma_f32_16x16x32_bf16 v[148:151], v[244:247], a[108:111], v[148:151]
v_accvgpr_write_b32 a59, 0
v_accvgpr_write_b32 a60, 0
v_mfma_f32_16x16x32_bf16 v[152:155], v[240:243], a[112:115], v[152:155]
v_accvgpr_write_b32 a61, 0
v_accvgpr_write_b32 a62, 0
v_mfma_f32_16x16x32_bf16 v[152:155], v[244:247], a[116:119], v[152:155]
buffer_load_dwordx4 v1, s[56:59], s67 offen offset:3072 sc1 lds
v_mfma_f32_16x16x32_bf16 v[156:159], v[240:243], a[120:123], v[156:159]
v_accvgpr_write_b32 a63, 0
v_mov_b32 v209, v208
v_mfma_f32_16x16x32_bf16 v[156:159], v[244:247], a[124:127], v[156:159]
v_mov_b32 v210, v208
v_mov_b32 v211, v208
v_mfma_f32_16x16x32_bf16 v[160:163], v[248:251], a[96:99], v[160:163]
v_min_f32 v216, 0, v216
v_min_f32 v217, 0, v217
v_mfma_f32_16x16x32_bf16 v[160:163], v[252:255], a[100:103], v[160:163]
v_min_f32 v218, 0, v218
v_min_f32 v219, 0, v219
v_mfma_f32_16x16x32_bf16 v[164:167], v[248:251], a[104:107], v[164:167]
v_min_f32 v220, 0, v220
v_min_f32 v221, 0, v221
v_mfma_f32_16x16x32_bf16 v[164:167], v[252:255], a[108:111], v[164:167]
s_waitcnt lgkmcnt(0)
v_min_f32 v222, 0, v222
v_min_f32 v223, 0, v223
v_mfma_f32_16x16x32_bf16 v[168:171], v[248:251], a[112:115], v[168:171]
s_barrier
ds_read_b64_tr_b16 v[240:241], v0 offset:16384
v_min_f32 v224, 0, v224
v_mfma_f32_16x16x32_bf16 v[168:171], v[252:255], a[116:119], v[168:171]
ds_read_b64_tr_b16 v[242:243], v0 offset:16640
v_min_f32 v225, 0, v225
v_mfma_f32_16x16x32_bf16 v[172:175], v[248:251], a[120:123], v[172:175]
s_add_u32 m0, s66, 32768
s_add_u32 s67, s66, 75776
ds_read_b64_tr_b16 v[244:245], v0 offset:20480
v_mfma_f32_16x16x32_bf16 v[172:175], v[252:255], a[124:127], v[172:175]
ds_read_b64_tr_b16 v[246:247], v0 offset:20736
v_min_f32 v226, 0, v226
v_mfma_f32_16x16x32_bf16 v[176:179], v[232:235], a[96:99], v[176:179]
buffer_load_dwordx4 v1, s[56:59], s67 offen offset:0 sc1 lds
v_mfma_f32_16x16x32_bf16 v[176:179], v[236:239], a[100:103], v[176:179]
ds_read_b64_tr_b16 v[248:249], v0 offset:32768
v_min_f32 v227, 0, v227
v_mfma_f32_16x16x32_bf16 v[180:183], v[232:235], a[104:107], v[180:183]
ds_read_b64_tr_b16 v[250:251], v0 offset:33024
v_add_u32 v7, s100, v7
v_mfma_f32_16x16x32_bf16 v[180:183], v[236:239], a[108:111], v[180:183]
s_cmp_eq_u32 s60, 3
s_cselect_b32 s78, s78, 1073741824
ds_read_b64_tr_b16 v[252:253], v0 offset:36864
v_mfma_f32_16x16x32_bf16 v[184:187], v[232:235], a[112:115], v[184:187]
buffer_store_dwordx4 a[224:227], v1, s[52:55], s1 offen offset:0 sc1
v_mfma_f32_16x16x32_bf16 v[184:187], v[236:239], a[116:119], v[184:187]
ds_read_b64_tr_b16 v[254:255], v0 offset:37120
v_mfma_f32_16x16x32_bf16 v[188:191], v[232:235], a[120:123], v[188:191]
ds_read_b64_tr_b16 v[232:233], v3 offset:16384
v_mfma_f32_16x16x32_bf16 v[188:191], v[236:239], a[124:127], v[188:191]
s_waitcnt vmcnt(33) lgkmcnt(3)
ds_read_b64_tr_b16 v[234:235], v3 offset:16640
v_mfma_f32_16x16x32_bf16 v[192:195], v[240:243], a[128:131], v[192:195]
s_waitcnt vmcnt(32)
ds_read_b64_tr_b16 v[236:237], v3 offset:20480
v_mfma_f32_16x16x32_bf16 v[192:195], v[244:247], a[132:135], v[192:195]
s_waitcnt vmcnt(31)
ds_read_b64_tr_b16 v[238:239], v3 offset:20736
v_mfma_f32_16x16x32_bf16 v[196:199], v[240:243], a[136:139], v[196:199]
s_waitcnt vmcnt(28)
v_mfma_f32_16x16x32_bf16 v[196:199], v[244:247], a[140:143], v[196:199]
buffer_load_dwordx4 v1, s[56:59], s67 offen offset:1024 sc1 lds
v_mfma_f32_16x16x32_bf16 v[200:203], v[240:243], a[144:147], v[200:203]
s_waitcnt vmcnt(28)
v_mfma_f32_16x16x32_bf16 v[200:203], v[244:247], a[148:151], v[200:203]
s_waitcnt vmcnt(27)
v_mfma_f32_16x16x32_bf16 v[204:207], v[240:243], a[152:155], v[204:207]
s_waitcnt vmcnt(24)
ds_read_b64_tr_b16 v[240:241], v3 offset:32768
v_mfma_f32_16x16x32_bf16 v[204:207], v[244:247], a[156:159], v[204:207]
buffer_load_dwordx4 v1, s[56:59], s67 offen offset:2048 sc1 lds
v_mfma_f32_16x16x32_bf16 v[192:195], v[248:251], a[160:163], v[192:195]
s_waitcnt vmcnt(24) lgkmcnt(1)
ds_read_b64_tr_b16 v[242:243], v3 offset:33024
v_mfma_f32_16x16x32_bf16 v[192:195], v[252:255], a[164:167], v[192:195]
s_waitcnt vmcnt(22)
ds_read_b64_tr_b16 v[244:245], v3 offset:36864
v_mfma_f32_16x16x32_bf16 v[196:199], v[248:251], a[168:171], v[196:199]
s_waitcnt vmcnt(21)
ds_read_b64_tr_b16 v[246:247], v3 offset:37120
v_mfma_f32_16x16x32_bf16 v[196:199], v[252:255], a[172:175], v[196:199]
s_waitcnt vmcnt(20)
v_mfma_f32_16x16x32_bf16 v[200:203], v[248:251], a[176:179], v[200:203]
s_waitcnt vmcnt(17)
v_mfma_f32_16x16x32_bf16 v[200:203], v[252:255], a[180:183], v[200:203]
buffer_load_dwordx4 v1, s[56:59], s67 offen offset:3072 sc1 lds
v_mfma_f32_16x16x32_bf16 v[204:207], v[248:251], a[184:187], v[204:207]
s_waitcnt vmcnt(17)
ds_read_b64_tr_b16 v[248:249], v0 offset:16896
v_cvt_pk_bf16_f32 v192, v192, v193
v_mfma_f32_16x16x32_bf16 v[204:207], v[252:255], a[188:191], v[204:207]
ds_read_b64_tr_b16 v[250:251], v0 offset:17152
v_cvt_pk_bf16_f32 v193, v194, v195
v_mfma_f32_16x16x32_bf16 v[80:83], v[232:235], a[128:131], v[80:83]
ds_read_b64_tr_b16 v[252:253], v0 offset:20992
v_accvgpr_write_b32 a64, v192
v_mfma_f32_16x16x32_bf16 v[80:83], v[236:239], a[132:135], v[80:83]
buffer_store_dwordx4 a[232:235], v1, s[52:55], s1 offen offset:1024 sc1
v_mfma_f32_16x16x32_bf16 v[84:87], v[232:235], a[136:139], v[84:87]
s_add_u32 m0, s66, 49152
s_add_u32 s67, s66, 16384
ds_read_b64_tr_b16 v[254:255], v0 offset:21248
v_mfma_f32_16x16x32_bf16 v[84:87], v[236:239], a[140:143], v[84:87]
v_accvgpr_write_b32 a65, v193
v_cvt_pk_bf16_f32 v196, v196, v197
v_mfma_f32_16x16x32_bf16 v[88:91], v[232:235], a[144:147], v[88:91]
v_accvgpr_write_b32 a80, v196
v_cvt_pk_bf16_f32 v197, v198, v199
v_mfma_f32_16x16x32_bf16 v[88:91], v[236:239], a[148:151], v[88:91]
v_accvgpr_write_b32 a81, v197
v_cvt_pk_bf16_f32 v200, v200, v201
v_mfma_f32_16x16x32_bf16 v[92:95], v[232:235], a[152:155], v[92:95]
ds_read_b64_tr_b16 v[232:233], v0 offset:33280
v_cvt_pk_bf16_f32 v201, v202, v203
v_mfma_f32_16x16x32_bf16 v[92:95], v[236:239], a[156:159], v[92:95]
s_waitcnt lgkmcnt(1)
ds_read_b64_tr_b16 v[234:235], v0 offset:33536
v_accvgpr_write_b32 a96, v200
v_mfma_f32_16x16x32_bf16 v[80:83], v[240:243], a[160:163], v[80:83]
buffer_load_dwordx4 v1, s[56:59], s67 offen offset:0 sc1 lds
v_mfma_f32_16x16x32_bf16 v[80:83], v[244:247], a[164:167], v[80:83]
ds_read_b64_tr_b16 v[236:237], v0 offset:37376
v_accvgpr_write_b32 a97, v201
v_mfma_f32_16x16x32_bf16 v[84:87], v[240:243], a[168:171], v[84:87]
ds_read_b64_tr_b16 v[238:239], v0 offset:37632
v_cvt_pk_bf16_f32 v204, v204, v205
v_mfma_f32_16x16x32_bf16 v[84:87], v[244:247], a[172:175], v[84:87]
v_accvgpr_write_b32 a112, v204
v_cvt_pk_bf16_f32 v205, v206, v207
v_mfma_f32_16x16x32_bf16 v[88:91], v[240:243], a[176:179], v[88:91]
buffer_load_dwordx4 v1, s[56:59], s67 offen offset:1024 sc1 lds
v_mfma_f32_16x16x32_bf16 v[88:91], v[244:247], a[180:183], v[88:91]
v_cvt_pk_bf16_f32 v80, v80, v81
v_cvt_pk_bf16_f32 v81, v82, v83
v_mfma_f32_16x16x32_bf16 v[92:95], v[240:243], a[184:187], v[92:95]
ds_read_b64_tr_b16 v[240:241], v3 offset:16896
v_accvgpr_write_b32 a66, v80
v_mfma_f32_16x16x32_bf16 v[92:95], v[244:247], a[188:191], v[92:95]
ds_read_b64_tr_b16 v[242:243], v3 offset:17152
v_accvgpr_write_b32 a67, v81
v_mfma_f32_16x16x32_bf16 v[96:99], v[248:251], a[128:131], v[96:99]
ds_read_b64_tr_b16 v[244:245], v3 offset:20992
v_mul_f32 v80, s71, v212
v_mfma_f32_16x16x32_bf16 v[96:99], v[252:255], a[132:135], v[96:99]
ds_read_b64_tr_b16 v[246:247], v3 offset:21248
v_mul_f32 v81, s71, v213
v_mfma_f32_16x16x32_bf16 v[100:103], v[248:251], a[136:139], v[100:103]
buffer_load_dwordx4 v1, s[56:59], s67 offen offset:2048 sc1 lds
v_mfma_f32_16x16x32_bf16 v[100:103], v[252:255], a[140:143], v[100:103]
v_mul_f32 v82, s71, v214
v_mul_f32 v83, s71, v215
v_mfma_f32_16x16x32_bf16 v[104:107], v[248:251], a[144:147], v[104:107]
v_cvt_pk_bf16_f32 v84, v84, v85
v_cvt_pk_bf16_f32 v85, v86, v87
v_mfma_f32_16x16x32_bf16 v[104:107], v[252:255], a[148:151], v[104:107]
v_accvgpr_write_b32 a82, v84
v_accvgpr_write_b32 a83, v85
v_mfma_f32_16x16x32_bf16 v[108:111], v[248:251], a[152:155], v[108:111]
L_pad_a_3_1:
s_cbranch_execz L_pad_a_3_2
ds_read_b64_tr_b16 v[248:249], v3 offset:33280
v_cvt_pk_bf16_f32 v88, v88, v89
v_mfma_f32_16x16x32_bf16 v[108:111], v[252:255], a[156:159], v[108:111]
s_waitcnt lgkmcnt(1)
ds_read_b64_tr_b16 v[250:251], v3 offset:33536
v_cvt_pk_bf16_f32 v89, v90, v91
v_mfma_f32_16x16x32_bf16 v[96:99], v[232:235], a[160:163], v[96:99]
buffer_store_dwordx4 a[208:211], v1, s[52:55], s2 offen offset:0 sc1
v_mfma_f32_16x16x32_bf16 v[96:99], v[236:239], a[164:167], v[96:99]
ds_read_b64_tr_b16 v[252:253], v3 offset:37376
v_accvgpr_write_b32 a98, v88
v_mfma_f32_16x16x32_bf16 v[100:103], v[232:235], a[168:171], v[100:103]
ds_read_b64_tr_b16 v[254:255], v3 offset:37632
v_accvgpr_write_b32 a99, v89
v_mfma_f32_16x16x32_bf16 v[100:103], v[236:239], a[172:175], v[100:103]
v_cvt_pk_bf16_f32 v92, v92, v93
v_cvt_pk_bf16_f32 v93, v94, v95
v_mfma_f32_16x16x32_bf16 v[104:107], v[232:235], a[176:179], v[104:107]
v_accvgpr_write_b32 a113, v205
v_accvgpr_write_b32 a114, v92
v_mfma_f32_16x16x32_bf16 v[104:107], v[236:239], a[180:183], v[104:107]
v_cvt_pk_bf16_f32 v96, v96, v97
v_cvt_pk_bf16_f32 v97, v98, v99
v_mfma_f32_16x16x32_bf16 v[108:111], v[232:235], a[184:187], v[108:111]
v_accvgpr_write_b32 a68, v96
v_accvgpr_write_b32 a69, v97
v_mfma_f32_16x16x32_bf16 v[108:111], v[236:239], a[188:191], v[108:111]
buffer_load_dwordx4 v1, s[56:59], s67 offen offset:3072 sc1 lds
v_mfma_f32_16x16x32_bf16 v[112:115], v[240:243], a[128:131], v[112:115]
v_cvt_pk_bf16_f32 v100, v100, v101
v_cvt_pk_bf16_f32 v101, v102, v103
v_mfma_f32_16x16x32_bf16 v[112:115], v[244:247], a[132:135], v[112:115]
v_accvgpr_write_b32 a84, v100
v_accvgpr_write_b32 a85, v101
v_mfma_f32_16x16x32_bf16 v[116:119], v[240:243], a[136:139], v[116:119]
v_cvt_pk_bf16_f32 v104, v104, v105
v_cvt_pk_bf16_f32 v105, v106, v107
v_mfma_f32_16x16x32_bf16 v[116:119], v[244:247], a[140:143], v[116:119]
s_waitcnt vmcnt(16) lgkmcnt(0)
v_accvgpr_write_b32 a100, v104
v_accvgpr_write_b32 a101, v105
v_mfma_f32_16x16x32_bf16 v[120:123], v[240:243], a[144:147], v[120:123]
s_barrier
ds_read_b64_tr_b16 v[232:233], v0 offset:24576
v_cvt_pk_bf16_f32 v108, v108, v109
v_mfma_f32_16x16x32_bf16 v[120:123], v[244:247], a[148:151], v[120:123]
ds_read_b64_tr_b16 v[234:235], v0 offset:24832
v_accvgpr_write_b32 a115, v93
v_mfma_f32_16x16x32_bf16 v[124:127], v[240:243], a[152:155], v[124:127]
s_add_u32 m0, s66, 65536
s_add_u32 s67, s66, 113664
ds_read_b64_tr_b16 v[236:237], v0 offset:28672
v_mfma_f32_16x16x32_bf16 v[124:127], v[244:247], a[156:159], v[124:127]
ds_read_b64_tr_b16 v[238:239], v0 offset:28928
v_cvt_pk_bf16_f32 v109, v110, v111
v_mfma_f32_16x16x32_bf16 v[112:115], v[248:251], a[160:163], v[112:115]
buffer_load_dwordx4 v1, s[56:59], s67 offen offset:0 sc1 lds
v_mfma_f32_16x16x32_bf16 v[112:115], v[252:255], a[164:167], v[112:115]
ds_read_b64_tr_b16 v[240:241], v0 offset:40960
v_accvgpr_write_b32 a116, v108
v_mfma_f32_16x16x32_bf16 v[116:119], v[248:251], a[168:171], v[116:119]
ds_read_b64_tr_b16 v[242:243], v0 offset:41216
v_accvgpr_write_b32 a117, v109
v_mfma_f32_16x16x32_bf16 v[116:119], v[252:255], a[172:175], v[116:119]
ds_read_b64_tr_b16 v[244:245], v0 offset:45056
v_mov_b32 v192, 0
v_mfma_f32_16x16x32_bf16 v[120:123], v[248:251], a[176:179], v[120:123]
ds_read_b64_tr_b16 v[246:247], v0 offset:45312
v_mov_b32 v193, 0
v_mfma_f32_16x16x32_bf16 v[120:123], v[252:255], a[180:183], v[120:123]
v_cvt_pk_bf16_f32 v112, v112, v113
v_cvt_pk_bf16_f32 v113, v114, v115
v_mfma_f32_16x16x32_bf16 v[124:127], v[248:251], a[184:187], v[124:127]
s_waitcnt lgkmcnt(2)
ds_read_b64_tr_b16 v[248:249], v3 offset:24576
v_accvgpr_write_b32 a70, v112
v_mfma_f32_16x16x32_bf16 v[124:127], v[252:255], a[188:191], v[124:127]
buffer_store_dwordx4 a[212:215], v1, s[52:55], s2 offen offset:2048 sc1
v_mfma_f32_16x16x32_bf16 v[128:131], v[232:235], a[128:131], v[128:131]
ds_read_b64_tr_b16 v[250:251], v3 offset:24832
v_accvgpr_write_b32 a71, v113
v_mfma_f32_16x16x32_bf16 v[128:131], v[236:239], a[132:135], v[128:131]
ds_read_b64_tr_b16 v[252:253], v3 offset:28672
)ASM"
R"ASM(
v_cvt_pk_bf16_f32 v116, v116, v117
v_mfma_f32_16x16x32_bf16 v[132:135], v[232:235], a[136:139], v[132:135]
ds_read_b64_tr_b16 v[254:255], v3 offset:28928
v_cvt_pk_bf16_f32 v117, v118, v119
v_mfma_f32_16x16x32_bf16 v[132:135], v[236:239], a[140:143], v[132:135]
v_accvgpr_write_b32 a86, v116
v_accvgpr_write_b32 a87, v117
v_mfma_f32_16x16x32_bf16 v[136:139], v[232:235], a[144:147], v[136:139]
v_cvt_pk_bf16_f32 v120, v120, v121
v_cvt_pk_bf16_f32 v121, v122, v123
v_mfma_f32_16x16x32_bf16 v[136:139], v[236:239], a[148:151], v[136:139]
v_accvgpr_write_b32 a102, v120
v_accvgpr_write_b32 a103, v121
v_mfma_f32_16x16x32_bf16 v[140:143], v[232:235], a[152:155], v[140:143]
buffer_load_dwordx4 v1, s[56:59], s67 offen offset:1024 sc1 lds
v_mfma_f32_16x16x32_bf16 v[140:143], v[236:239], a[156:159], v[140:143]
ds_read_b64_tr_b16 v[232:233], v3 offset:40960
v_cvt_pk_bf16_f32 v124, v124, v125
v_mfma_f32_16x16x32_bf16 v[128:131], v[240:243], a[160:163], v[128:131]
s_waitcnt lgkmcnt(1)
ds_read_b64_tr_b16 v[234:235], v3 offset:41216
v_cvt_pk_bf16_f32 v125, v126, v127
v_mfma_f32_16x16x32_bf16 v[128:131], v[244:247], a[164:167], v[128:131]
ds_read_b64_tr_b16 v[236:237], v3 offset:45056
v_accvgpr_write_b32 a118, v124
v_mfma_f32_16x16x32_bf16 v[132:135], v[240:243], a[168:171], v[132:135]
buffer_load_dwordx4 v1, s[56:59], s67 offen offset:2048 sc1 lds
v_mfma_f32_16x16x32_bf16 v[132:135], v[244:247], a[172:175], v[132:135]
ds_read_b64_tr_b16 v[238:239], v3 offset:45312
v_accvgpr_write_b32 a119, v125
v_mfma_f32_16x16x32_bf16 v[136:139], v[240:243], a[176:179], v[136:139]
v_mov_b32 v194, 0
v_mov_b32 v195, 0
v_mfma_f32_16x16x32_bf16 v[136:139], v[244:247], a[180:183], v[136:139]
v_cvt_pk_bf16_f32 v128, v128, v129
v_cvt_pk_bf16_f32 v129, v130, v131
v_mfma_f32_16x16x32_bf16 v[140:143], v[240:243], a[184:187], v[140:143]
buffer_load_dwordx4 v1, s[56:59], s67 offen offset:3072 sc1 lds
v_mfma_f32_16x16x32_bf16 v[140:143], v[244:247], a[188:191], v[140:143]
ds_read_b64_tr_b16 v[240:241], v0 offset:25088
v_accvgpr_write_b32 a72, v128
v_mfma_f32_16x16x32_bf16 v[144:147], v[248:251], a[128:131], v[144:147]
ds_read_b64_tr_b16 v[242:243], v0 offset:25344
v_accvgpr_write_b32 a73, v129
v_mfma_f32_16x16x32_bf16 v[144:147], v[252:255], a[132:135], v[144:147]
ds_read_b64_tr_b16 v[244:245], v0 offset:29184
v_cvt_pk_bf16_f32 v132, v132, v133
v_mfma_f32_16x16x32_bf16 v[148:151], v[248:251], a[136:139], v[148:151]
s_add_u32 m0, s66, 81920
s_add_u32 s67, s66, 54272
ds_read_b64_tr_b16 v[246:247], v0 offset:29440
v_mfma_f32_16x16x32_bf16 v[148:151], v[252:255], a[140:143], v[148:151]
s_add_u32 s66, s66, 151552
v_accvgpr_write_b32 a88, v132
v_cvt_pk_bf16_f32 v133, v134, v135
v_mfma_f32_16x16x32_bf16 v[152:155], v[248:251], a[144:147], v[152:155]
buffer_load_dwordx4 v1, s[56:59], s67 offen offset:0 sc1 lds
v_mfma_f32_16x16x32_bf16 v[152:155], v[252:255], a[148:151], v[152:155]
v_accvgpr_write_b32 a89, v133
v_cvt_pk_bf16_f32 v136, v136, v137
v_mfma_f32_16x16x32_bf16 v[156:159], v[248:251], a[152:155], v[156:159]
ds_read_b64_tr_b16 v[248:249], v0 offset:41472
v_cvt_pk_bf16_f32 v137, v138, v139
v_mfma_f32_16x16x32_bf16 v[156:159], v[252:255], a[156:159], v[156:159]
s_waitcnt lgkmcnt(1)
ds_read_b64_tr_b16 v[250:251], v0 offset:41728
v_accvgpr_write_b32 a104, v136
v_mfma_f32_16x16x32_bf16 v[144:147], v[232:235], a[160:163], v[144:147]
buffer_store_dwordx4 a[216:219], v1, s[52:55], s3 offen offset:0 sc1
v_mfma_f32_16x16x32_bf16 v[144:147], v[236:239], a[164:167], v[144:147]
ds_read_b64_tr_b16 v[252:253], v0 offset:45568
v_accvgpr_write_b32 a105, v137
v_mfma_f32_16x16x32_bf16 v[148:151], v[232:235], a[168:171], v[148:151]
ds_read_b64_tr_b16 v[254:255], v0 offset:45824
v_cvt_pk_bf16_f32 v140, v140, v141
v_mfma_f32_16x16x32_bf16 v[148:151], v[236:239], a[172:175], v[148:151]
v_accvgpr_write_b32 a120, v140
v_cvt_pk_bf16_f32 v141, v142, v143
v_mfma_f32_16x16x32_bf16 v[152:155], v[232:235], a[176:179], v[152:155]
v_accvgpr_write_b32 a121, v141
v_mov_b32 v196, 0
v_mfma_f32_16x16x32_bf16 v[152:155], v[236:239], a[180:183], v[152:155]
v_cvt_pk_bf16_f32 v144, v144, v145
v_cvt_pk_bf16_f32 v145, v146, v147
v_mfma_f32_16x16x32_bf16 v[156:159], v[232:235], a[184:187], v[156:159]
ds_read_b64_tr_b16 v[232:233], v3 offset:25088
v_accvgpr_write_b32 a74, v144
v_mfma_f32_16x16x32_bf16 v[156:159], v[236:239], a[188:191], v[156:159]
buffer_load_dwordx4 v1, s[56:59], s67 offen offset:1024 sc1 lds
v_mfma_f32_16x16x32_bf16 v[160:163], v[240:243], a[128:131], v[160:163]
ds_read_b64_tr_b16 v[234:235], v3 offset:25344
v_accvgpr_write_b32 a75, v145
v_mfma_f32_16x16x32_bf16 v[160:163], v[244:247], a[132:135], v[160:163]
ds_read_b64_tr_b16 v[236:237], v3 offset:29184
v_cvt_pk_bf16_f32 v148, v148, v149
v_mfma_f32_16x16x32_bf16 v[164:167], v[240:243], a[136:139], v[164:167]
ds_read_b64_tr_b16 v[238:239], v3 offset:29440
v_cvt_pk_bf16_f32 v149, v150, v151
v_mfma_f32_16x16x32_bf16 v[164:167], v[244:247], a[140:143], v[164:167]
buffer_load_dwordx4 v1, s[56:59], s67 offen offset:2048 sc1 lds
v_mfma_f32_16x16x32_bf16 v[168:171], v[240:243], a[144:147], v[168:171]
v_accvgpr_write_b32 a90, v148
v_accvgpr_write_b32 a91, v149
v_mfma_f32_16x16x32_bf16 v[168:171], v[244:247], a[148:151], v[168:171]
v_cvt_pk_bf16_f32 v152, v152, v153
v_cvt_pk_bf16_f32 v153, v154, v155
v_mfma_f32_16x16x32_bf16 v[172:175], v[240:243], a[152:155], v[172:175]
ds_read_b64_tr_b16 v[240:241], v3 offset:41472
v_accvgpr_write_b32 a106, v152
v_mfma_f32_16x16x32_bf16 v[172:175], v[244:247], a[156:159], v[172:175]
s_waitcnt lgkmcnt(1)
ds_read_b64_tr_b16 v[242:243], v3 offset:41728
v_accvgpr_write_b32 a107, v153
v_mfma_f32_16x16x32_bf16 v[160:163], v[248:251], a[160:163], v[160:163]
ds_read_b64_tr_b16 v[244:245], v3 offset:45568
v_cvt_pk_bf16_f32 v156, v156, v157
v_mfma_f32_16x16x32_bf16 v[160:163], v[252:255], a[164:167], v[160:163]
buffer_load_dwordx4 v1, s[56:59], s67 offen offset:3072 sc1 lds
v_mfma_f32_16x16x32_bf16 v[164:167], v[248:251], a[168:171], v[164:167]
ds_read_b64_tr_b16 v[246:247], v3 offset:45824
v_cvt_pk_bf16_f32 v157, v158, v159
v_mfma_f32_16x16x32_bf16 v[164:167], v[252:255], a[172:175], v[164:167]
v_accvgpr_write_b32 a122, v156
v_accvgpr_write_b32 a123, v157
v_mfma_f32_16x16x32_bf16 v[168:171], v[248:251], a[176:179], v[168:171]
v_mov_b32 v197, 0
v_mov_b32 v198, 0
v_mfma_f32_16x16x32_bf16 v[168:171], v[252:255], a[180:183], v[168:171]
buffer_store_dwordx4 a[220:223], v1, s[52:55], s3 offen offset:2048 sc1
v_mfma_f32_16x16x32_bf16 v[172:175], v[248:251], a[184:187], v[172:175]
v_cvt_pk_bf16_f32 v160, v160, v161
v_cvt_pk_bf16_f32 v161, v162, v163
v_mfma_f32_16x16x32_bf16 v[172:175], v[252:255], a[188:191], v[172:175]
v_accvgpr_write_b32 a76, v160
v_accvgpr_write_b32 a77, v161
v_mfma_f32_16x16x32_bf16 v[176:179], v[232:235], a[128:131], v[176:179]
ds_read_b128 a[128:131], v12 offset:0
v_cvt_pk_bf16_f32 v164, v164, v165
v_mfma_f32_16x16x32_bf16 v[176:179], v[236:239], a[132:135], v[176:179]
ds_read_b128 a[132:135], v12 offset:1024
v_cvt_pk_bf16_f32 v165, v166, v167
v_mfma_f32_16x16x32_bf16 v[180:183], v[232:235], a[136:139], v[180:183]
ds_read_b128 a[136:139], v12 offset:2048
v_accvgpr_write_b32 a92, v164
v_mfma_f32_16x16x32_bf16 v[180:183], v[236:239], a[140:143], v[180:183]
ds_read_b128 a[140:143], v12 offset:3072
v_accvgpr_write_b32 a93, v165
v_mfma_f32_16x16x32_bf16 v[184:187], v[232:235], a[144:147], v[184:187]
ds_read_b128 a[144:147], v12 offset:4096
v_cvt_pk_bf16_f32 v168, v168, v169
v_mfma_f32_16x16x32_bf16 v[184:187], v[236:239], a[148:151], v[184:187]
ds_read_b128 a[148:151], v12 offset:5120
v_cvt_pk_bf16_f32 v169, v170, v171
v_mfma_f32_16x16x32_bf16 v[188:191], v[232:235], a[152:155], v[188:191]
ds_read_b128 a[152:155], v12 offset:6144
v_accvgpr_write_b32 a108, v168
v_mfma_f32_16x16x32_bf16 v[188:191], v[236:239], a[156:159], v[188:191]
s_waitcnt lgkmcnt(4)
ds_read_b128 a[156:159], v12 offset:7168
v_accvgpr_write_b32 a109, v169
v_mfma_f32_16x16x32_bf16 v[176:179], v[240:243], a[160:163], v[176:179]
ds_read_b128 a[160:163], v12 offset:8192
v_cvt_pk_bf16_f32 v172, v172, v173
v_mfma_f32_16x16x32_bf16 v[176:179], v[244:247], a[164:167], v[176:179]
ds_read_b128 a[164:167], v12 offset:9216
v_cvt_pk_bf16_f32 v173, v174, v175
v_mfma_f32_16x16x32_bf16 v[180:183], v[240:243], a[168:171], v[180:183]
ds_read_b128 a[168:171], v12 offset:10240
v_accvgpr_write_b32 a124, v172
v_mfma_f32_16x16x32_bf16 v[180:183], v[244:247], a[172:175], v[180:183]
ds_read_b128 a[172:175], v12 offset:11264
v_accvgpr_write_b32 a125, v173
v_mfma_f32_16x16x32_bf16 v[184:187], v[240:243], a[176:179], v[184:187]
ds_read_b128 a[176:179], v12 offset:12288
v_mov_b32 v199, 0
v_mfma_f32_16x16x32_bf16 v[184:187], v[244:247], a[180:183], v[184:187]
v_cvt_pk_bf16_f32 v176, v176, v177
v_cvt_pk_bf16_f32 v177, v178, v179
v_mfma_f32_16x16x32_bf16 v[188:191], v[240:243], a[184:187], v[188:191]
v_accvgpr_write_b32 a78, v176
v_accvgpr_write_b32 a79, v177
v_mfma_f32_16x16x32_bf16 v[188:191], v[244:247], a[188:191], v[188:191]
v_cvt_pk_bf16_f32 v180, v180, v181
v_cvt_pk_bf16_f32 v181, v182, v183
v_mfma_f32_16x16x32_bf16 v[80:83], a[128:131], a[64:67], v[80:83]
v_accvgpr_write_b32 a94, v180
v_accvgpr_write_b32 a95, v181
v_mfma_f32_16x16x32_bf16 v[80:83], a[132:135], a[68:71], v[80:83]
ds_read_b128 a[180:183], v12 offset:13312
v_cvt_pk_bf16_f32 v184, v184, v185
v_mfma_f32_16x16x32_bf16 v[80:83], a[136:139], a[72:75], v[80:83]
s_waitcnt lgkmcnt(1)
ds_read_b128 a[184:187], v12 offset:14336
v_cvt_pk_bf16_f32 v185, v186, v187
v_mfma_f32_16x16x32_bf16 v[80:83], a[140:143], a[76:79], v[80:83]
ds_read_b128 a[188:191], v12 offset:15360
v_accvgpr_write_b32 a110, v184
v_mfma_f32_16x16x32_bf16 v[96:99], a[128:131], a[80:83], 0
v_accvgpr_write_b32 a111, v185
v_cvt_pk_bf16_f32 v188, v188, v189
v_mfma_f32_16x16x32_bf16 v[96:99], a[132:135], a[84:87], v[96:99]
v_accvgpr_write_b32 a126, v188
v_cvt_pk_bf16_f32 v189, v190, v191
v_mfma_f32_16x16x32_bf16 v[96:99], a[136:139], a[88:91], v[96:99]
v_accvgpr_write_b32 a127, v189
v_mov_b32 v200, 0
v_mfma_f32_16x16x32_bf16 v[96:99], a[140:143], a[92:95], v[96:99]
v_max3_f32 v84, v80, v81, v82
v_max_f32 v84, v84, v83
v_mfma_f32_16x16x32_bf16 v[112:115], a[128:131], a[96:99], 0
v_mov_b32 v201, 0
v_mov_b32 v85, v84
v_mfma_f32_16x16x32_bf16 v[112:115], a[132:135], a[100:103], v[112:115]
s_nop 0
v_permlane32_swap_b32 v84, v85
v_mfma_f32_16x16x32_bf16 v[112:115], a[136:139], a[104:107], v[112:115]
v_max_f32 v84, v84, v85
v_mov_b32 v85, v84
v_mfma_f32_16x16x32_bf16 v[112:115], a[140:143], a[108:111], v[112:115]
v_max3_f32 v100, v96, v97, v98
v_max_f32 v100, v100, v99
v_mfma_f32_16x16x32_bf16 v[128:131], a[128:131], a[112:115], 0
v_mov_b32 v202, 0
v_mov_b32 v101, v100
v_mfma_f32_16x16x32_bf16 v[128:131], a[132:135], a[116:119], v[128:131]
s_waitcnt vmcnt(22) lgkmcnt(0)
v_permlane32_swap_b32 v100, v101
v_mfma_f32_16x16x32_bf16 v[128:131], a[136:139], a[120:123], v[128:131]
s_barrier
v_max_f32 v100, v100, v101
v_mov_b32 v101, v100
v_mfma_f32_16x16x32_bf16 v[128:131], a[140:143], a[124:127], v[128:131]
v_mov_b32 v203, 0
v_mov_b32 v204, 0
v_mov_b32 v205, 0
v_mov_b32 v206, 0
v_mov_b32 v207, 0
v_permlane16_swap_b32 v84, v85
v_max_f32 v84, v84, v85
v_sub_f32 v176, 0, v84
v_sub_f32 v177, 0, v84
v_sub_f32 v178, 0, v84
v_sub_f32 v179, 0, v84
v_permlane16_swap_b32 v100, v101
v_max_f32 v100, v100, v101
v_sub_f32 v180, 0, v100
v_sub_f32 v181, 0, v100
v_sub_f32 v182, 0, v100
v_sub_f32 v183, 0, v100
v_max3_f32 v116, v112, v113, v114
v_max_f32 v116, v116, v115
v_mov_b32 v117, v116
v_max3_f32 v132, v128, v129, v130
v_max_f32 v132, v132, v131
v_permlane32_swap_b32 v116, v117
v_max_f32 v116, v116, v117
v_mov_b32 v117, v116
v_mov_b32 v133, v132
v_mov_b32 v12, v1
v_permlane16_swap_b32 v116, v117
v_max_f32 v116, v116, v117
v_sub_f32 v184, 0, v116
v_sub_f32 v185, 0, v116
v_sub_f32 v186, 0, v116
v_sub_f32 v187, 0, v116
v_permlane32_swap_b32 v132, v133
v_max_f32 v132, v132, v133
v_mov_b32 v133, v132
s_nop 1
v_permlane16_swap_b32 v132, v133
v_max_f32 v132, v132, v133
v_sub_f32 v188, 0, v132
v_sub_f32 v189, 0, v132
v_sub_f32 v190, 0, v132
v_sub_f32 v191, 0, v132
s_mov_b32 s94, 0x5f7fffff
L_attn_P0:
v_mfma_f32_16x16x32_bf16 v[96:99], a[128:131], a[80:83], v[180:183]
s_mov_b32 m0, s68
v_fma_f32 v80, v212, s71, v176
v_fma_f32 v81, v213, s71, v176
v_mfma_f32_16x16x32_bf16 v[96:99], a[132:135], a[84:87], v[96:99]
buffer_load_dwordx4 v1, s[56:59], s66 offen offset:0 sc1 lds
v_mfma_f32_16x16x32_bf16 v[96:99], a[136:139], a[88:91], v[96:99]
s_sub_u32 s67, s66, 59392
v_fma_f32 v82, v214, s71, v176
v_fma_f32 v83, v215, s71, v176
v_mfma_f32_16x16x32_bf16 v[96:99], a[140:143], a[92:95], v[96:99]
s_mov_b32 s89, s84
v_fma_f32 v84, v216, s71, v176
v_fma_f32 v85, v217, s71, v176
v_mfma_f32_16x16x32_bf16 v[100:103], a[144:147], a[80:83], v[180:183]
s_add_u32 s69, s69, 32768
v_fma_f32 v86, v218, s71, v176
v_fma_f32 v87, v219, s71, v176
v_mfma_f32_16x16x32_bf16 v[100:103], a[148:151], a[84:87], v[100:103]
buffer_load_dwordx2 v[8:9], v6, s[52:55], 0 offen sc1
v_mfma_f32_16x16x32_bf16 v[100:103], a[152:155], a[88:91], v[100:103]
s_and_b32 s69, s69, 131071
v_fma_f32 v88, v220, s71, v176
v_fma_f32 v89, v221, s71, v176
v_mfma_f32_16x16x32_bf16 v[100:103], a[156:159], a[92:95], v[100:103]
s_add_u32 s84, s84, s85
v_fma_f32 v90, v222, s71, v176
v_fma_f32 v91, v223, s71, v176
v_mfma_f32_16x16x32_bf16 v[104:107], a[160:163], a[80:83], v[180:183]
v_fma_f32 v92, v224, s71, v176
v_fma_f32 v93, v225, s71, v176
v_mfma_f32_16x16x32_bf16 v[104:107], a[164:167], a[84:87], v[104:107]
buffer_load_dwordx4 v1, s[56:59], s66 offen offset:1024 sc1 lds
v_mfma_f32_16x16x32_bf16 v[104:107], a[168:171], a[88:91], v[104:107]
v_fma_f32 v94, v226, s71, v176
v_fma_f32 v95, v227, s71, v176
v_mfma_f32_16x16x32_bf16 v[104:107], a[172:175], a[92:95], v[104:107]
v_exp_f32 v96, v96
v_mfma_f32_16x16x32_bf16 v[108:111], a[176:179], a[80:83], v[180:183]
v_exp_f32 v97, v97
v_mfma_f32_16x16x32_bf16 v[108:111], a[180:183], a[84:87], v[108:111]
v_exp_f32 v98, v98
v_mfma_f32_16x16x32_bf16 v[108:111], a[184:187], a[88:91], v[108:111]
v_exp_f32 v99, v99
v_mfma_f32_16x16x32_bf16 v[108:111], a[188:191], a[92:95], v[108:111]
buffer_load_dwordx4 v1, s[56:59], s66 offen offset:2048 sc1 lds
v_mfma_f32_16x16x32_bf16 v[80:83], a[128:131], a[64:67], v[80:83]
v_exp_f32 v100, v100
v_mfma_f32_16x16x32_bf16 v[80:83], a[132:135], a[68:71], v[80:83]
v_exp_f32 v101, v101
v_mfma_f32_16x16x32_bf16 v[80:83], a[136:139], a[72:75], v[80:83]
v_exp_f32 v102, v102
v_mfma_f32_16x16x32_bf16 v[80:83], a[140:143], a[76:79], v[80:83]
v_exp_f32 v103, v103
v_mfma_f32_16x16x32_bf16 v[84:87], a[144:147], a[64:67], v[84:87]
v_exp_f32 v104, v104
v_mfma_f32_16x16x32_bf16 v[84:87], a[148:151], a[68:71], v[84:87]
v_exp_f32 v105, v105
v_mfma_f32_16x16x32_bf16 v[84:87], a[152:155], a[72:75], v[84:87]
buffer_load_dwordx4 v1, s[56:59], s66 offen offset:3072 sc1 lds
v_mfma_f32_16x16x32_bf16 v[84:87], a[156:159], a[76:79], v[84:87]
s_add_u32 m0, s68, 16384
v_exp_f32 v106, v106
v_mfma_f32_16x16x32_bf16 v[88:91], a[160:163], a[64:67], v[88:91]
s_add_u32 s66, s66, 37888
v_exp_f32 v107, v107
v_mfma_f32_16x16x32_bf16 v[88:91], a[164:167], a[68:71], v[88:91]
v_exp_f32 v108, v108
v_mfma_f32_16x16x32_bf16 v[88:91], a[168:171], a[72:75], v[88:91]
v_exp_f32 v109, v109
v_mfma_f32_16x16x32_bf16 v[88:91], a[172:175], a[76:79], v[88:91]
v_exp_f32 v110, v110
v_mfma_f32_16x16x32_bf16 v[92:95], a[176:179], a[64:67], v[92:95]
v_exp_f32 v111, v111
v_mfma_f32_16x16x32_bf16 v[92:95], a[180:183], a[68:71], v[92:95]
buffer_load_dwordx4 v1, s[56:59], s67 offen offset:0 sc1 lds
v_mfma_f32_16x16x32_bf16 v[92:95], a[184:187], a[72:75], v[92:95]
v_exp_f32 v80, v80
v_mfma_f32_16x16x32_bf16 v[92:95], a[188:191], a[76:79], v[92:95]
v_exp_f32 v81, v81
v_mfma_f32_16x16x32_bf16 v[112:115], a[128:131], a[96:99], v[184:187]
v_exp_f32 v82, v82
v_mfma_f32_16x16x32_bf16 v[112:115], a[132:135], a[100:103], v[112:115]
v_exp_f32 v83, v83
v_mfma_f32_16x16x32_bf16 v[112:115], a[136:139], a[104:107], v[112:115]
v_exp_f32 v84, v84
v_mfma_f32_16x16x32_bf16 v[112:115], a[140:143], a[108:111], v[112:115]
v_exp_f32 v85, v85
v_mfma_f32_16x16x32_bf16 v[116:119], a[144:147], a[96:99], v[184:187]
buffer_load_dwordx4 v1, s[56:59], s67 offen offset:1024 sc1 lds
v_mfma_f32_16x16x32_bf16 v[116:119], a[148:151], a[100:103], v[116:119]
s_mov_b32 m0, s90
v_exp_f32 v86, v86
v_mfma_f32_16x16x32_bf16 v[116:119], a[152:155], a[104:107], v[116:119]
v_exp_f32 v87, v87
v_mfma_f32_16x16x32_bf16 v[116:119], a[156:159], a[108:111], v[116:119]
v_exp_f32 v88, v88
v_mfma_f32_16x16x32_bf16 v[120:123], a[160:163], a[96:99], v[184:187]
buffer_load_dwordx4 v4, s[80:83], s89 offen offset:0 lds
v_mfma_f32_16x16x32_bf16 v[120:123], a[164:167], a[100:103], v[120:123]
s_add_u32 m0, s68, 16384
s_add_u32 s89, s89, s86
v_cvt_pk_bf16_f32 v152, v96, v97
v_mfma_f32_16x16x32_bf16 v[120:123], a[168:171], a[104:107], v[120:123]
v_exp_f32 v89, v89
v_mfma_f32_16x16x32_bf16 v[120:123], a[172:175], a[108:111], v[120:123]
v_exp_f32 v90, v90
v_mfma_f32_16x16x32_bf16 v[124:127], a[176:179], a[96:99], v[184:187]
buffer_load_dwordx4 v1, s[56:59], s67 offen offset:2048 sc1 lds
v_mfma_f32_16x16x32_bf16 v[124:127], a[180:183], a[100:103], v[124:127]
s_mov_b32 m0, s90
v_exp_f32 v91, v91
v_mfma_f32_16x16x32_bf16 v[124:127], a[184:187], a[104:107], v[124:127]
v_exp_f32 v92, v92
v_mfma_f32_16x16x32_bf16 v[124:127], a[188:191], a[108:111], v[124:127]
v_exp_f32 v93, v93
v_mfma_f32_16x16x32_bf16 v[128:131], a[128:131], a[112:115], v[188:191]
buffer_load_dwordx4 v4, s[80:83], s89 offen offset:1024 lds
v_mfma_f32_16x16x32_bf16 v[128:131], a[132:135], a[116:119], v[128:131]
s_add_u32 m0, s68, 16384
ds_read_b128 a[128:131], v12 offset:0
ds_read_b128 a[132:135], v12 offset:1024
v_mfma_f32_16x16x32_bf16 v[128:131], a[136:139], a[120:123], v[128:131]
)ASM"
R"ASM(
s_add_u32 s89, s89, s86
s_add_u32 s68, s68, 32768
ds_read_b128 a[136:139], v12 offset:2048
v_mfma_f32_16x16x32_bf16 v[128:131], a[140:143], a[124:127], v[128:131]
s_and_b32 s68, s68, 131071
ds_read_b128 a[140:143], v12 offset:3072
v_cvt_pk_bf16_f32 v153, v98, v99
v_mfma_f32_16x16x32_bf16 v[132:135], a[144:147], a[112:115], v[188:191]
buffer_load_dwordx4 v1, s[56:59], s67 offen offset:3072 sc1 lds
v_mfma_f32_16x16x32_bf16 v[132:135], a[148:151], a[116:119], v[132:135]
s_mov_b32 m0, s90
ds_read_b128 a[144:147], v12 offset:4096
ds_read_b128 a[148:151], v12 offset:5120
v_mfma_f32_16x16x32_bf16 v[132:135], a[152:155], a[120:123], v[132:135]
ds_read_b128 a[152:155], v12 offset:6144
v_cvt_pk_bf16_f32 v154, v100, v101
v_mfma_f32_16x16x32_bf16 v[132:135], a[156:159], a[124:127], v[132:135]
ds_read_b128 a[156:159], v12 offset:7168
v_cvt_pk_bf16_f32 v155, v102, v103
v_mfma_f32_16x16x32_bf16 v[136:139], a[160:163], a[112:115], v[188:191]
ds_read_b128 a[160:163], v12 offset:8192
v_cvt_pk_bf16_f32 v156, v104, v105
v_mfma_f32_16x16x32_bf16 v[136:139], a[164:167], a[116:119], v[136:139]
ds_read_b128 a[164:167], v12 offset:9216
v_cvt_pk_bf16_f32 v157, v106, v107
v_mfma_f32_16x16x32_bf16 v[136:139], a[168:171], a[120:123], v[136:139]
ds_read_b128 a[168:171], v12 offset:10240
v_cvt_pk_bf16_f32 v158, v108, v109
v_mfma_f32_16x16x32_bf16 v[136:139], a[172:175], a[124:127], v[136:139]
ds_read_b128 a[172:175], v12 offset:11264
buffer_load_dwordx4 v4, s[80:83], s89 offen offset:2048 lds
v_mfma_f32_16x16x32_bf16 v[140:143], a[176:179], a[112:115], v[188:191]
s_add_u32 s89, s89, s86
s_mov_b32 m0, s90
ds_read_b128 a[176:179], v12 offset:12288
v_mfma_f32_16x16x32_bf16 v[140:143], a[180:183], a[116:119], v[140:143]
s_waitcnt vmcnt(10)
ds_read_b128 a[180:183], v12 offset:13312
buffer_load_dwordx4 v4, s[80:83], s89 offen offset:3072 lds
v_cmp_ne_u64 vcc, s[64:65], v[8:9]
s_nop 0
s_cbranch_vccnz L_stub_kv1
L_ret_kv1:
v_mfma_f32_16x16x32_bf16 v[140:143], a[184:187], a[120:123], v[140:143]
v_exp_f32 v94, v94
v_exp_f32 v95, v95
v_exp_f32 v112, v112
v_exp_f32 v113, v113
v_exp_f32 v114, v114
v_exp_f32 v115, v115
v_exp_f32 v116, v116
v_exp_f32 v117, v117
v_exp_f32 v118, v118
v_exp_f32 v119, v119
v_exp_f32 v120, v120
v_exp_f32 v121, v121
v_exp_f32 v122, v122
v_exp_f32 v123, v123
v_exp_f32 v124, v124
v_exp_f32 v125, v125
v_exp_f32 v126, v126
v_exp_f32 v127, v127
ds_read_b128 a[184:187], v12 offset:14336
v_add_u32 v6, s101, v6
v_mfma_f32_16x16x32_bf16 v[140:143], a[188:191], a[124:127], v[140:143]
v_exp_f32 v128, v128
v_exp_f32 v129, v129
v_exp_f32 v130, v130
v_exp_f32 v131, v131
v_exp_f32 v132, v132
v_exp_f32 v133, v133
v_exp_f32 v134, v134
v_exp_f32 v135, v135
ds_read_b128 a[188:191], v12 offset:15360
v_cvt_pk_bf16_f32 v159, v110, v111
v_cvt_pk_bf16_f32 v144, v80, v81
v_cvt_pk_bf16_f32 v145, v82, v83
v_cvt_pk_bf16_f32 v146, v84, v85
v_cvt_pk_bf16_f32 v147, v86, v87
v_cvt_pk_bf16_f32 v148, v88, v89
v_cvt_pk_bf16_f32 v149, v90, v91
v_cvt_pk_bf16_f32 v150, v92, v93
v_cvt_pk_bf16_f32 v151, v94, v95
v_cvt_pk_bf16_f32 v160, v112, v113
v_cvt_pk_bf16_f32 v161, v114, v115
v_cvt_pk_bf16_f32 v162, v116, v117
v_cvt_pk_bf16_f32 v163, v118, v119
v_cvt_pk_bf16_f32 v164, v120, v121
v_cvt_pk_bf16_f32 v165, v122, v123
v_cvt_pk_bf16_f32 v166, v124, v125
v_cvt_pk_bf16_f32 v167, v126, v127
s_mov_b32 s101, 0
s_xor_b32 s90, s90, 16384
v_add_u32 v12, s69, v1
s_waitcnt lgkmcnt(0)
s_barrier
buffer_store_dwordx2 v[10:11], v5, s[52:55], s93 offen sc1
s_lshl_b32 s70, s62, 1
s_sub_u32 s70, s70, 1
s_cbranch_scc1 L_attn_T1
s_cmp_lt_u32 s62, 4
s_cbranch_scc1 L_attn_D
s_mov_b32 s70, 7
s_nop 0
s_nop 0
s_nop 0
s_nop 0
s_nop 0
s_nop 0
L_attn_Du:
v_mfma_f32_16x16x32_bf16 v[80:83], a[128:131], a[64:67], v[176:179]
s_sub_u32 s91, s70, 2
ds_read_b128 a[192:195], v12 offset:16384
v_cvt_pk_bf16_f32 v168, v128, v129
v_mfma_f32_16x16x32_bf16 v[80:83], a[132:135], a[68:71], v[80:83]
s_mov_b32 m0, s68
L_pad_a_0_2:
s_cbranch_execz L_pad_a_0_3
v_exp_f32 v136, v136
v_mfma_f32_16x16x32_bf16 v[80:83], a[136:139], a[72:75], v[80:83]
s_sub_u32 s67, s66, 59392
s_add_u32 s69, s69, 32768
ds_read_b64_tr_b16 v[228:229], v7 offset:0
v_cvt_pk_bf16_f32 v169, v130, v131
v_mfma_f32_16x16x32_bf16 v[80:83], a[140:143], a[76:79], v[80:83]
s_and_b32 s69, s69, 131071
v_exp_f32 v137, v137
v_mfma_f32_16x16x32_bf16 v[84:87], a[144:147], a[64:67], v[176:179]
ds_read_b128 a[196:199], v12 offset:17408
v_cvt_pk_bf16_f32 v170, v132, v133
v_mfma_f32_16x16x32_bf16 v[84:87], a[148:151], a[68:71], v[84:87]
v_exp_f32 v138, v138
v_mfma_f32_16x16x32_bf16 v[84:87], a[152:155], a[72:75], v[84:87]
s_cmp_eq_u32 s70, 1
s_cselect_b32 s5, 0, 1073741824
ds_read_b64_tr_b16 v[230:231], v7 offset:2048
v_cvt_pk_bf16_f32 v171, v134, v135
v_mfma_f32_16x16x32_bf16 v[84:87], a[156:159], a[76:79], v[84:87]
v_exp_f32 v139, v139
v_mfma_f32_16x16x32_bf16 v[88:91], a[160:163], a[64:67], v[176:179]
ds_read_b128 a[200:203], v12 offset:18432
v_cvt_pk_bf16_f32 v172, v136, v137
v_mfma_f32_16x16x32_bf16 v[88:91], a[164:167], a[68:71], v[88:91]
v_exp_f32 v140, v140
v_mfma_f32_16x16x32_bf16 v[88:91], a[168:171], a[72:75], v[88:91]
s_cmp_lt_u32 s91, 5
s_cselect_b32 s91, s87, 1073741824
ds_read_b64_tr_b16 v[232:233], v7 offset:32
v_cvt_pk_bf16_f32 v173, v138, v139
v_mfma_f32_16x16x32_bf16 v[88:91], a[172:175], a[76:79], v[88:91]
s_add_u32 s87, s87, s88
v_exp_f32 v141, v141
v_mfma_f32_16x16x32_bf16 v[92:95], a[176:179], a[64:67], v[176:179]
ds_read_b128 a[204:207], v12 offset:19456
v_cvt_pk_bf16_f32 v174, v140, v141
v_mfma_f32_16x16x32_bf16 v[92:95], a[180:183], a[68:71], v[92:95]
v_exp_f32 v142, v142
v_mfma_f32_16x16x32_bf16 v[92:95], a[184:187], a[72:75], v[92:95]
s_cmp_gt_u32 s70, 3
s_cselect_b32 s89, s84, 1073741824
ds_read_b64_tr_b16 v[234:235], v7 offset:2080
v_mfma_f32_16x16x32_bf16 v[92:95], a[188:191], a[76:79], v[92:95]
s_sub_u32 s70, s70, 1
v_exp_f32 v143, v143
v_mfma_f32_16x16x32_bf16 v[96:99], a[128:131], a[80:83], v[180:183]
s_add_u32 s84, s84, s85
ds_read_b128 a[208:211], v12 offset:20480
v_cvt_pk_bf16_f32 v175, v142, v143
v_mfma_f32_16x16x32_bf16 v[96:99], a[132:135], a[84:87], v[96:99]
v_exp_f32 v80, v80
v_mfma_f32_16x16x32_bf16 v[96:99], a[136:139], a[88:91], v[96:99]
ds_read_b64_tr_b16 v[236:237], v7 offset:64
v_mfma_f32_16x16x32_bf16 v[96:99], a[140:143], a[92:95], v[96:99]
v_exp_f32 v81, v81
v_mfma_f32_16x16x32_bf16 v[100:103], a[144:147], a[80:83], v[180:183]
ds_read_b128 a[212:215], v12 offset:21504
v_mfma_f32_16x16x32_bf16 v[100:103], a[148:151], a[84:87], v[100:103]
v_exp_f32 v82, v82
v_mfma_f32_16x16x32_bf16 v[100:103], a[152:155], a[88:91], v[100:103]
buffer_load_dwordx4 v1, s[56:59], s66 offen offset:0 sc1 lds
v_mfma_f32_16x16x32_bf16 v[100:103], a[156:159], a[92:95], v[100:103]
v_exp_f32 v83, v83
v_mfma_f32_16x16x32_bf16 v[104:107], a[160:163], a[80:83], v[180:183]
ds_read_b128 a[216:219], v12 offset:22528
v_mfma_f32_16x16x32_bf16 v[104:107], a[164:167], a[84:87], v[104:107]
v_exp_f32 v84, v84
v_mfma_f32_16x16x32_bf16 v[104:107], a[168:171], a[88:91], v[104:107]
ds_read_b64_tr_b16 v[238:239], v7 offset:2112
v_mfma_f32_16x16x32_bf16 v[104:107], a[172:175], a[92:95], v[104:107]
v_exp_f32 v85, v85
v_mfma_f32_16x16x32_bf16 v[108:111], a[176:179], a[80:83], v[180:183]
ds_read_b128 a[220:223], v12 offset:23552
v_mfma_f32_16x16x32_bf16 v[108:111], a[180:183], a[84:87], v[108:111]
v_exp_f32 v86, v86
v_mfma_f32_16x16x32_bf16 v[108:111], a[184:187], a[88:91], v[108:111]
ds_read_b64_tr_b16 v[240:241], v7 offset:96
v_mfma_f32_16x16x32_bf16 v[108:111], a[188:191], a[92:95], v[108:111]
v_exp_f32 v87, v87
v_mfma_f32_16x16x32_bf16 v[112:115], a[128:131], a[96:99], v[184:187]
ds_read_b128 a[224:227], v12 offset:24576
v_mfma_f32_16x16x32_bf16 v[112:115], a[132:135], a[100:103], v[112:115]
v_exp_f32 v88, v88
v_mfma_f32_16x16x32_bf16 v[112:115], a[136:139], a[104:107], v[112:115]
ds_read_b64_tr_b16 v[242:243], v7 offset:2144
v_mfma_f32_16x16x32_bf16 v[112:115], a[140:143], a[108:111], v[112:115]
v_exp_f32 v89, v89
v_mfma_f32_16x16x32_bf16 v[116:119], a[144:147], a[96:99], v[184:187]
ds_read_b128 a[228:231], v12 offset:25600
v_mfma_f32_16x16x32_bf16 v[116:119], a[148:151], a[100:103], v[116:119]
v_exp_f32 v90, v90
v_mfma_f32_16x16x32_bf16 v[116:119], a[152:155], a[104:107], v[116:119]
s_waitcnt lgkmcnt(2)
buffer_store_dwordx4 v[228:231], v1, s[52:55], s91 offen sc1
v_mfma_f32_16x16x32_bf16 v[116:119], a[156:159], a[108:111], v[116:119]
s_add_u32 s91, s91, s79
v_exp_f32 v91, v91
v_mfma_f32_16x16x32_bf16 v[120:123], a[160:163], a[96:99], v[184:187]
ds_read_b128 a[232:235], v12 offset:26624
v_mfma_f32_16x16x32_bf16 v[120:123], a[164:167], a[100:103], v[120:123]
v_exp_f32 v92, v92
v_mfma_f32_16x16x32_bf16 v[120:123], a[168:171], a[104:107], v[120:123]
buffer_load_dwordx2 v[8:9], v6, s[52:55], s5 offen sc1
v_mfma_f32_16x16x32_bf16 v[120:123], a[172:175], a[108:111], v[120:123]
v_exp_f32 v93, v93
v_mfma_f32_16x16x32_bf16 v[124:127], a[176:179], a[96:99], v[184:187]
s_waitcnt lgkmcnt(1)
ds_read_b128 a[236:239], v12 offset:27648
v_xor_b32 v7, 16384, v7
v_mfma_f32_16x16x32_bf16 v[124:127], a[180:183], a[100:103], v[124:127]
v_exp_f32 v94, v94
v_mfma_f32_16x16x32_bf16 v[124:127], a[184:187], a[104:107], v[124:127]
ds_read_b128 a[240:243], v12 offset:28672
v_mfma_f32_16x16x32_bf16 v[124:127], a[188:191], a[108:111], v[124:127]
v_exp_f32 v95, v95
v_mfma_f32_16x16x32_bf16 v[128:131], a[128:131], a[112:115], v[188:191]
ds_read_b128 a[244:247], v12 offset:29696
v_mfma_f32_16x16x32_bf16 v[128:131], a[132:135], a[116:119], v[128:131]
v_exp_f32 v96, v96
v_mfma_f32_16x16x32_bf16 v[128:131], a[136:139], a[120:123], v[128:131]
buffer_load_dwordx4 v1, s[56:59], s66 offen offset:1024 sc1 lds
v_mfma_f32_16x16x32_bf16 v[128:131], a[140:143], a[124:127], v[128:131]
v_exp_f32 v97, v97
v_mfma_f32_16x16x32_bf16 v[132:135], a[144:147], a[112:115], v[188:191]
ds_read_b128 a[248:251], v12 offset:30720
v_mfma_f32_16x16x32_bf16 v[132:135], a[148:151], a[116:119], v[132:135]
v_exp_f32 v98, v98
v_mfma_f32_16x16x32_bf16 v[132:135], a[152:155], a[120:123], v[132:135]
buffer_load_dwordx4 v1, s[56:59], s66 offen offset:2048 sc1 lds
v_mfma_f32_16x16x32_bf16 v[132:135], a[156:159], a[124:127], v[132:135]
v_exp_f32 v99, v99
v_mfma_f32_16x16x32_bf16 v[136:139], a[160:163], a[112:115], v[188:191]
ds_read_b128 a[252:255], v12 offset:31744
v_mfma_f32_16x16x32_bf16 v[136:139], a[164:167], a[116:119], v[136:139]
v_exp_f32 v100, v100
v_mfma_f32_16x16x32_bf16 v[136:139], a[168:171], a[120:123], v[136:139]
buffer_load_dwordx4 v1, s[56:59], s66 offen offset:3072 sc1 lds
v_mfma_f32_16x16x32_bf16 v[136:139], a[172:175], a[124:127], v[136:139]
s_add_u32 m0, s68, 16384
v_exp_f32 v101, v101
v_mfma_f32_16x16x32_bf16 v[140:143], a[176:179], a[112:115], v[188:191]
s_add_u32 s66, s66, 37888
v_mfma_f32_16x16x32_bf16 v[140:143], a[180:183], a[116:119], v[140:143]
s_add_u32 s68, s68, 32768
v_exp_f32 v102, v102
v_mfma_f32_16x16x32_bf16 v[140:143], a[184:187], a[120:123], v[140:143]
buffer_load_dwordx4 v1, s[56:59], s67 offen offset:0 sc1 lds
v_mfma_f32_16x16x32_bf16 v[140:143], a[188:191], a[124:127], v[140:143]
s_and_b32 s68, s68, 131071
v_exp_f32 v103, v103
v_mfma_f32_16x16x32_bf16 v[192:195], v[208:211], v[144:147], v[192:195]
ds_read_b128 a[128:131], v12 offset:0
v_mfma_f32_16x16x32_bf16 v[192:195], v[208:211], v[148:151], v[192:195]
v_exp_f32 v104, v104
v_mfma_f32_16x16x32_bf16 a[0:3], a[192:195], v[144:147], a[0:3]
buffer_load_dwordx4 v1, s[56:59], s67 offen offset:1024 sc1 lds
v_mfma_f32_16x16x32_bf16 a[0:3], a[196:199], v[148:151], a[0:3]
v_exp_f32 v105, v105
v_mfma_f32_16x16x32_bf16 a[4:7], a[200:203], v[144:147], a[4:7]
ds_read_b128 a[132:135], v12 offset:1024
v_mfma_f32_16x16x32_bf16 a[4:7], a[204:207], v[148:151], a[4:7]
v_exp_f32 v106, v106
v_mfma_f32_16x16x32_bf16 a[8:11], a[208:211], v[144:147], a[8:11]
buffer_load_dwordx4 v1, s[56:59], s67 offen offset:2048 sc1 lds
v_mfma_f32_16x16x32_bf16 a[8:11], a[212:215], v[148:151], a[8:11]
v_exp_f32 v107, v107
v_mfma_f32_16x16x32_bf16 a[12:15], a[216:219], v[144:147], a[12:15]
ds_read_b128 a[136:139], v12 offset:2048
v_mfma_f32_16x16x32_bf16 a[12:15], a[220:223], v[148:151], a[12:15]
s_waitcnt lgkmcnt(1)
v_exp_f32 v108, v108
v_mfma_f32_16x16x32_bf16 a[16:19], a[224:227], v[144:147], a[16:19]
buffer_load_dwordx4 v1, s[56:59], s67 offen offset:3072 sc1 lds
v_mfma_f32_16x16x32_bf16 a[16:19], a[228:231], v[148:151], a[16:19]
s_mov_b32 m0, s90
v_exp_f32 v109, v109
v_mfma_f32_16x16x32_bf16 a[20:23], a[232:235], v[144:147], a[20:23]
v_mfma_f32_16x16x32_bf16 a[20:23], a[236:239], v[148:151], a[20:23]
v_exp_f32 v110, v110
v_mfma_f32_16x16x32_bf16 a[24:27], a[240:243], v[144:147], a[24:27]
buffer_load_dwordx4 v4, s[80:83], s89 offen offset:0 lds
v_mfma_f32_16x16x32_bf16 a[24:27], a[244:247], v[148:151], a[24:27]
s_mov_b32 m0, s90
v_exp_f32 v111, v111
v_mfma_f32_16x16x32_bf16 a[28:31], a[248:251], v[144:147], a[28:31]
s_add_u32 s89, s89, s86
ds_read_b128 a[140:143], v12 offset:3072
v_cvt_pk_bf16_f32 v144, v80, v81
v_mfma_f32_16x16x32_bf16 a[28:31], a[252:255], v[148:151], a[28:31]
v_exp_f32 v112, v112
v_mfma_f32_16x16x32_bf16 v[196:199], v[208:211], v[152:155], v[196:199]
buffer_load_dwordx4 v4, s[80:83], s89 offen offset:1024 lds
v_mfma_f32_16x16x32_bf16 v[196:199], v[208:211], v[156:159], v[196:199]
s_mov_b32 m0, s90
v_exp_f32 v113, v113
v_mfma_f32_16x16x32_bf16 a[32:35], a[192:195], v[152:155], a[32:35]
s_add_u32 s89, s89, s86
ds_read_b128 a[144:147], v12 offset:4096
v_cvt_pk_bf16_f32 v145, v82, v83
v_mfma_f32_16x16x32_bf16 a[32:35], a[196:199], v[156:159], a[32:35]
v_exp_f32 v114, v114
v_mfma_f32_16x16x32_bf16 a[36:39], a[200:203], v[152:155], a[36:39]
buffer_load_dwordx4 v4, s[80:83], s89 offen offset:2048 lds
v_mfma_f32_16x16x32_bf16 a[36:39], a[204:207], v[156:159], a[36:39]
s_mov_b32 m0, s90
v_exp_f32 v115, v115
v_mfma_f32_16x16x32_bf16 a[40:43], a[208:211], v[152:155], a[40:43]
s_add_u32 s89, s89, s86
ds_read_b128 a[148:151], v12 offset:5120
v_cvt_pk_bf16_f32 v146, v84, v85
v_mfma_f32_16x16x32_bf16 a[40:43], a[212:215], v[156:159], a[40:43]
s_xor_b32 s90, s90, 16384
v_exp_f32 v116, v116
v_mfma_f32_16x16x32_bf16 a[44:47], a[216:219], v[152:155], a[44:47]
buffer_load_dwordx4 v4, s[80:83], s89 offen offset:3072 lds
v_mfma_f32_16x16x32_bf16 a[44:47], a[220:223], v[156:159], a[44:47]
v_exp_f32 v117, v117
v_mfma_f32_16x16x32_bf16 a[48:51], a[224:227], v[152:155], a[48:51]
v_cvt_pk_bf16_f32 v147, v86, v87
v_cvt_pk_bf16_f32 v148, v88, v89
v_mfma_f32_16x16x32_bf16 a[48:51], a[228:231], v[156:159], a[48:51]
v_exp_f32 v118, v118
v_mfma_f32_16x16x32_bf16 a[52:55], a[232:235], v[152:155], a[52:55]
ds_read_b128 a[152:155], v12 offset:6144
v_cvt_pk_bf16_f32 v149, v90, v91
v_mfma_f32_16x16x32_bf16 a[52:55], a[236:239], v[156:159], a[52:55]
v_exp_f32 v119, v119
v_mfma_f32_16x16x32_bf16 a[56:59], a[240:243], v[152:155], a[56:59]
ds_read_b128 a[156:159], v12 offset:7168
v_cvt_pk_bf16_f32 v150, v92, v93
v_mfma_f32_16x16x32_bf16 a[56:59], a[244:247], v[156:159], a[56:59]
v_exp_f32 v120, v120
v_mfma_f32_16x16x32_bf16 a[60:63], a[248:251], v[152:155], a[60:63]
v_cvt_pk_bf16_f32 v151, v94, v95
v_cvt_pk_bf16_f32 v152, v96, v97
v_mfma_f32_16x16x32_bf16 a[60:63], a[252:255], v[156:159], a[60:63]
v_exp_f32 v121, v121
v_mfma_f32_16x16x32_bf16 v[200:203], v[208:211], v[160:163], v[200:203]
ds_read_b128 a[160:163], v12 offset:8192
v_cvt_pk_bf16_f32 v153, v98, v99
v_mfma_f32_16x16x32_bf16 v[200:203], v[208:211], v[164:167], v[200:203]
v_exp_f32 v122, v122
v_mfma_f32_16x16x32_bf16 v[16:19], a[192:195], v[160:163], v[16:19]
buffer_store_dwordx4 v[232:235], v1, s[52:55], s91 offen sc1
v_mfma_f32_16x16x32_bf16 v[16:19], a[196:199], v[164:167], v[16:19]
s_add_u32 s91, s91, s79
v_exp_f32 v123, v123
v_mfma_f32_16x16x32_bf16 v[20:23], a[200:203], v[160:163], v[20:23]
ds_read_b128 a[164:167], v12 offset:9216
v_cvt_pk_bf16_f32 v154, v100, v101
v_mfma_f32_16x16x32_bf16 v[20:23], a[204:207], v[164:167], v[20:23]
v_exp_f32 v124, v124
v_mfma_f32_16x16x32_bf16 v[24:27], a[208:211], v[160:163], v[24:27]
ds_read_b128 a[168:171], v12 offset:10240
v_cvt_pk_bf16_f32 v155, v102, v103
v_mfma_f32_16x16x32_bf16 v[24:27], a[212:215], v[164:167], v[24:27]
v_exp_f32 v125, v125
v_mfma_f32_16x16x32_bf16 v[28:31], a[216:219], v[160:163], v[28:31]
v_cvt_pk_bf16_f32 v156, v104, v105
v_cvt_pk_bf16_f32 v157, v106, v107
v_mfma_f32_16x16x32_bf16 v[28:31], a[220:223], v[164:167], v[28:31]
v_exp_f32 v126, v126
v_mfma_f32_16x16x32_bf16 v[32:35], a[224:227], v[160:163], v[32:35]
ds_read_b128 a[172:175], v12 offset:11264
v_cvt_pk_bf16_f32 v158, v108, v109
v_mfma_f32_16x16x32_bf16 v[32:35], a[228:231], v[164:167], v[32:35]
v_exp_f32 v127, v127
v_mfma_f32_16x16x32_bf16 v[36:39], a[232:235], v[160:163], v[36:39]
ds_read_b128 a[176:179], v12 offset:12288
v_cvt_pk_bf16_f32 v159, v110, v111
v_mfma_f32_16x16x32_bf16 v[36:39], a[236:239], v[164:167], v[36:39]
v_exp_f32 v128, v128
v_mfma_f32_16x16x32_bf16 v[40:43], a[240:243], v[160:163], v[40:43]
buffer_store_dwordx4 v[236:239], v1, s[52:55], s91 offen sc1
v_mfma_f32_16x16x32_bf16 v[40:43], a[244:247], v[164:167], v[40:43]
s_add_u32 s91, s91, s79
v_exp_f32 v129, v129
v_mfma_f32_16x16x32_bf16 v[44:47], a[248:251], v[160:163], v[44:47]
ds_read_b128 a[180:183], v12 offset:13312
v_cvt_pk_bf16_f32 v160, v112, v113
v_mfma_f32_16x16x32_bf16 v[44:47], a[252:255], v[164:167], v[44:47]
)ASM"
R"ASM(
v_exp_f32 v130, v130
v_mfma_f32_16x16x32_bf16 v[204:207], v[208:211], v[168:171], v[204:207]
v_cvt_pk_bf16_f32 v161, v114, v115
v_cvt_pk_bf16_f32 v162, v116, v117
v_mfma_f32_16x16x32_bf16 v[204:207], v[208:211], v[172:175], v[204:207]
v_exp_f32 v131, v131
v_mfma_f32_16x16x32_bf16 v[48:51], a[192:195], v[168:171], v[48:51]
ds_read_b128 a[184:187], v12 offset:14336
v_cvt_pk_bf16_f32 v163, v118, v119
v_mfma_f32_16x16x32_bf16 v[48:51], a[196:199], v[172:175], v[48:51]
v_exp_f32 v132, v132
v_mfma_f32_16x16x32_bf16 v[52:55], a[200:203], v[168:171], v[52:55]
ds_read_b128 a[188:191], v12 offset:15360
v_cvt_pk_bf16_f32 v164, v120, v121
v_mfma_f32_16x16x32_bf16 v[52:55], a[204:207], v[172:175], v[52:55]
v_exp_f32 v133, v133
v_mfma_f32_16x16x32_bf16 v[56:59], a[208:211], v[168:171], v[56:59]
buffer_store_dwordx4 v[240:243], v1, s[52:55], s91 offen sc1
v_mfma_f32_16x16x32_bf16 v[56:59], a[212:215], v[172:175], v[56:59]
v_exp_f32 v134, v134
v_mfma_f32_16x16x32_bf16 v[60:63], a[216:219], v[168:171], v[60:63]
v_cvt_pk_bf16_f32 v165, v122, v123
v_cvt_pk_bf16_f32 v166, v124, v125
v_mfma_f32_16x16x32_bf16 v[60:63], a[220:223], v[172:175], v[60:63]
v_exp_f32 v135, v135
v_mfma_f32_16x16x32_bf16 v[64:67], a[224:227], v[168:171], v[64:67]
v_cvt_pk_bf16_f32 v167, v126, v127
v_add_u32 v12, s69, v1
v_mfma_f32_16x16x32_bf16 v[64:67], a[228:231], v[172:175], v[64:67]
v_mfma_f32_16x16x32_bf16 v[68:71], a[232:235], v[168:171], v[68:71]
v_mfma_f32_16x16x32_bf16 v[68:71], a[236:239], v[172:175], v[68:71]
s_cmp_lg_u32 s70, 0
v_mfma_f32_16x16x32_bf16 v[72:75], a[240:243], v[168:171], v[72:75]
v_mfma_f32_16x16x32_bf16 v[72:75], a[244:247], v[172:175], v[72:75]
s_waitcnt vmcnt(14) lgkmcnt(0)
v_mfma_f32_16x16x32_bf16 v[76:79], a[248:251], v[168:171], v[76:79]
s_barrier
v_mfma_f32_16x16x32_bf16 v[76:79], a[252:255], v[172:175], v[76:79]
s_cbranch_scc1 L_attn_Du
buffer_store_dwordx2 v[10:11], v5, s[52:55], s78 offen sc1
v_cmp_ne_u64 vcc, s[64:65], v[8:9]
s_nop 0
s_cbranch_vccnz L_stub_kv2
L_ret_kv2:
s_lshl_b32 s70, s62, 1
s_sub_u32 s70, s70, 8
s_cmp_eq_u32 s70, 0
s_cbranch_scc1 L_attn_Dm
s_nop 0
s_nop 0
s_nop 0
s_nop 0
L_pad_a_1_2:
s_cbranch_execz L_pad_a_1_3
L_attn_D:
v_mfma_f32_16x16x32_bf16 v[80:83], a[128:131], a[64:67], v[176:179]
s_mov_b32 m0, s68
ds_read_b128 a[192:195], v12 offset:16384
v_cvt_pk_bf16_f32 v168, v128, v129
v_mfma_f32_16x16x32_bf16 v[80:83], a[132:135], a[68:71], v[80:83]
s_sub_u32 s70, s70, 1
v_exp_f32 v136, v136
v_mfma_f32_16x16x32_bf16 v[80:83], a[136:139], a[72:75], v[80:83]
s_sub_u32 s67, s66, 59392
ds_read_b128 a[196:199], v12 offset:17408
v_cvt_pk_bf16_f32 v169, v130, v131
v_mfma_f32_16x16x32_bf16 v[80:83], a[140:143], a[76:79], v[80:83]
s_add_u32 s69, s69, 32768
v_exp_f32 v137, v137
v_mfma_f32_16x16x32_bf16 v[84:87], a[144:147], a[64:67], v[176:179]
s_and_b32 s69, s69, 131071
v_cvt_pk_bf16_f32 v170, v132, v133
v_cvt_pk_bf16_f32 v171, v134, v135
v_mfma_f32_16x16x32_bf16 v[84:87], a[148:151], a[68:71], v[84:87]
v_exp_f32 v138, v138
v_mfma_f32_16x16x32_bf16 v[84:87], a[152:155], a[72:75], v[84:87]
buffer_load_dwordx4 v1, s[56:59], s66 offen offset:0 sc1 lds
v_mfma_f32_16x16x32_bf16 v[84:87], a[156:159], a[76:79], v[84:87]
v_exp_f32 v139, v139
v_mfma_f32_16x16x32_bf16 v[88:91], a[160:163], a[64:67], v[176:179]
ds_read_b128 a[200:203], v12 offset:18432
v_cvt_pk_bf16_f32 v172, v136, v137
v_mfma_f32_16x16x32_bf16 v[88:91], a[164:167], a[68:71], v[88:91]
v_exp_f32 v140, v140
v_mfma_f32_16x16x32_bf16 v[88:91], a[168:171], a[72:75], v[88:91]
v_cvt_pk_bf16_f32 v173, v138, v139
v_mfma_f32_16x16x32_bf16 v[88:91], a[172:175], a[76:79], v[88:91]
v_exp_f32 v141, v141
v_mfma_f32_16x16x32_bf16 v[92:95], a[176:179], a[64:67], v[176:179]
ds_read_b128 a[204:207], v12 offset:19456
v_cvt_pk_bf16_f32 v174, v140, v141
v_mfma_f32_16x16x32_bf16 v[92:95], a[180:183], a[68:71], v[92:95]
v_exp_f32 v142, v142
v_mfma_f32_16x16x32_bf16 v[92:95], a[184:187], a[72:75], v[92:95]
ds_read_b128 a[208:211], v12 offset:20480
v_mfma_f32_16x16x32_bf16 v[92:95], a[188:191], a[76:79], v[92:95]
v_exp_f32 v143, v143
v_mfma_f32_16x16x32_bf16 v[96:99], a[128:131], a[80:83], v[180:183]
v_cvt_pk_bf16_f32 v175, v142, v143
v_mfma_f32_16x16x32_bf16 v[96:99], a[132:135], a[84:87], v[96:99]
v_exp_f32 v80, v80
v_mfma_f32_16x16x32_bf16 v[96:99], a[136:139], a[88:91], v[96:99]
ds_read_b128 a[212:215], v12 offset:21504
v_mfma_f32_16x16x32_bf16 v[96:99], a[140:143], a[92:95], v[96:99]
v_exp_f32 v81, v81
v_mfma_f32_16x16x32_bf16 v[100:103], a[144:147], a[80:83], v[180:183]
v_mfma_f32_16x16x32_bf16 v[100:103], a[148:151], a[84:87], v[100:103]
v_exp_f32 v82, v82
v_mfma_f32_16x16x32_bf16 v[100:103], a[152:155], a[88:91], v[100:103]
buffer_load_dwordx4 v1, s[56:59], s66 offen offset:1024 sc1 lds
v_mfma_f32_16x16x32_bf16 v[100:103], a[156:159], a[92:95], v[100:103]
v_exp_f32 v83, v83
v_mfma_f32_16x16x32_bf16 v[104:107], a[160:163], a[80:83], v[180:183]
ds_read_b128 a[216:219], v12 offset:22528
v_mfma_f32_16x16x32_bf16 v[104:107], a[164:167], a[84:87], v[104:107]
v_exp_f32 v84, v84
v_mfma_f32_16x16x32_bf16 v[104:107], a[168:171], a[88:91], v[104:107]
ds_read_b128 a[220:223], v12 offset:23552
v_mfma_f32_16x16x32_bf16 v[104:107], a[172:175], a[92:95], v[104:107]
v_exp_f32 v85, v85
v_mfma_f32_16x16x32_bf16 v[108:111], a[176:179], a[80:83], v[180:183]
v_mfma_f32_16x16x32_bf16 v[108:111], a[180:183], a[84:87], v[108:111]
v_exp_f32 v86, v86
v_mfma_f32_16x16x32_bf16 v[108:111], a[184:187], a[88:91], v[108:111]
ds_read_b128 a[224:227], v12 offset:24576
v_mfma_f32_16x16x32_bf16 v[108:111], a[188:191], a[92:95], v[108:111]
v_exp_f32 v87, v87
v_mfma_f32_16x16x32_bf16 v[112:115], a[128:131], a[96:99], v[184:187]
v_mfma_f32_16x16x32_bf16 v[112:115], a[132:135], a[100:103], v[112:115]
v_exp_f32 v88, v88
v_mfma_f32_16x16x32_bf16 v[112:115], a[136:139], a[104:107], v[112:115]
ds_read_b128 a[228:231], v12 offset:25600
v_mfma_f32_16x16x32_bf16 v[112:115], a[140:143], a[108:111], v[112:115]
v_exp_f32 v89, v89
v_mfma_f32_16x16x32_bf16 v[116:119], a[144:147], a[96:99], v[184:187]
ds_read_b128 a[232:235], v12 offset:26624
v_mfma_f32_16x16x32_bf16 v[116:119], a[148:151], a[100:103], v[116:119]
v_exp_f32 v90, v90
v_mfma_f32_16x16x32_bf16 v[116:119], a[152:155], a[104:107], v[116:119]
buffer_load_dwordx4 v1, s[56:59], s66 offen offset:2048 sc1 lds
v_mfma_f32_16x16x32_bf16 v[116:119], a[156:159], a[108:111], v[116:119]
v_exp_f32 v91, v91
v_mfma_f32_16x16x32_bf16 v[120:123], a[160:163], a[96:99], v[184:187]
v_mfma_f32_16x16x32_bf16 v[120:123], a[164:167], a[100:103], v[120:123]
v_exp_f32 v92, v92
v_mfma_f32_16x16x32_bf16 v[120:123], a[168:171], a[104:107], v[120:123]
ds_read_b128 a[236:239], v12 offset:27648
v_mfma_f32_16x16x32_bf16 v[120:123], a[172:175], a[108:111], v[120:123]
v_exp_f32 v93, v93
v_mfma_f32_16x16x32_bf16 v[124:127], a[176:179], a[96:99], v[184:187]
v_mfma_f32_16x16x32_bf16 v[124:127], a[180:183], a[100:103], v[124:127]
v_exp_f32 v94, v94
v_mfma_f32_16x16x32_bf16 v[124:127], a[184:187], a[104:107], v[124:127]
ds_read_b128 a[240:243], v12 offset:28672
v_mfma_f32_16x16x32_bf16 v[124:127], a[188:191], a[108:111], v[124:127]
v_exp_f32 v95, v95
v_mfma_f32_16x16x32_bf16 v[128:131], a[128:131], a[112:115], v[188:191]
ds_read_b128 a[244:247], v12 offset:29696
v_mfma_f32_16x16x32_bf16 v[128:131], a[132:135], a[116:119], v[128:131]
v_exp_f32 v96, v96
v_mfma_f32_16x16x32_bf16 v[128:131], a[136:139], a[120:123], v[128:131]
v_mfma_f32_16x16x32_bf16 v[128:131], a[140:143], a[124:127], v[128:131]
v_exp_f32 v97, v97
v_mfma_f32_16x16x32_bf16 v[132:135], a[144:147], a[112:115], v[188:191]
ds_read_b128 a[248:251], v12 offset:30720
v_mfma_f32_16x16x32_bf16 v[132:135], a[148:151], a[116:119], v[132:135]
v_exp_f32 v98, v98
v_mfma_f32_16x16x32_bf16 v[132:135], a[152:155], a[120:123], v[132:135]
buffer_load_dwordx4 v1, s[56:59], s66 offen offset:3072 sc1 lds
v_mfma_f32_16x16x32_bf16 v[132:135], a[156:159], a[124:127], v[132:135]
s_add_u32 m0, s68, 16384
v_exp_f32 v99, v99
v_mfma_f32_16x16x32_bf16 v[136:139], a[160:163], a[112:115], v[188:191]
s_add_u32 s66, s66, 37888
v_mfma_f32_16x16x32_bf16 v[136:139], a[164:167], a[116:119], v[136:139]
s_add_u32 s68, s68, 32768
v_exp_f32 v100, v100
v_mfma_f32_16x16x32_bf16 v[136:139], a[168:171], a[120:123], v[136:139]
s_and_b32 s68, s68, 131071
ds_read_b128 a[252:255], v12 offset:31744
v_mfma_f32_16x16x32_bf16 v[136:139], a[172:175], a[124:127], v[136:139]
v_exp_f32 v101, v101
v_mfma_f32_16x16x32_bf16 v[140:143], a[176:179], a[112:115], v[188:191]
v_mfma_f32_16x16x32_bf16 v[140:143], a[180:183], a[116:119], v[140:143]
v_exp_f32 v102, v102
v_mfma_f32_16x16x32_bf16 v[140:143], a[184:187], a[120:123], v[140:143]
ds_read_b128 a[128:131], v12 offset:0
v_mfma_f32_16x16x32_bf16 v[140:143], a[188:191], a[124:127], v[140:143]
v_exp_f32 v103, v103
v_mfma_f32_16x16x32_bf16 v[192:195], v[208:211], v[144:147], v[192:195]
ds_read_b128 a[132:135], v12 offset:1024
v_mfma_f32_16x16x32_bf16 v[192:195], v[208:211], v[148:151], v[192:195]
s_waitcnt lgkmcnt(2)
v_exp_f32 v104, v104
v_mfma_f32_16x16x32_bf16 a[0:3], a[192:195], v[144:147], a[0:3]
v_mfma_f32_16x16x32_bf16 a[0:3], a[196:199], v[148:151], a[0:3]
v_exp_f32 v105, v105
v_mfma_f32_16x16x32_bf16 a[4:7], a[200:203], v[144:147], a[4:7]
ds_read_b128 a[136:139], v12 offset:2048
v_mfma_f32_16x16x32_bf16 a[4:7], a[204:207], v[148:151], a[4:7]
v_exp_f32 v106, v106
v_mfma_f32_16x16x32_bf16 a[8:11], a[208:211], v[144:147], a[8:11]
buffer_load_dwordx4 v1, s[56:59], s67 offen offset:0 sc1 lds
v_mfma_f32_16x16x32_bf16 a[8:11], a[212:215], v[148:151], a[8:11]
v_exp_f32 v107, v107
v_mfma_f32_16x16x32_bf16 a[12:15], a[216:219], v[144:147], a[12:15]
v_mfma_f32_16x16x32_bf16 a[12:15], a[220:223], v[148:151], a[12:15]
v_exp_f32 v108, v108
v_mfma_f32_16x16x32_bf16 a[16:19], a[224:227], v[144:147], a[16:19]
ds_read_b128 a[140:143], v12 offset:3072
v_mfma_f32_16x16x32_bf16 a[16:19], a[228:231], v[148:151], a[16:19]
v_exp_f32 v109, v109
v_mfma_f32_16x16x32_bf16 a[20:23], a[232:235], v[144:147], a[20:23]
v_mfma_f32_16x16x32_bf16 a[20:23], a[236:239], v[148:151], a[20:23]
v_exp_f32 v110, v110
v_mfma_f32_16x16x32_bf16 a[24:27], a[240:243], v[144:147], a[24:27]
ds_read_b128 a[144:147], v12 offset:4096
v_mfma_f32_16x16x32_bf16 a[24:27], a[244:247], v[148:151], a[24:27]
v_exp_f32 v111, v111
v_mfma_f32_16x16x32_bf16 a[28:31], a[248:251], v[144:147], a[28:31]
v_cvt_pk_bf16_f32 v144, v80, v81
v_cvt_pk_bf16_f32 v145, v82, v83
v_mfma_f32_16x16x32_bf16 a[28:31], a[252:255], v[148:151], a[28:31]
v_exp_f32 v112, v112
v_mfma_f32_16x16x32_bf16 v[196:199], v[208:211], v[152:155], v[196:199]
ds_read_b128 a[148:151], v12 offset:5120
v_cvt_pk_bf16_f32 v146, v84, v85
v_mfma_f32_16x16x32_bf16 v[196:199], v[208:211], v[156:159], v[196:199]
v_exp_f32 v113, v113
v_mfma_f32_16x16x32_bf16 a[32:35], a[192:195], v[152:155], a[32:35]
ds_read_b128 a[152:155], v12 offset:6144
v_cvt_pk_bf16_f32 v147, v86, v87
v_mfma_f32_16x16x32_bf16 a[32:35], a[196:199], v[156:159], a[32:35]
v_exp_f32 v114, v114
v_mfma_f32_16x16x32_bf16 a[36:39], a[200:203], v[152:155], a[36:39]
buffer_load_dwordx4 v1, s[56:59], s67 offen offset:1024 sc1 lds
v_mfma_f32_16x16x32_bf16 a[36:39], a[204:207], v[156:159], a[36:39]
v_exp_f32 v115, v115
v_mfma_f32_16x16x32_bf16 a[40:43], a[208:211], v[152:155], a[40:43]
v_cvt_pk_bf16_f32 v148, v88, v89
v_cvt_pk_bf16_f32 v149, v90, v91
v_mfma_f32_16x16x32_bf16 a[40:43], a[212:215], v[156:159], a[40:43]
v_exp_f32 v116, v116
v_mfma_f32_16x16x32_bf16 a[44:47], a[216:219], v[152:155], a[44:47]
ds_read_b128 a[156:159], v12 offset:7168
v_cvt_pk_bf16_f32 v150, v92, v93
v_mfma_f32_16x16x32_bf16 a[44:47], a[220:223], v[156:159], a[44:47]
v_exp_f32 v117, v117
v_mfma_f32_16x16x32_bf16 a[48:51], a[224:227], v[152:155], a[48:51]
v_cvt_pk_bf16_f32 v151, v94, v95
v_mfma_f32_16x16x32_bf16 a[48:51], a[228:231], v[156:159], a[48:51]
v_exp_f32 v118, v118
v_mfma_f32_16x16x32_bf16 a[52:55], a[232:235], v[152:155], a[52:55]
ds_read_b128 a[160:163], v12 offset:8192
v_mfma_f32_16x16x32_bf16 a[52:55], a[236:239], v[156:159], a[52:55]
v_exp_f32 v119, v119
v_mfma_f32_16x16x32_bf16 a[56:59], a[240:243], v[152:155], a[56:59]
v_mfma_f32_16x16x32_bf16 a[56:59], a[244:247], v[156:159], a[56:59]
v_exp_f32 v120, v120
v_mfma_f32_16x16x32_bf16 a[60:63], a[248:251], v[152:155], a[60:63]
ds_read_b128 a[164:167], v12 offset:9216
v_cvt_pk_bf16_f32 v152, v96, v97
v_mfma_f32_16x16x32_bf16 a[60:63], a[252:255], v[156:159], a[60:63]
v_exp_f32 v121, v121
v_mfma_f32_16x16x32_bf16 v[200:203], v[208:211], v[160:163], v[200:203]
v_cvt_pk_bf16_f32 v153, v98, v99
v_cvt_pk_bf16_f32 v154, v100, v101
v_mfma_f32_16x16x32_bf16 v[200:203], v[208:211], v[164:167], v[200:203]
v_exp_f32 v122, v122
v_mfma_f32_16x16x32_bf16 v[16:19], a[192:195], v[160:163], v[16:19]
buffer_load_dwordx4 v1, s[56:59], s67 offen offset:2048 sc1 lds
v_mfma_f32_16x16x32_bf16 v[16:19], a[196:199], v[164:167], v[16:19]
v_exp_f32 v123, v123
v_mfma_f32_16x16x32_bf16 v[20:23], a[200:203], v[160:163], v[20:23]
ds_read_b128 a[168:171], v12 offset:10240
v_cvt_pk_bf16_f32 v155, v102, v103
v_mfma_f32_16x16x32_bf16 v[20:23], a[204:207], v[164:167], v[20:23]
v_exp_f32 v124, v124
v_mfma_f32_16x16x32_bf16 v[24:27], a[208:211], v[160:163], v[24:27]
ds_read_b128 a[172:175], v12 offset:11264
v_cvt_pk_bf16_f32 v156, v104, v105
v_mfma_f32_16x16x32_bf16 v[24:27], a[212:215], v[164:167], v[24:27]
v_exp_f32 v125, v125
v_mfma_f32_16x16x32_bf16 v[28:31], a[216:219], v[160:163], v[28:31]
v_cvt_pk_bf16_f32 v157, v106, v107
v_cvt_pk_bf16_f32 v158, v108, v109
v_mfma_f32_16x16x32_bf16 v[28:31], a[220:223], v[164:167], v[28:31]
v_exp_f32 v126, v126
v_mfma_f32_16x16x32_bf16 v[32:35], a[224:227], v[160:163], v[32:35]
ds_read_b128 a[176:179], v12 offset:12288
v_cvt_pk_bf16_f32 v159, v110, v111
v_mfma_f32_16x16x32_bf16 v[32:35], a[228:231], v[164:167], v[32:35]
v_exp_f32 v127, v127
v_mfma_f32_16x16x32_bf16 v[36:39], a[232:235], v[160:163], v[36:39]
v_mfma_f32_16x16x32_bf16 v[36:39], a[236:239], v[164:167], v[36:39]
v_exp_f32 v128, v128
v_mfma_f32_16x16x32_bf16 v[40:43], a[240:243], v[160:163], v[40:43]
ds_read_b128 a[180:183], v12 offset:13312
v_mfma_f32_16x16x32_bf16 v[40:43], a[244:247], v[164:167], v[40:43]
v_exp_f32 v129, v129
v_mfma_f32_16x16x32_bf16 v[44:47], a[248:251], v[160:163], v[44:47]
v_cvt_pk_bf16_f32 v160, v112, v113
v_cvt_pk_bf16_f32 v161, v114, v115
v_mfma_f32_16x16x32_bf16 v[44:47], a[252:255], v[164:167], v[44:47]
v_exp_f32 v130, v130
v_mfma_f32_16x16x32_bf16 v[204:207], v[208:211], v[168:171], v[204:207]
buffer_load_dwordx4 v1, s[56:59], s67 offen offset:3072 sc1 lds
v_mfma_f32_16x16x32_bf16 v[204:207], v[208:211], v[172:175], v[204:207]
v_exp_f32 v131, v131
v_mfma_f32_16x16x32_bf16 v[48:51], a[192:195], v[168:171], v[48:51]
L_pad_a_2_2:
s_cbranch_execz L_pad_a_2_3
ds_read_b128 a[184:187], v12 offset:14336
v_cvt_pk_bf16_f32 v162, v116, v117
v_mfma_f32_16x16x32_bf16 v[48:51], a[196:199], v[172:175], v[48:51]
v_exp_f32 v132, v132
v_mfma_f32_16x16x32_bf16 v[52:55], a[200:203], v[168:171], v[52:55]
v_cvt_pk_bf16_f32 v163, v118, v119
v_cvt_pk_bf16_f32 v164, v120, v121
v_mfma_f32_16x16x32_bf16 v[52:55], a[204:207], v[172:175], v[52:55]
v_exp_f32 v133, v133
v_mfma_f32_16x16x32_bf16 v[56:59], a[208:211], v[168:171], v[56:59]
ds_read_b128 a[188:191], v12 offset:15360
v_cvt_pk_bf16_f32 v165, v122, v123
v_mfma_f32_16x16x32_bf16 v[56:59], a[212:215], v[172:175], v[56:59]
v_exp_f32 v134, v134
v_mfma_f32_16x16x32_bf16 v[60:63], a[216:219], v[168:171], v[60:63]
v_cvt_pk_bf16_f32 v166, v124, v125
v_cvt_pk_bf16_f32 v167, v126, v127
v_mfma_f32_16x16x32_bf16 v[60:63], a[220:223], v[172:175], v[60:63]
v_exp_f32 v135, v135
v_mfma_f32_16x16x32_bf16 v[64:67], a[224:227], v[168:171], v[64:67]
v_add_u32 v12, s69, v1
v_mfma_f32_16x16x32_bf16 v[64:67], a[228:231], v[172:175], v[64:67]
v_mfma_f32_16x16x32_bf16 v[68:71], a[232:235], v[168:171], v[68:71]
v_mfma_f32_16x16x32_bf16 v[68:71], a[236:239], v[172:175], v[68:71]
s_cmp_lg_u32 s70, 0
v_mfma_f32_16x16x32_bf16 v[72:75], a[240:243], v[168:171], v[72:75]
v_mfma_f32_16x16x32_bf16 v[72:75], a[244:247], v[172:175], v[72:75]
s_waitcnt vmcnt(16) lgkmcnt(0)
v_mfma_f32_16x16x32_bf16 v[76:79], a[248:251], v[168:171], v[76:79]
s_barrier
v_mfma_f32_16x16x32_bf16 v[76:79], a[252:255], v[172:175], v[76:79]
s_cbranch_scc1 L_attn_D
L_attn_Dm:
v_mfma_f32_16x16x32_bf16 v[96:99], a[128:131], a[80:83], v[180:183]
s_mov_b32 m0, s68
L_pad_a_3_2:
s_cbranch_execz L_pad_a_3_3
ds_read_b128 a[192:195], v12 offset:16384
v_add_f32 v80, v212, v176
v_mfma_f32_16x16x32_bf16 v[96:99], a[132:135], a[84:87], v[96:99]
s_sub_u32 s67, s66, 59392
v_exp_f32 v136, v136
v_mfma_f32_16x16x32_bf16 v[96:99], a[136:139], a[88:91], v[96:99]
s_add_u32 s69, s69, 32768
ds_read_b128 a[196:199], v12 offset:17408
v_add_f32 v81, v213, v176
v_mfma_f32_16x16x32_bf16 v[96:99], a[140:143], a[92:95], v[96:99]
s_and_b32 s69, s69, 131071
v_exp_f32 v137, v137
v_mfma_f32_16x16x32_bf16 v[100:103], a[144:147], a[80:83], v[180:183]
v_add_f32 v82, v214, v176
v_add_f32 v83, v215, v176
v_mfma_f32_16x16x32_bf16 v[100:103], a[148:151], a[84:87], v[100:103]
v_exp_f32 v138, v138
v_mfma_f32_16x16x32_bf16 v[100:103], a[152:155], a[88:91], v[100:103]
buffer_load_dwordx4 v1, s[56:59], s66 offen offset:0 sc1 lds
v_mfma_f32_16x16x32_bf16 v[100:103], a[156:159], a[92:95], v[100:103]
v_exp_f32 v139, v139
v_mfma_f32_16x16x32_bf16 v[104:107], a[160:163], a[80:83], v[180:183]
ds_read_b128 a[200:203], v12 offset:18432
v_add_f32 v84, v216, v176
v_mfma_f32_16x16x32_bf16 v[104:107], a[164:167], a[84:87], v[104:107]
v_exp_f32 v140, v140
v_mfma_f32_16x16x32_bf16 v[104:107], a[168:171], a[88:91], v[104:107]
v_add_f32 v85, v217, v176
v_add_f32 v86, v218, v176
v_mfma_f32_16x16x32_bf16 v[104:107], a[172:175], a[92:95], v[104:107]
v_exp_f32 v141, v141
v_mfma_f32_16x16x32_bf16 v[108:111], a[176:179], a[80:83], v[180:183]
ds_read_b128 a[204:207], v12 offset:19456
v_add_f32 v87, v219, v176
v_mfma_f32_16x16x32_bf16 v[108:111], a[180:183], a[84:87], v[108:111]
v_exp_f32 v142, v142
v_mfma_f32_16x16x32_bf16 v[108:111], a[184:187], a[88:91], v[108:111]
ds_read_b128 a[208:211], v12 offset:20480
v_add_f32 v88, v220, v176
v_mfma_f32_16x16x32_bf16 v[108:111], a[188:191], a[92:95], v[108:111]
v_exp_f32 v143, v143
v_mfma_f32_16x16x32_bf16 v[80:83], a[128:131], a[64:67], v[80:83]
v_add_f32 v89, v221, v176
v_add_f32 v90, v222, v176
v_mfma_f32_16x16x32_bf16 v[80:83], a[132:135], a[68:71], v[80:83]
v_exp_f32 v96, v96
v_mfma_f32_16x16x32_bf16 v[80:83], a[136:139], a[72:75], v[80:83]
ds_read_b128 a[212:215], v12 offset:21504
v_add_f32 v91, v223, v176
v_mfma_f32_16x16x32_bf16 v[80:83], a[140:143], a[76:79], v[80:83]
v_exp_f32 v97, v97
)ASM"
R"ASM(
v_mfma_f32_16x16x32_bf16 v[84:87], a[144:147], a[64:67], v[84:87]
v_add_f32 v92, v224, v176
v_add_f32 v93, v225, v176
v_mfma_f32_16x16x32_bf16 v[84:87], a[148:151], a[68:71], v[84:87]
v_exp_f32 v98, v98
v_mfma_f32_16x16x32_bf16 v[84:87], a[152:155], a[72:75], v[84:87]
buffer_load_dwordx4 v1, s[56:59], s66 offen offset:1024 sc1 lds
v_mfma_f32_16x16x32_bf16 v[84:87], a[156:159], a[76:79], v[84:87]
v_exp_f32 v99, v99
v_mfma_f32_16x16x32_bf16 v[88:91], a[160:163], a[64:67], v[88:91]
ds_read_b128 a[216:219], v12 offset:22528
v_add_f32 v94, v226, v176
v_mfma_f32_16x16x32_bf16 v[88:91], a[164:167], a[68:71], v[88:91]
v_exp_f32 v100, v100
v_mfma_f32_16x16x32_bf16 v[88:91], a[168:171], a[72:75], v[88:91]
ds_read_b128 a[220:223], v12 offset:23552
v_add_f32 v95, v227, v176
v_mfma_f32_16x16x32_bf16 v[88:91], a[172:175], a[76:79], v[88:91]
v_exp_f32 v101, v101
v_mfma_f32_16x16x32_bf16 v[92:95], a[176:179], a[64:67], v[92:95]
v_cvt_pk_bf16_f32 v168, v128, v129
v_cvt_pk_bf16_f32 v169, v130, v131
v_mfma_f32_16x16x32_bf16 v[92:95], a[180:183], a[68:71], v[92:95]
v_exp_f32 v102, v102
v_mfma_f32_16x16x32_bf16 v[92:95], a[184:187], a[72:75], v[92:95]
ds_read_b128 a[224:227], v12 offset:24576
v_cvt_pk_bf16_f32 v170, v132, v133
v_mfma_f32_16x16x32_bf16 v[92:95], a[188:191], a[76:79], v[92:95]
v_exp_f32 v103, v103
v_mfma_f32_16x16x32_bf16 v[112:115], a[128:131], a[96:99], v[184:187]
v_cvt_pk_bf16_f32 v171, v134, v135
v_cvt_pk_bf16_f32 v172, v136, v137
v_mfma_f32_16x16x32_bf16 v[112:115], a[132:135], a[100:103], v[112:115]
v_exp_f32 v104, v104
v_mfma_f32_16x16x32_bf16 v[112:115], a[136:139], a[104:107], v[112:115]
ds_read_b128 a[228:231], v12 offset:25600
v_cvt_pk_bf16_f32 v173, v138, v139
v_mfma_f32_16x16x32_bf16 v[112:115], a[140:143], a[108:111], v[112:115]
v_exp_f32 v105, v105
v_mfma_f32_16x16x32_bf16 v[116:119], a[144:147], a[96:99], v[184:187]
ds_read_b128 a[232:235], v12 offset:26624
v_cvt_pk_bf16_f32 v174, v140, v141
v_mfma_f32_16x16x32_bf16 v[116:119], a[148:151], a[100:103], v[116:119]
v_exp_f32 v106, v106
v_mfma_f32_16x16x32_bf16 v[116:119], a[152:155], a[104:107], v[116:119]
buffer_load_dwordx4 v1, s[56:59], s66 offen offset:2048 sc1 lds
v_mfma_f32_16x16x32_bf16 v[116:119], a[156:159], a[108:111], v[116:119]
v_exp_f32 v107, v107
v_mfma_f32_16x16x32_bf16 v[120:123], a[160:163], a[96:99], v[184:187]
v_cvt_pk_bf16_f32 v175, v142, v143
v_mfma_f32_16x16x32_bf16 v[120:123], a[164:167], a[100:103], v[120:123]
v_exp_f32 v108, v108
v_mfma_f32_16x16x32_bf16 v[120:123], a[168:171], a[104:107], v[120:123]
ds_read_b128 a[236:239], v12 offset:27648
v_mfma_f32_16x16x32_bf16 v[120:123], a[172:175], a[108:111], v[120:123]
v_exp_f32 v109, v109
v_mfma_f32_16x16x32_bf16 v[124:127], a[176:179], a[96:99], v[184:187]
v_mfma_f32_16x16x32_bf16 v[124:127], a[180:183], a[100:103], v[124:127]
v_exp_f32 v110, v110
v_mfma_f32_16x16x32_bf16 v[124:127], a[184:187], a[104:107], v[124:127]
ds_read_b128 a[240:243], v12 offset:28672
v_mfma_f32_16x16x32_bf16 v[124:127], a[188:191], a[108:111], v[124:127]
v_exp_f32 v111, v111
v_mfma_f32_16x16x32_bf16 v[128:131], a[128:131], a[112:115], v[188:191]
ds_read_b128 a[244:247], v12 offset:29696
v_mfma_f32_16x16x32_bf16 v[128:131], a[132:135], a[116:119], v[128:131]
v_exp_f32 v80, v80
v_mfma_f32_16x16x32_bf16 v[128:131], a[136:139], a[120:123], v[128:131]
v_mfma_f32_16x16x32_bf16 v[128:131], a[140:143], a[124:127], v[128:131]
v_exp_f32 v81, v81
v_mfma_f32_16x16x32_bf16 v[132:135], a[144:147], a[112:115], v[188:191]
ds_read_b128 a[248:251], v12 offset:30720
v_mfma_f32_16x16x32_bf16 v[132:135], a[148:151], a[116:119], v[132:135]
v_exp_f32 v82, v82
v_mfma_f32_16x16x32_bf16 v[132:135], a[152:155], a[120:123], v[132:135]
buffer_load_dwordx4 v1, s[56:59], s66 offen offset:3072 sc1 lds
v_mfma_f32_16x16x32_bf16 v[132:135], a[156:159], a[124:127], v[132:135]
s_add_u32 m0, s68, 16384
v_exp_f32 v83, v83
v_mfma_f32_16x16x32_bf16 v[136:139], a[160:163], a[112:115], v[188:191]
s_add_u32 s66, s66, 37888
v_mfma_f32_16x16x32_bf16 v[136:139], a[164:167], a[116:119], v[136:139]
s_add_u32 s68, s68, 32768
v_exp_f32 v84, v84
v_mfma_f32_16x16x32_bf16 v[136:139], a[168:171], a[120:123], v[136:139]
s_and_b32 s68, s68, 131071
ds_read_b128 a[252:255], v12 offset:31744
v_mfma_f32_16x16x32_bf16 v[136:139], a[172:175], a[124:127], v[136:139]
v_exp_f32 v85, v85
v_mfma_f32_16x16x32_bf16 v[140:143], a[176:179], a[112:115], v[188:191]
v_mfma_f32_16x16x32_bf16 v[140:143], a[180:183], a[116:119], v[140:143]
v_exp_f32 v86, v86
v_mfma_f32_16x16x32_bf16 v[140:143], a[184:187], a[120:123], v[140:143]
ds_read_b128 a[128:131], v12 offset:0
v_mfma_f32_16x16x32_bf16 v[140:143], a[188:191], a[124:127], v[140:143]
v_exp_f32 v87, v87
v_mfma_f32_16x16x32_bf16 v[192:195], v[208:211], v[144:147], v[192:195]
ds_read_b128 a[132:135], v12 offset:1024
v_mfma_f32_16x16x32_bf16 v[192:195], v[208:211], v[148:151], v[192:195]
s_waitcnt lgkmcnt(2)
v_exp_f32 v88, v88
v_mfma_f32_16x16x32_bf16 a[0:3], a[192:195], v[144:147], a[0:3]
v_mfma_f32_16x16x32_bf16 a[0:3], a[196:199], v[148:151], a[0:3]
v_exp_f32 v89, v89
v_mfma_f32_16x16x32_bf16 a[4:7], a[200:203], v[144:147], a[4:7]
ds_read_b128 a[136:139], v12 offset:2048
v_mfma_f32_16x16x32_bf16 a[4:7], a[204:207], v[148:151], a[4:7]
v_exp_f32 v90, v90
v_mfma_f32_16x16x32_bf16 a[8:11], a[208:211], v[144:147], a[8:11]
buffer_load_dwordx4 v1, s[56:59], s67 offen offset:0 sc1 lds
v_mfma_f32_16x16x32_bf16 a[8:11], a[212:215], v[148:151], a[8:11]
v_exp_f32 v91, v91
v_mfma_f32_16x16x32_bf16 a[12:15], a[216:219], v[144:147], a[12:15]
v_mfma_f32_16x16x32_bf16 a[12:15], a[220:223], v[148:151], a[12:15]
v_exp_f32 v92, v92
v_mfma_f32_16x16x32_bf16 a[16:19], a[224:227], v[144:147], a[16:19]
ds_read_b128 a[140:143], v12 offset:3072
v_mfma_f32_16x16x32_bf16 a[16:19], a[228:231], v[148:151], a[16:19]
v_exp_f32 v93, v93
v_mfma_f32_16x16x32_bf16 a[20:23], a[232:235], v[144:147], a[20:23]
v_mfma_f32_16x16x32_bf16 a[20:23], a[236:239], v[148:151], a[20:23]
v_exp_f32 v94, v94
v_mfma_f32_16x16x32_bf16 a[24:27], a[240:243], v[144:147], a[24:27]
ds_read_b128 a[144:147], v12 offset:4096
v_mfma_f32_16x16x32_bf16 a[24:27], a[244:247], v[148:151], a[24:27]
v_exp_f32 v95, v95
v_mfma_f32_16x16x32_bf16 a[28:31], a[248:251], v[144:147], a[28:31]
v_cvt_pk_bf16_f32 v144, v80, v81
v_cvt_pk_bf16_f32 v145, v82, v83
v_mfma_f32_16x16x32_bf16 a[28:31], a[252:255], v[148:151], a[28:31]
v_exp_f32 v112, v112
v_mfma_f32_16x16x32_bf16 v[196:199], v[208:211], v[152:155], v[196:199]
ds_read_b128 a[148:151], v12 offset:5120
v_cvt_pk_bf16_f32 v146, v84, v85
v_mfma_f32_16x16x32_bf16 v[196:199], v[208:211], v[156:159], v[196:199]
v_exp_f32 v113, v113
v_mfma_f32_16x16x32_bf16 a[32:35], a[192:195], v[152:155], a[32:35]
ds_read_b128 a[152:155], v12 offset:6144
v_cvt_pk_bf16_f32 v147, v86, v87
v_mfma_f32_16x16x32_bf16 a[32:35], a[196:199], v[156:159], a[32:35]
v_exp_f32 v114, v114
v_mfma_f32_16x16x32_bf16 a[36:39], a[200:203], v[152:155], a[36:39]
buffer_load_dwordx4 v1, s[56:59], s67 offen offset:1024 sc1 lds
v_mfma_f32_16x16x32_bf16 a[36:39], a[204:207], v[156:159], a[36:39]
v_exp_f32 v115, v115
v_mfma_f32_16x16x32_bf16 a[40:43], a[208:211], v[152:155], a[40:43]
v_cvt_pk_bf16_f32 v148, v88, v89
v_cvt_pk_bf16_f32 v149, v90, v91
v_mfma_f32_16x16x32_bf16 a[40:43], a[212:215], v[156:159], a[40:43]
v_exp_f32 v116, v116
v_mfma_f32_16x16x32_bf16 a[44:47], a[216:219], v[152:155], a[44:47]
ds_read_b128 a[156:159], v12 offset:7168
v_cvt_pk_bf16_f32 v150, v92, v93
v_mfma_f32_16x16x32_bf16 a[44:47], a[220:223], v[156:159], a[44:47]
v_exp_f32 v117, v117
v_mfma_f32_16x16x32_bf16 a[48:51], a[224:227], v[152:155], a[48:51]
v_cvt_pk_bf16_f32 v151, v94, v95
v_mfma_f32_16x16x32_bf16 a[48:51], a[228:231], v[156:159], a[48:51]
v_exp_f32 v118, v118
v_mfma_f32_16x16x32_bf16 a[52:55], a[232:235], v[152:155], a[52:55]
ds_read_b128 a[160:163], v12 offset:8192
v_mfma_f32_16x16x32_bf16 a[52:55], a[236:239], v[156:159], a[52:55]
v_exp_f32 v119, v119
v_mfma_f32_16x16x32_bf16 a[56:59], a[240:243], v[152:155], a[56:59]
v_mfma_f32_16x16x32_bf16 a[56:59], a[244:247], v[156:159], a[56:59]
v_exp_f32 v120, v120
v_mfma_f32_16x16x32_bf16 a[60:63], a[248:251], v[152:155], a[60:63]
ds_read_b128 a[164:167], v12 offset:9216
v_cvt_pk_bf16_f32 v152, v96, v97
v_mfma_f32_16x16x32_bf16 a[60:63], a[252:255], v[156:159], a[60:63]
v_exp_f32 v121, v121
v_mfma_f32_16x16x32_bf16 v[200:203], v[208:211], v[160:163], v[200:203]
v_cvt_pk_bf16_f32 v153, v98, v99
v_cvt_pk_bf16_f32 v154, v100, v101
v_mfma_f32_16x16x32_bf16 v[200:203], v[208:211], v[164:167], v[200:203]
v_exp_f32 v122, v122
v_mfma_f32_16x16x32_bf16 v[16:19], a[192:195], v[160:163], v[16:19]
buffer_load_dwordx4 v1, s[56:59], s67 offen offset:2048 sc1 lds
v_mfma_f32_16x16x32_bf16 v[16:19], a[196:199], v[164:167], v[16:19]
v_exp_f32 v123, v123
v_mfma_f32_16x16x32_bf16 v[20:23], a[200:203], v[160:163], v[20:23]
ds_read_b128 a[168:171], v12 offset:10240
v_cvt_pk_bf16_f32 v155, v102, v103
v_mfma_f32_16x16x32_bf16 v[20:23], a[204:207], v[164:167], v[20:23]
v_exp_f32 v124, v124
v_mfma_f32_16x16x32_bf16 v[24:27], a[208:211], v[160:163], v[24:27]
ds_read_b128 a[172:175], v12 offset:11264
v_cvt_pk_bf16_f32 v156, v104, v105
v_mfma_f32_16x16x32_bf16 v[24:27], a[212:215], v[164:167], v[24:27]
v_exp_f32 v125, v125
v_mfma_f32_16x16x32_bf16 v[28:31], a[216:219], v[160:163], v[28:31]
v_cvt_pk_bf16_f32 v157, v106, v107
v_cvt_pk_bf16_f32 v158, v108, v109
v_mfma_f32_16x16x32_bf16 v[28:31], a[220:223], v[164:167], v[28:31]
v_exp_f32 v126, v126
v_mfma_f32_16x16x32_bf16 v[32:35], a[224:227], v[160:163], v[32:35]
ds_read_b128 a[176:179], v12 offset:12288
v_cvt_pk_bf16_f32 v159, v110, v111
v_mfma_f32_16x16x32_bf16 v[32:35], a[228:231], v[164:167], v[32:35]
v_exp_f32 v127, v127
v_mfma_f32_16x16x32_bf16 v[36:39], a[232:235], v[160:163], v[36:39]
v_mfma_f32_16x16x32_bf16 v[36:39], a[236:239], v[164:167], v[36:39]
v_exp_f32 v128, v128
v_mfma_f32_16x16x32_bf16 v[40:43], a[240:243], v[160:163], v[40:43]
ds_read_b128 a[180:183], v12 offset:13312
v_mfma_f32_16x16x32_bf16 v[40:43], a[244:247], v[164:167], v[40:43]
v_exp_f32 v129, v129
v_mfma_f32_16x16x32_bf16 v[44:47], a[248:251], v[160:163], v[44:47]
v_cvt_pk_bf16_f32 v160, v112, v113
v_cvt_pk_bf16_f32 v161, v114, v115
v_mfma_f32_16x16x32_bf16 v[44:47], a[252:255], v[164:167], v[44:47]
v_exp_f32 v130, v130
v_mfma_f32_16x16x32_bf16 v[204:207], v[208:211], v[168:171], v[204:207]
buffer_load_dwordx4 v1, s[56:59], s67 offen offset:3072 sc1 lds
v_mfma_f32_16x16x32_bf16 v[204:207], v[208:211], v[172:175], v[204:207]
v_exp_f32 v131, v131
v_mfma_f32_16x16x32_bf16 v[48:51], a[192:195], v[168:171], v[48:51]
ds_read_b128 a[184:187], v12 offset:14336
v_cvt_pk_bf16_f32 v162, v116, v117
v_mfma_f32_16x16x32_bf16 v[48:51], a[196:199], v[172:175], v[48:51]
v_exp_f32 v132, v132
v_mfma_f32_16x16x32_bf16 v[52:55], a[200:203], v[168:171], v[52:55]
v_cvt_pk_bf16_f32 v163, v118, v119
v_cvt_pk_bf16_f32 v164, v120, v121
v_mfma_f32_16x16x32_bf16 v[52:55], a[204:207], v[172:175], v[52:55]
v_exp_f32 v133, v133
v_mfma_f32_16x16x32_bf16 v[56:59], a[208:211], v[168:171], v[56:59]
ds_read_b128 a[188:191], v12 offset:15360
v_cvt_pk_bf16_f32 v165, v122, v123
v_mfma_f32_16x16x32_bf16 v[56:59], a[212:215], v[172:175], v[56:59]
v_exp_f32 v134, v134
v_mfma_f32_16x16x32_bf16 v[60:63], a[216:219], v[168:171], v[60:63]
v_cvt_pk_bf16_f32 v166, v124, v125
v_cvt_pk_bf16_f32 v167, v126, v127
v_mfma_f32_16x16x32_bf16 v[60:63], a[220:223], v[172:175], v[60:63]
v_exp_f32 v135, v135
v_mfma_f32_16x16x32_bf16 v[64:67], a[224:227], v[168:171], v[64:67]
v_add_u32 v12, s69, v1
v_mfma_f32_16x16x32_bf16 v[64:67], a[228:231], v[172:175], v[64:67]
v_mfma_f32_16x16x32_bf16 v[68:71], a[232:235], v[168:171], v[68:71]
v_mfma_f32_16x16x32_bf16 v[68:71], a[236:239], v[172:175], v[68:71]
v_mfma_f32_16x16x32_bf16 v[72:75], a[240:243], v[168:171], v[72:75]
v_mfma_f32_16x16x32_bf16 v[72:75], a[244:247], v[172:175], v[72:75]
s_waitcnt vmcnt(16) lgkmcnt(0)
v_mfma_f32_16x16x32_bf16 v[76:79], a[248:251], v[168:171], v[76:79]
s_barrier
v_mfma_f32_16x16x32_bf16 v[76:79], a[252:255], v[172:175], v[76:79]
L_attn_T1:
L_pad_a_0_3:
s_cbranch_execz L_pad_a_0_4
v_mfma_f32_16x16x32_bf16 v[112:115], a[128:131], a[96:99], v[184:187]
s_and_b32 s78, s63, 7
s_lshl_b32 s78, s78, 1
s_lshr_b32 s83, s63, 7
v_mfma_f32_16x16x32_bf16 v[112:115], a[132:135], a[100:103], v[112:115]
s_add_u32 s78, s78, s83
s_lshr_b32 s79, s63, 3
s_and_b32 s79, s79, 15
v_mfma_f32_16x16x32_bf16 v[112:115], a[136:139], a[104:107], v[112:115]
s_lshr_b32 s83, s79, 3
s_lshl_b32 s80, s78, 1
s_add_u32 s80, s80, s83
v_mfma_f32_16x16x32_bf16 v[112:115], a[140:143], a[108:111], v[112:115]
s_and_b32 s81, s79, 7
s_lshl_b32 s83, s80, 2
s_add_u32 s83, s83, s60
v_mfma_f32_16x16x32_bf16 v[116:119], a[144:147], a[96:99], v[184:187]
s_mul_i32 s83, s83, 20480
s_lshr_b32 s84, s81, 1
v_lshrrev_b32 v0, 2, v2
v_mfma_f32_16x16x32_bf16 v[116:119], a[148:151], a[100:103], v[116:119]
s_mul_i32 s84, s84, 320
s_add_u32 s83, s83, s84
v_mul_u32_u24 v0, 1280, v0
v_mfma_f32_16x16x32_bf16 v[116:119], a[152:155], a[104:107], v[116:119]
s_and_b32 s84, s81, 1
s_lshl_b32 s85, s60, 12
v_and_b32 v3, 3, v2
v_mfma_f32_16x16x32_bf16 v[116:119], a[156:159], a[108:111], v[116:119]
s_lshl_b32 s84, s84, 7
s_add_u32 m0, s85, 131072
v_lshl_add_u32 v0, v3, 4, v0
v_mfma_f32_16x16x32_bf16 v[120:123], a[160:163], a[96:99], v[184:187]
s_add_u32 s83, s83, s84
ds_read_b128 a[192:195], v12 offset:16384
ds_read_b128 a[196:199], v12 offset:17408
v_mfma_f32_16x16x32_bf16 v[120:123], a[164:167], a[100:103], v[120:123]
buffer_load_dwordx4 v0, s[8:11], s83 offen offset:0 lds
v_mfma_f32_16x16x32_bf16 v[120:123], a[168:171], a[104:107], v[120:123]
s_add_u32 m0, s85, 132032
ds_read_b128 a[200:203], v12 offset:18432
ds_read_b128 a[204:207], v12 offset:19456
v_mfma_f32_16x16x32_bf16 v[120:123], a[172:175], a[108:111], v[120:123]
s_sub_u32 s67, s66, 59392
ds_read_b128 a[208:211], v12 offset:20480
v_add_f32 v96, v212, v180
v_mfma_f32_16x16x32_bf16 v[124:127], a[176:179], a[96:99], v[184:187]
s_add_u32 s69, s69, 32768
v_add_f32 v97, v213, v180
v_add_f32 v98, v214, v180
v_mfma_f32_16x16x32_bf16 v[124:127], a[180:183], a[100:103], v[124:127]
buffer_load_dwordx4 v0, s[8:11], s83 offen offset:64 lds
v_mfma_f32_16x16x32_bf16 v[124:127], a[184:187], a[104:107], v[124:127]
s_add_u32 m0, s85, 132992
ds_read_b128 a[212:215], v12 offset:21504
v_add_f32 v99, v215, v180
v_mfma_f32_16x16x32_bf16 v[124:127], a[188:191], a[108:111], v[124:127]
s_and_b32 s69, s69, 131071
v_add_f32 v100, v216, v180
v_add_f32 v101, v217, v180
v_mfma_f32_16x16x32_bf16 v[96:99], a[128:131], a[80:83], v[96:99]
v_add_f32 v102, v218, v180
v_add_f32 v103, v219, v180
v_mfma_f32_16x16x32_bf16 v[96:99], a[132:135], a[84:87], v[96:99]
buffer_load_dwordx4 v0, s[8:11], s83 offen offset:128 lds
v_mfma_f32_16x16x32_bf16 v[96:99], a[136:139], a[88:91], v[96:99]
s_mov_b32 m0, s68
ds_read_b128 a[216:219], v12 offset:22528
v_add_f32 v104, v220, v180
v_mfma_f32_16x16x32_bf16 v[96:99], a[140:143], a[92:95], v[96:99]
v_add_f32 v105, v221, v180
v_add_f32 v106, v222, v180
v_mfma_f32_16x16x32_bf16 v[100:103], a[144:147], a[80:83], v[100:103]
ds_read_b128 a[220:223], v12 offset:23552
v_add_f32 v107, v223, v180
v_mfma_f32_16x16x32_bf16 v[100:103], a[148:151], a[84:87], v[100:103]
buffer_load_dwordx4 v1, s[56:59], s66 offen offset:0 sc1 lds
v_mfma_f32_16x16x32_bf16 v[100:103], a[152:155], a[88:91], v[100:103]
v_add_f32 v108, v224, v180
v_add_f32 v109, v225, v180
v_mfma_f32_16x16x32_bf16 v[100:103], a[156:159], a[92:95], v[100:103]
v_add_f32 v110, v226, v180
v_add_f32 v111, v227, v180
v_mfma_f32_16x16x32_bf16 v[104:107], a[160:163], a[80:83], v[104:107]
ds_read_b128 a[224:227], v12 offset:24576
v_cvt_pk_bf16_f32 v168, v128, v129
v_mfma_f32_16x16x32_bf16 v[104:107], a[164:167], a[84:87], v[104:107]
buffer_load_dwordx4 v1, s[56:59], s66 offen offset:1024 sc1 lds
v_mfma_f32_16x16x32_bf16 v[104:107], a[168:171], a[88:91], v[104:107]
ds_read_b128 a[228:231], v12 offset:25600
v_cvt_pk_bf16_f32 v169, v130, v131
v_mfma_f32_16x16x32_bf16 v[104:107], a[172:175], a[92:95], v[104:107]
ds_read_b128 a[232:235], v12 offset:26624
v_cvt_pk_bf16_f32 v170, v132, v133
v_mfma_f32_16x16x32_bf16 v[108:111], a[176:179], a[80:83], v[108:111]
ds_read_b128 a[236:239], v12 offset:27648
v_cvt_pk_bf16_f32 v171, v134, v135
v_mfma_f32_16x16x32_bf16 v[108:111], a[180:183], a[84:87], v[108:111]
buffer_load_dwordx4 v1, s[56:59], s66 offen offset:2048 sc1 lds
v_mfma_f32_16x16x32_bf16 v[108:111], a[184:187], a[88:91], v[108:111]
v_exp_f32 v136, v136
v_mfma_f32_16x16x32_bf16 v[108:111], a[188:191], a[92:95], v[108:111]
v_exp_f32 v137, v137
v_mfma_f32_16x16x32_bf16 v[128:131], a[128:131], a[112:115], v[188:191]
v_exp_f32 v138, v138
v_mfma_f32_16x16x32_bf16 v[128:131], a[132:135], a[116:119], v[128:131]
v_exp_f32 v139, v139
v_mfma_f32_16x16x32_bf16 v[128:131], a[136:139], a[120:123], v[128:131]
v_cvt_pk_bf16_f32 v172, v136, v137
v_cvt_pk_bf16_f32 v173, v138, v139
v_mfma_f32_16x16x32_bf16 v[128:131], a[140:143], a[124:127], v[128:131]
v_exp_f32 v140, v140
v_mfma_f32_16x16x32_bf16 v[132:135], a[144:147], a[112:115], v[188:191]
v_exp_f32 v141, v141
v_mfma_f32_16x16x32_bf16 v[132:135], a[148:151], a[116:119], v[132:135]
v_exp_f32 v142, v142
v_mfma_f32_16x16x32_bf16 v[132:135], a[152:155], a[120:123], v[132:135]
ds_read_b128 a[240:243], v12 offset:28672
v_cvt_pk_bf16_f32 v174, v140, v141
v_mfma_f32_16x16x32_bf16 v[132:135], a[156:159], a[124:127], v[132:135]
v_exp_f32 v143, v143
v_mfma_f32_16x16x32_bf16 v[136:139], a[160:163], a[112:115], v[188:191]
ds_read_b128 a[244:247], v12 offset:29696
v_cvt_pk_bf16_f32 v175, v142, v143
v_mfma_f32_16x16x32_bf16 v[136:139], a[164:167], a[116:119], v[136:139]
v_exp_f32 v112, v112
v_mfma_f32_16x16x32_bf16 v[136:139], a[168:171], a[120:123], v[136:139]
v_exp_f32 v113, v113
v_mfma_f32_16x16x32_bf16 v[136:139], a[172:175], a[124:127], v[136:139]
buffer_load_dwordx4 v1, s[56:59], s66 offen offset:3072 sc1 lds
v_mfma_f32_16x16x32_bf16 v[140:143], a[176:179], a[112:115], v[188:191]
s_add_u32 m0, s68, 16384
ds_read_b128 a[248:251], v12 offset:30720
ds_read_b128 a[128:131], v12 offset:0
v_mfma_f32_16x16x32_bf16 v[140:143], a[180:183], a[116:119], v[140:143]
s_add_u32 s68, s68, 32768
v_exp_f32 v114, v114
v_mfma_f32_16x16x32_bf16 v[140:143], a[184:187], a[120:123], v[140:143]
s_add_u32 s66, s66, 37888
v_exp_f32 v115, v115
v_mfma_f32_16x16x32_bf16 v[140:143], a[188:191], a[124:127], v[140:143]
s_and_b32 s68, s68, 131071
ds_read_b128 a[252:255], v12 offset:31744
ds_read_b128 a[132:135], v12 offset:1024
v_mfma_f32_16x16x32_bf16 v[192:195], v[208:211], v[144:147], v[192:195]
v_exp_f32 v116, v116
v_mfma_f32_16x16x32_bf16 v[192:195], v[208:211], v[148:151], v[192:195]
s_waitcnt lgkmcnt(2)
v_exp_f32 v117, v117
v_mfma_f32_16x16x32_bf16 a[0:3], a[192:195], v[144:147], a[0:3]
)ASM"
R"ASM(
v_exp_f32 v118, v118
v_mfma_f32_16x16x32_bf16 a[0:3], a[196:199], v[148:151], a[0:3]
v_exp_f32 v119, v119
v_mfma_f32_16x16x32_bf16 a[4:7], a[200:203], v[144:147], a[4:7]
v_exp_f32 v120, v120
v_mfma_f32_16x16x32_bf16 a[4:7], a[204:207], v[148:151], a[4:7]
v_rcp_f32 v228, v192
v_mfma_f32_16x16x32_bf16 a[8:11], a[208:211], v[144:147], a[8:11]
ds_read_b128 a[136:139], v12 offset:2048
ds_read_b128 a[140:143], v12 offset:3072
v_mfma_f32_16x16x32_bf16 a[8:11], a[212:215], v[148:151], a[8:11]
ds_read_b128 a[144:147], v12 offset:4096
v_accvgpr_read_b32 v232, a0
v_mfma_f32_16x16x32_bf16 a[12:15], a[216:219], v[144:147], a[12:15]
v_mul_f32 v232, v228, v232
v_accvgpr_read_b32 v233, a1
v_mfma_f32_16x16x32_bf16 a[12:15], a[220:223], v[148:151], a[12:15]
buffer_load_dwordx4 v1, s[56:59], s67 offen offset:0 sc1 lds
v_mfma_f32_16x16x32_bf16 a[16:19], a[224:227], v[144:147], a[16:19]
v_mul_f32 v233, v228, v233
v_accvgpr_read_b32 v234, a2
v_mfma_f32_16x16x32_bf16 a[16:19], a[228:231], v[148:151], a[16:19]
v_mul_f32 v234, v228, v234
v_accvgpr_read_b32 v235, a3
v_mfma_f32_16x16x32_bf16 a[20:23], a[232:235], v[144:147], a[20:23]
v_mul_f32 v235, v228, v235
v_accvgpr_read_b32 v236, a4
v_mfma_f32_16x16x32_bf16 a[20:23], a[236:239], v[148:151], a[20:23]
v_accvgpr_read_b32 v237, a5
v_accvgpr_read_b32 v238, a6
v_mfma_f32_16x16x32_bf16 a[24:27], a[240:243], v[144:147], a[24:27]
v_accvgpr_read_b32 v239, a7
v_cvt_pk_bf16_f32 v232, v232, v233
v_mfma_f32_16x16x32_bf16 a[24:27], a[244:247], v[148:151], a[24:27]
v_mul_f32 v236, v228, v236
v_mul_f32 v237, v228, v237
v_mfma_f32_16x16x32_bf16 a[28:31], a[248:251], v[144:147], a[28:31]
s_waitcnt lgkmcnt(0)
v_mul_f32 v238, v228, v238
v_mul_f32 v239, v228, v239
v_mfma_f32_16x16x32_bf16 a[28:31], a[252:255], v[148:151], a[28:31]
ds_read_b128 a[148:151], v12 offset:5120
v_cvt_pk_bf16_f32 v233, v234, v235
v_mfma_f32_16x16x32_bf16 v[196:199], v[208:211], v[152:155], v[196:199]
v_cvt_pk_bf16_f32 v234, v236, v237
v_cvt_pk_bf16_f32 v235, v238, v239
v_mfma_f32_16x16x32_bf16 v[196:199], v[208:211], v[156:159], v[196:199]
buffer_store_dwordx4 v[232:235], v1, s[52:55], s73 offen offset:0 sc1
v_mfma_f32_16x16x32_bf16 a[32:35], a[192:195], v[152:155], a[32:35]
ds_read_b128 a[152:155], v12 offset:6144
v_accvgpr_read_b32 v242, a8
v_mfma_f32_16x16x32_bf16 a[32:35], a[196:199], v[156:159], a[32:35]
ds_read_b128 a[156:159], v12 offset:7168
v_accvgpr_read_b32 v243, a9
v_mfma_f32_16x16x32_bf16 a[36:39], a[200:203], v[152:155], a[36:39]
ds_read_b128 a[160:163], v12 offset:8192
v_mul_f32 v242, v228, v242
v_mfma_f32_16x16x32_bf16 a[36:39], a[204:207], v[156:159], a[36:39]
v_mul_f32 v243, v228, v243
v_accvgpr_read_b32 v244, a10
v_mfma_f32_16x16x32_bf16 a[40:43], a[208:211], v[152:155], a[40:43]
v_mul_f32 v244, v228, v244
v_accvgpr_read_b32 v245, a11
v_mfma_f32_16x16x32_bf16 a[40:43], a[212:215], v[156:159], a[40:43]
v_mul_f32 v245, v228, v245
v_accvgpr_read_b32 v246, a12
v_mfma_f32_16x16x32_bf16 a[44:47], a[216:219], v[152:155], a[44:47]
buffer_load_dwordx4 v1, s[56:59], s67 offen offset:1024 sc1 lds
v_mfma_f32_16x16x32_bf16 a[44:47], a[220:223], v[156:159], a[44:47]
v_accvgpr_read_b32 v247, a13
v_accvgpr_read_b32 v248, a14
v_mfma_f32_16x16x32_bf16 a[48:51], a[224:227], v[152:155], a[48:51]
v_accvgpr_read_b32 v249, a15
v_cvt_pk_bf16_f32 v242, v242, v243
v_mfma_f32_16x16x32_bf16 a[48:51], a[228:231], v[156:159], a[48:51]
v_mul_f32 v246, v228, v246
v_mul_f32 v247, v228, v247
v_mfma_f32_16x16x32_bf16 a[52:55], a[232:235], v[152:155], a[52:55]
v_mul_f32 v248, v228, v248
v_mul_f32 v249, v228, v249
v_mfma_f32_16x16x32_bf16 a[52:55], a[236:239], v[156:159], a[52:55]
v_cvt_pk_bf16_f32 v243, v244, v245
v_cvt_pk_bf16_f32 v244, v246, v247
v_mfma_f32_16x16x32_bf16 a[56:59], a[240:243], v[152:155], a[56:59]
ds_read_b128 a[164:167], v12 offset:9216
v_cvt_pk_bf16_f32 v245, v248, v249
v_mfma_f32_16x16x32_bf16 a[56:59], a[244:247], v[156:159], a[56:59]
buffer_store_dwordx4 v[242:245], v1, s[52:55], s73 offen offset:1024 sc1
v_mfma_f32_16x16x32_bf16 a[60:63], a[248:251], v[152:155], a[60:63]
ds_read_b128 a[168:171], v12 offset:10240
v_accvgpr_read_b32 v232, a16
v_mfma_f32_16x16x32_bf16 a[60:63], a[252:255], v[156:159], a[60:63]
ds_read_b128 a[172:175], v12 offset:11264
v_accvgpr_read_b32 v233, a17
v_mfma_f32_16x16x32_bf16 v[200:203], v[208:211], v[160:163], v[200:203]
v_mul_f32 v232, v228, v232
v_mul_f32 v233, v228, v233
v_mfma_f32_16x16x32_bf16 v[200:203], v[208:211], v[164:167], v[200:203]
v_accvgpr_read_b32 v234, a18
v_accvgpr_read_b32 v235, a19
v_mfma_f32_16x16x32_bf16 v[16:19], a[192:195], v[160:163], v[16:19]
ds_read_b128 a[176:179], v12 offset:12288
v_mul_f32 v234, v228, v234
v_mfma_f32_16x16x32_bf16 v[16:19], a[196:199], v[164:167], v[16:19]
v_mul_f32 v235, v228, v235
v_accvgpr_read_b32 v236, a20
v_mfma_f32_16x16x32_bf16 v[20:23], a[200:203], v[160:163], v[20:23]
buffer_load_dwordx4 v1, s[56:59], s67 offen offset:2048 sc1 lds
v_mfma_f32_16x16x32_bf16 v[20:23], a[204:207], v[164:167], v[20:23]
v_accvgpr_read_b32 v237, a21
v_accvgpr_read_b32 v238, a22
v_mfma_f32_16x16x32_bf16 v[24:27], a[208:211], v[160:163], v[24:27]
v_accvgpr_read_b32 v239, a23
v_cvt_pk_bf16_f32 v232, v232, v233
v_mfma_f32_16x16x32_bf16 v[24:27], a[212:215], v[164:167], v[24:27]
L_pad_a_1_3:
s_cbranch_execz L_padret_a
v_mul_f32 v236, v228, v236
v_mul_f32 v237, v228, v237
v_mfma_f32_16x16x32_bf16 v[28:31], a[216:219], v[160:163], v[28:31]
v_mul_f32 v238, v228, v238
v_mul_f32 v239, v228, v239
v_mfma_f32_16x16x32_bf16 v[28:31], a[220:223], v[164:167], v[28:31]
v_cvt_pk_bf16_f32 v233, v234, v235
v_cvt_pk_bf16_f32 v234, v236, v237
v_mfma_f32_16x16x32_bf16 v[32:35], a[224:227], v[160:163], v[32:35]
ds_read_b128 a[180:183], v12 offset:13312
v_cvt_pk_bf16_f32 v235, v238, v239
v_mfma_f32_16x16x32_bf16 v[32:35], a[228:231], v[164:167], v[32:35]
buffer_store_dwordx4 v[232:235], v1, s[52:55], s73 offen offset:2048 sc1
v_mfma_f32_16x16x32_bf16 v[36:39], a[232:235], v[160:163], v[36:39]
ds_read_b128 a[184:187], v12 offset:14336
v_accvgpr_read_b32 v242, a24
v_mfma_f32_16x16x32_bf16 v[36:39], a[236:239], v[164:167], v[36:39]
v_mul_f32 v242, v228, v242
v_accvgpr_read_b32 v243, a25
v_mfma_f32_16x16x32_bf16 v[40:43], a[240:243], v[160:163], v[40:43]
ds_read_b128 a[188:191], v12 offset:15360
v_mul_f32 v243, v228, v243
v_mfma_f32_16x16x32_bf16 v[40:43], a[244:247], v[164:167], v[40:43]
v_accvgpr_read_b32 v244, a26
v_accvgpr_read_b32 v245, a27
v_mfma_f32_16x16x32_bf16 v[44:47], a[248:251], v[160:163], v[44:47]
v_mul_f32 v244, v228, v244
v_mul_f32 v245, v228, v245
v_mfma_f32_16x16x32_bf16 v[44:47], a[252:255], v[164:167], v[44:47]
v_accvgpr_read_b32 v246, a28
v_accvgpr_read_b32 v247, a29
v_mfma_f32_16x16x32_bf16 v[204:207], v[208:211], v[168:171], v[204:207]
buffer_load_dwordx4 v1, s[56:59], s67 offen offset:3072 sc1 lds
v_mfma_f32_16x16x32_bf16 v[204:207], v[208:211], v[172:175], v[204:207]
v_accvgpr_read_b32 v248, a30
v_accvgpr_read_b32 v249, a31
v_mfma_f32_16x16x32_bf16 v[48:51], a[192:195], v[168:171], v[48:51]
v_mul_f32 v246, v228, v246
v_cvt_pk_bf16_f32 v242, v242, v243
v_mfma_f32_16x16x32_bf16 v[48:51], a[196:199], v[172:175], v[48:51]
v_mul_f32 v247, v228, v247
v_mul_f32 v248, v228, v248
v_mfma_f32_16x16x32_bf16 v[52:55], a[200:203], v[168:171], v[52:55]
v_mul_f32 v249, v228, v249
v_cvt_pk_bf16_f32 v243, v244, v245
v_mfma_f32_16x16x32_bf16 v[52:55], a[204:207], v[172:175], v[52:55]
v_cvt_pk_bf16_f32 v244, v246, v247
v_cvt_pk_bf16_f32 v245, v248, v249
v_mfma_f32_16x16x32_bf16 v[56:59], a[208:211], v[168:171], v[56:59]
buffer_store_dwordx4 v[242:245], v1, s[52:55], s73 offen offset:3072 sc1
v_mfma_f32_16x16x32_bf16 v[56:59], a[212:215], v[172:175], v[56:59]
v_exp_f32 v121, v121
v_mfma_f32_16x16x32_bf16 v[60:63], a[216:219], v[168:171], v[60:63]
v_exp_f32 v122, v122
v_mfma_f32_16x16x32_bf16 v[60:63], a[220:223], v[172:175], v[60:63]
v_exp_f32 v123, v123
v_mfma_f32_16x16x32_bf16 v[64:67], a[224:227], v[168:171], v[64:67]
v_exp_f32 v124, v124
v_mfma_f32_16x16x32_bf16 v[64:67], a[228:231], v[172:175], v[64:67]
v_exp_f32 v125, v125
v_mfma_f32_16x16x32_bf16 v[68:71], a[232:235], v[168:171], v[68:71]
v_exp_f32 v126, v126
v_mfma_f32_16x16x32_bf16 v[68:71], a[236:239], v[172:175], v[68:71]
v_exp_f32 v127, v127
v_mfma_f32_16x16x32_bf16 v[72:75], a[240:243], v[168:171], v[72:75]
v_exp_f32 v96, v96
v_mfma_f32_16x16x32_bf16 v[72:75], a[244:247], v[172:175], v[72:75]
v_exp_f32 v97, v97
v_mfma_f32_16x16x32_bf16 v[76:79], a[248:251], v[168:171], v[76:79]
v_exp_f32 v98, v98
v_exp_f32 v99, v99
v_exp_f32 v100, v100
v_exp_f32 v101, v101
v_exp_f32 v102, v102
v_exp_f32 v103, v103
v_exp_f32 v104, v104
v_exp_f32 v105, v105
v_exp_f32 v106, v106
v_exp_f32 v107, v107
v_exp_f32 v108, v108
v_exp_f32 v109, v109
v_exp_f32 v110, v110
v_exp_f32 v111, v111
s_waitcnt vmcnt(23) lgkmcnt(0)
s_barrier
v_mfma_f32_16x16x32_bf16 v[76:79], a[252:255], v[172:175], v[76:79]
v_exp_f32 v128, v128
v_exp_f32 v129, v129
v_exp_f32 v130, v130
v_exp_f32 v131, v131
v_exp_f32 v132, v132
v_exp_f32 v133, v133
v_exp_f32 v134, v134
v_exp_f32 v135, v135
v_max_u32 v13, v13, v192
v_cvt_pk_bf16_f32 v152, v96, v97
v_cvt_pk_bf16_f32 v153, v98, v99
v_cvt_pk_bf16_f32 v154, v100, v101
v_cvt_pk_bf16_f32 v155, v102, v103
v_cvt_pk_bf16_f32 v156, v104, v105
v_cvt_pk_bf16_f32 v157, v106, v107
v_cvt_pk_bf16_f32 v158, v108, v109
v_cvt_pk_bf16_f32 v159, v110, v111
v_cvt_pk_bf16_f32 v160, v112, v113
v_cvt_pk_bf16_f32 v161, v114, v115
v_cvt_pk_bf16_f32 v162, v116, v117
v_cvt_pk_bf16_f32 v163, v118, v119
v_cvt_pk_bf16_f32 v164, v120, v121
v_cvt_pk_bf16_f32 v165, v122, v123
v_cvt_pk_bf16_f32 v166, v124, v125
v_cvt_pk_bf16_f32 v167, v126, v127
v_add_u32 v12, s69, v1
v_fma_f32 v112, v212, s72, v184
v_fma_f32 v113, v213, s72, v184
v_fma_f32 v114, v214, s72, v184
v_fma_f32 v115, v215, s72, v184
s_nop 1
v_mfma_f32_16x16x32_bf16 v[112:115], a[128:131], a[96:99], v[112:115]
s_mov_b32 m0, s68
ds_read_b128 a[192:195], v12 offset:16384
v_fma_f32 v116, v216, s72, v184
v_mfma_f32_16x16x32_bf16 v[112:115], a[132:135], a[100:103], v[112:115]
buffer_load_dwordx4 v1, s[56:59], s66 offen offset:0 sc1 lds
v_mfma_f32_16x16x32_bf16 v[112:115], a[136:139], a[104:107], v[112:115]
v_fma_f32 v117, v217, s72, v184
v_fma_f32 v118, v218, s72, v184
v_fma_f32 v119, v219, s72, v184
v_mfma_f32_16x16x32_bf16 v[112:115], a[140:143], a[108:111], v[112:115]
s_lshl_b32 s77, s60, 13
ds_read_b128 a[196:199], v12 offset:17408
ds_read_b128 a[200:203], v12 offset:18432
v_mfma_f32_16x16x32_bf16 v[116:119], a[144:147], a[96:99], v[116:119]
s_add_u32 s77, s77, 524288
v_fma_f32 v120, v220, s72, v184
v_fma_f32 v121, v221, s72, v184
v_mfma_f32_16x16x32_bf16 v[116:119], a[148:151], a[100:103], v[116:119]
s_sub_u32 s67, s66, 59392
v_fma_f32 v122, v222, s72, v184
v_fma_f32 v123, v223, s72, v184
v_mfma_f32_16x16x32_bf16 v[116:119], a[152:155], a[104:107], v[116:119]
s_add_u32 s69, s69, 32768
ds_read_b128 a[204:207], v12 offset:19456
ds_read_b128 a[208:211], v12 offset:20480
v_mfma_f32_16x16x32_bf16 v[116:119], a[156:159], a[108:111], v[116:119]
s_and_b32 s69, s69, 131071
v_fma_f32 v124, v224, s72, v184
v_fma_f32 v125, v225, s72, v184
v_mfma_f32_16x16x32_bf16 v[120:123], a[160:163], a[96:99], v[120:123]
v_fma_f32 v126, v226, s72, v184
v_fma_f32 v127, v227, s72, v184
v_mfma_f32_16x16x32_bf16 v[120:123], a[164:167], a[100:103], v[120:123]
ds_read_b128 a[212:215], v12 offset:21504
v_cvt_pk_bf16_f32 v168, v128, v129
v_mfma_f32_16x16x32_bf16 v[120:123], a[168:171], a[104:107], v[120:123]
buffer_load_dwordx4 v1, s[56:59], s66 offen offset:1024 sc1 lds
v_mfma_f32_16x16x32_bf16 v[120:123], a[172:175], a[108:111], v[120:123]
ds_read_b128 a[216:219], v12 offset:22528
v_cvt_pk_bf16_f32 v169, v130, v131
v_mfma_f32_16x16x32_bf16 v[124:127], a[176:179], a[96:99], v[124:127]
ds_read_b128 a[220:223], v12 offset:23552
v_cvt_pk_bf16_f32 v170, v132, v133
v_mfma_f32_16x16x32_bf16 v[124:127], a[180:183], a[100:103], v[124:127]
ds_read_b128 a[224:227], v12 offset:24576
v_cvt_pk_bf16_f32 v171, v134, v135
v_mfma_f32_16x16x32_bf16 v[124:127], a[184:187], a[104:107], v[124:127]
v_exp_f32 v136, v136
v_mfma_f32_16x16x32_bf16 v[124:127], a[188:191], a[108:111], v[124:127]
v_exp_f32 v137, v137
v_mfma_f32_16x16x32_bf16 v[128:131], a[128:131], a[112:115], v[188:191]
v_exp_f32 v138, v138
v_mfma_f32_16x16x32_bf16 v[128:131], a[132:135], a[116:119], v[128:131]
v_exp_f32 v139, v139
v_mfma_f32_16x16x32_bf16 v[128:131], a[136:139], a[120:123], v[128:131]
ds_read_b128 a[228:231], v12 offset:25600
v_cvt_pk_bf16_f32 v172, v136, v137
v_mfma_f32_16x16x32_bf16 v[128:131], a[140:143], a[124:127], v[128:131]
buffer_load_dwordx4 v1, s[56:59], s66 offen offset:2048 sc1 lds
v_mfma_f32_16x16x32_bf16 v[132:135], a[144:147], a[112:115], v[188:191]
ds_read_b128 a[232:235], v12 offset:26624
v_cvt_pk_bf16_f32 v173, v138, v139
v_mfma_f32_16x16x32_bf16 v[132:135], a[148:151], a[116:119], v[132:135]
v_exp_f32 v140, v140
v_mfma_f32_16x16x32_bf16 v[132:135], a[152:155], a[120:123], v[132:135]
v_exp_f32 v141, v141
v_mfma_f32_16x16x32_bf16 v[132:135], a[156:159], a[124:127], v[132:135]
v_exp_f32 v142, v142
v_mfma_f32_16x16x32_bf16 v[136:139], a[160:163], a[112:115], v[188:191]
v_exp_f32 v143, v143
v_mfma_f32_16x16x32_bf16 v[136:139], a[164:167], a[116:119], v[136:139]
ds_read_b128 a[236:239], v12 offset:27648
v_cvt_pk_bf16_f32 v174, v140, v141
v_mfma_f32_16x16x32_bf16 v[136:139], a[168:171], a[120:123], v[136:139]
ds_read_b128 a[240:243], v12 offset:28672
v_cvt_pk_bf16_f32 v175, v142, v143
v_mfma_f32_16x16x32_bf16 v[136:139], a[172:175], a[124:127], v[136:139]
v_and_b32 v8, 31, v2
v_lshlrev_b32 v8, 8, v8
v_mfma_f32_16x16x32_bf16 v[140:143], a[176:179], a[112:115], v[188:191]
buffer_load_dwordx4 v1, s[56:59], s66 offen offset:3072 sc1 lds
v_mfma_f32_16x16x32_bf16 v[140:143], a[180:183], a[116:119], v[140:143]
s_add_u32 m0, s68, 16384
ds_read_b128 a[244:247], v12 offset:29696
v_add_u32 v8, s77, v8
v_mfma_f32_16x16x32_bf16 v[140:143], a[184:187], a[120:123], v[140:143]
s_cmp_eq_u32 s62, 7
s_cselect_b64 vcc, -1, 0
v_cndmask_b32 v6, v6, v8, vcc
v_mfma_f32_16x16x32_bf16 v[140:143], a[188:191], a[124:127], v[140:143]
s_add_u32 s68, s68, 32768
ds_read_b128 a[248:251], v12 offset:30720
ds_read_b128 a[128:131], v12 offset:0
v_mfma_f32_16x16x32_bf16 v[196:199], v[208:211], v[152:155], v[196:199]
buffer_load_dwordx2 v[8:9], v6, s[52:55], 0 offen sc1
v_mfma_f32_16x16x32_bf16 v[196:199], v[208:211], v[156:159], v[196:199]
s_waitcnt lgkmcnt(3)
s_add_u32 s66, s66, 37888
ds_read_b128 a[252:255], v12 offset:31744
ds_read_b128 a[132:135], v12 offset:1024
v_mfma_f32_16x16x32_bf16 a[32:35], a[192:195], v[152:155], a[32:35]
s_and_b32 s68, s68, 131071
v_exp_f32 v112, v112
v_mfma_f32_16x16x32_bf16 a[32:35], a[196:199], v[156:159], a[32:35]
ds_read_b128 a[136:139], v12 offset:2048
ds_read_b128 a[140:143], v12 offset:3072
v_mfma_f32_16x16x32_bf16 a[36:39], a[200:203], v[152:155], a[36:39]
v_exp_f32 v113, v113
v_mfma_f32_16x16x32_bf16 a[36:39], a[204:207], v[156:159], a[36:39]
buffer_load_dwordx4 v1, s[56:59], s67 offen offset:0 sc1 lds
v_mfma_f32_16x16x32_bf16 a[40:43], a[208:211], v[152:155], a[40:43]
v_rcp_f32 v228, v196
v_mfma_f32_16x16x32_bf16 a[40:43], a[212:215], v[156:159], a[40:43]
ds_read_b128 a[144:147], v12 offset:4096
v_accvgpr_read_b32 v232, a32
v_mfma_f32_16x16x32_bf16 a[44:47], a[216:219], v[152:155], a[44:47]
v_mul_f32 v232, v228, v232
v_accvgpr_read_b32 v233, a33
v_mfma_f32_16x16x32_bf16 a[44:47], a[220:223], v[156:159], a[44:47]
v_mul_f32 v233, v228, v233
v_accvgpr_read_b32 v234, a34
v_mfma_f32_16x16x32_bf16 a[48:51], a[224:227], v[152:155], a[48:51]
v_mul_f32 v234, v228, v234
v_accvgpr_read_b32 v235, a35
v_mfma_f32_16x16x32_bf16 a[48:51], a[228:231], v[156:159], a[48:51]
v_mul_f32 v235, v228, v235
v_accvgpr_read_b32 v236, a36
v_mfma_f32_16x16x32_bf16 a[52:55], a[232:235], v[152:155], a[52:55]
v_accvgpr_read_b32 v237, a37
v_accvgpr_read_b32 v238, a38
v_mfma_f32_16x16x32_bf16 a[52:55], a[236:239], v[156:159], a[52:55]
s_waitcnt lgkmcnt(0)
v_accvgpr_read_b32 v239, a39
v_cvt_pk_bf16_f32 v232, v232, v233
v_mfma_f32_16x16x32_bf16 a[56:59], a[240:243], v[152:155], a[56:59]
buffer_load_dwordx4 v1, s[56:59], s67 offen offset:1024 sc1 lds
v_mfma_f32_16x16x32_bf16 a[56:59], a[244:247], v[156:159], a[56:59]
ds_read_b128 a[148:151], v12 offset:5120
v_mul_f32 v236, v228, v236
v_mfma_f32_16x16x32_bf16 a[60:63], a[248:251], v[152:155], a[60:63]
v_mul_f32 v237, v228, v237
v_mul_f32 v238, v228, v238
v_mfma_f32_16x16x32_bf16 a[60:63], a[252:255], v[156:159], a[60:63]
v_mul_f32 v239, v228, v239
v_cvt_pk_bf16_f32 v233, v234, v235
v_mfma_f32_16x16x32_bf16 v[200:203], v[208:211], v[160:163], v[200:203]
v_cvt_pk_bf16_f32 v234, v236, v237
v_cvt_pk_bf16_f32 v235, v238, v239
v_mfma_f32_16x16x32_bf16 v[200:203], v[208:211], v[164:167], v[200:203]
buffer_store_dwordx4 v[232:235], v1, s[52:55], s74 offen offset:0 sc1
v_mfma_f32_16x16x32_bf16 v[16:19], a[192:195], v[160:163], v[16:19]
ds_read_b128 a[152:155], v12 offset:6144
ds_read_b128 a[156:159], v12 offset:7168
v_mfma_f32_16x16x32_bf16 v[16:19], a[196:199], v[164:167], v[16:19]
ds_read_b128 a[160:163], v12 offset:8192
v_accvgpr_read_b32 v242, a40
v_mfma_f32_16x16x32_bf16 v[20:23], a[200:203], v[160:163], v[20:23]
v_mul_f32 v242, v228, v242
v_accvgpr_read_b32 v243, a41
v_mfma_f32_16x16x32_bf16 v[20:23], a[204:207], v[164:167], v[20:23]
v_mul_f32 v243, v228, v243
v_accvgpr_read_b32 v244, a42
v_mfma_f32_16x16x32_bf16 v[24:27], a[208:211], v[160:163], v[24:27]
v_mul_f32 v244, v228, v244
v_accvgpr_read_b32 v245, a43
)ASM"
R"ASM(
v_mfma_f32_16x16x32_bf16 v[24:27], a[212:215], v[164:167], v[24:27]
v_mul_f32 v245, v228, v245
v_accvgpr_read_b32 v246, a44
v_mfma_f32_16x16x32_bf16 v[28:31], a[216:219], v[160:163], v[28:31]
buffer_load_dwordx4 v1, s[56:59], s67 offen offset:2048 sc1 lds
v_mfma_f32_16x16x32_bf16 v[28:31], a[220:223], v[164:167], v[28:31]
v_accvgpr_read_b32 v247, a45
v_accvgpr_read_b32 v248, a46
v_mfma_f32_16x16x32_bf16 v[32:35], a[224:227], v[160:163], v[32:35]
v_accvgpr_read_b32 v249, a47
v_cvt_pk_bf16_f32 v242, v242, v243
v_mfma_f32_16x16x32_bf16 v[32:35], a[228:231], v[164:167], v[32:35]
ds_read_b128 a[164:167], v12 offset:9216
v_mul_f32 v246, v228, v246
v_mfma_f32_16x16x32_bf16 v[36:39], a[232:235], v[160:163], v[36:39]
v_mul_f32 v247, v228, v247
v_mul_f32 v248, v228, v248
v_mfma_f32_16x16x32_bf16 v[36:39], a[236:239], v[164:167], v[36:39]
v_mul_f32 v249, v228, v249
v_cvt_pk_bf16_f32 v243, v244, v245
v_mfma_f32_16x16x32_bf16 v[40:43], a[240:243], v[160:163], v[40:43]
buffer_load_dwordx4 v1, s[56:59], s67 offen offset:3072 sc1 lds
v_mfma_f32_16x16x32_bf16 v[40:43], a[244:247], v[164:167], v[40:43]
v_cvt_pk_bf16_f32 v244, v246, v247
v_cvt_pk_bf16_f32 v245, v248, v249
v_mfma_f32_16x16x32_bf16 v[44:47], a[248:251], v[160:163], v[44:47]
ds_read_b128 a[168:171], v12 offset:10240
ds_read_b128 a[172:175], v12 offset:11264
v_mfma_f32_16x16x32_bf16 v[44:47], a[252:255], v[164:167], v[44:47]
v_accvgpr_read_b32 v232, a48
v_accvgpr_read_b32 v233, a49
v_mfma_f32_16x16x32_bf16 v[204:207], v[208:211], v[168:171], v[204:207]
buffer_store_dwordx4 v[242:245], v1, s[52:55], s74 offen offset:1024 sc1
v_mfma_f32_16x16x32_bf16 v[204:207], v[208:211], v[172:175], v[204:207]
ds_read_b128 a[176:179], v12 offset:12288
v_mul_f32 v232, v228, v232
v_mfma_f32_16x16x32_bf16 v[48:51], a[192:195], v[168:171], v[48:51]
v_mul_f32 v233, v228, v233
v_accvgpr_read_b32 v234, a50
v_mfma_f32_16x16x32_bf16 v[48:51], a[196:199], v[172:175], v[48:51]
v_mul_f32 v234, v228, v234
v_accvgpr_read_b32 v235, a51
v_mfma_f32_16x16x32_bf16 v[52:55], a[200:203], v[168:171], v[52:55]
v_mul_f32 v235, v228, v235
v_accvgpr_read_b32 v236, a52
v_mfma_f32_16x16x32_bf16 v[52:55], a[204:207], v[172:175], v[52:55]
v_accvgpr_read_b32 v237, a53
v_accvgpr_read_b32 v238, a54
v_mfma_f32_16x16x32_bf16 v[56:59], a[208:211], v[168:171], v[56:59]
v_accvgpr_read_b32 v239, a55
v_cvt_pk_bf16_f32 v232, v232, v233
v_mfma_f32_16x16x32_bf16 v[56:59], a[212:215], v[172:175], v[56:59]
v_mul_f32 v236, v228, v236
v_mul_f32 v237, v228, v237
v_mfma_f32_16x16x32_bf16 v[60:63], a[216:219], v[168:171], v[60:63]
v_mul_f32 v238, v228, v238
v_mul_f32 v239, v228, v239
v_mfma_f32_16x16x32_bf16 v[60:63], a[220:223], v[172:175], v[60:63]
v_cvt_pk_bf16_f32 v233, v234, v235
v_cvt_pk_bf16_f32 v234, v236, v237
v_mfma_f32_16x16x32_bf16 v[64:67], a[224:227], v[168:171], v[64:67]
ds_read_b128 a[180:183], v12 offset:13312
ds_read_b128 a[184:187], v12 offset:14336
ds_read_b128 a[188:191], v12 offset:15360
v_mfma_f32_16x16x32_bf16 v[64:67], a[228:231], v[172:175], v[64:67]
v_cvt_pk_bf16_f32 v235, v238, v239
v_accvgpr_read_b32 v242, a56
v_mfma_f32_16x16x32_bf16 v[68:71], a[232:235], v[168:171], v[68:71]
buffer_store_dwordx4 v[232:235], v1, s[52:55], s74 offen offset:2048 sc1
v_mfma_f32_16x16x32_bf16 v[68:71], a[236:239], v[172:175], v[68:71]
v_mul_f32 v242, v228, v242
v_accvgpr_read_b32 v243, a57
v_mfma_f32_16x16x32_bf16 v[72:75], a[240:243], v[168:171], v[72:75]
v_mul_f32 v243, v228, v243
v_accvgpr_read_b32 v244, a58
v_mfma_f32_16x16x32_bf16 v[72:75], a[244:247], v[172:175], v[72:75]
s_waitcnt vmcnt(7)
v_cmp_ne_u64 vcc, s[64:65], v[8:9]
s_nop 0
s_cbranch_vccnz L_stub_kvT
L_ret_kvT:
v_mfma_f32_16x16x32_bf16 v[76:79], a[248:251], v[168:171], v[76:79]
v_exp_f32 v114, v114
v_exp_f32 v115, v115
v_exp_f32 v116, v116
v_exp_f32 v117, v117
v_exp_f32 v118, v118
v_exp_f32 v119, v119
v_exp_f32 v120, v120
v_exp_f32 v121, v121
v_exp_f32 v122, v122
v_exp_f32 v123, v123
v_exp_f32 v124, v124
v_exp_f32 v125, v125
v_exp_f32 v126, v126
v_exp_f32 v127, v127
v_mul_f32 v244, v228, v244
v_accvgpr_read_b32 v245, a59
v_mul_f32 v245, v228, v245
v_accvgpr_read_b32 v246, a60
v_mul_f32 v246, v228, v246
v_accvgpr_read_b32 v247, a61
v_mul_f32 v247, v228, v247
v_accvgpr_read_b32 v248, a62
v_mul_f32 v248, v228, v248
v_accvgpr_read_b32 v249, a63
v_mul_f32 v249, v228, v249
v_cvt_pk_bf16_f32 v242, v242, v243
v_cvt_pk_bf16_f32 v243, v244, v245
v_cvt_pk_bf16_f32 v244, v246, v247
v_cvt_pk_bf16_f32 v245, v248, v249
s_waitcnt lgkmcnt(0)
s_barrier
v_mfma_f32_16x16x32_bf16 v[76:79], a[252:255], v[172:175], v[76:79]
v_exp_f32 v128, v128
v_exp_f32 v129, v129
v_exp_f32 v130, v130
v_exp_f32 v131, v131
v_exp_f32 v132, v132
v_exp_f32 v133, v133
v_exp_f32 v134, v134
v_exp_f32 v135, v135
v_max_u32 v13, v13, v196
buffer_store_dwordx4 v[242:245], v1, s[52:55], s74 offen offset:3072 sc1
v_cvt_pk_bf16_f32 v160, v112, v113
v_cvt_pk_bf16_f32 v161, v114, v115
v_cvt_pk_bf16_f32 v162, v116, v117
v_cvt_pk_bf16_f32 v163, v118, v119
v_cvt_pk_bf16_f32 v164, v120, v121
v_cvt_pk_bf16_f32 v165, v122, v123
v_cvt_pk_bf16_f32 v166, v124, v125
v_cvt_pk_bf16_f32 v167, v126, v127
v_add_u32 v12, s69, v1
s_lshl_b32 s70, s62, 2
s_sub_u32 s70, 27, s70
s_cbranch_scc1 L_attn_U1
s_nop 0
s_nop 0
s_nop 0
s_nop 0
s_nop 0
s_nop 0
s_nop 0
s_nop 0
s_nop 0
s_nop 0
s_nop 0
s_nop 0
s_nop 0
s_nop 0
s_nop 0
s_nop 0
L_attn_S:
v_mfma_f32_16x16x32_bf16 v[112:115], a[128:131], a[96:99], v[184:187]
s_mov_b32 m0, s68
ds_read_b128 a[192:195], v12 offset:16384
v_cvt_pk_bf16_f32 v168, v128, v129
v_mfma_f32_16x16x32_bf16 v[112:115], a[132:135], a[100:103], v[112:115]
s_sub_u32 s70, s70, 1
v_exp_f32 v136, v136
v_mfma_f32_16x16x32_bf16 v[112:115], a[136:139], a[104:107], v[112:115]
buffer_load_dwordx4 v1, s[56:59], s66 offen offset:0 sc1 lds
v_mfma_f32_16x16x32_bf16 v[112:115], a[140:143], a[108:111], v[112:115]
s_sub_u32 s67, s66, 59392
v_exp_f32 v137, v137
v_mfma_f32_16x16x32_bf16 v[116:119], a[144:147], a[96:99], v[184:187]
s_add_u32 s69, s69, 32768
ds_read_b128 a[196:199], v12 offset:17408
v_cvt_pk_bf16_f32 v169, v130, v131
v_mfma_f32_16x16x32_bf16 v[116:119], a[148:151], a[100:103], v[116:119]
s_and_b32 s69, s69, 131071
v_exp_f32 v138, v138
v_mfma_f32_16x16x32_bf16 v[116:119], a[152:155], a[104:107], v[116:119]
ds_read_b128 a[200:203], v12 offset:18432
v_cvt_pk_bf16_f32 v170, v132, v133
v_mfma_f32_16x16x32_bf16 v[116:119], a[156:159], a[108:111], v[116:119]
v_exp_f32 v139, v139
v_mfma_f32_16x16x32_bf16 v[120:123], a[160:163], a[96:99], v[184:187]
ds_read_b128 a[204:207], v12 offset:19456
v_cvt_pk_bf16_f32 v171, v134, v135
v_mfma_f32_16x16x32_bf16 v[120:123], a[164:167], a[100:103], v[120:123]
v_exp_f32 v140, v140
v_mfma_f32_16x16x32_bf16 v[120:123], a[168:171], a[104:107], v[120:123]
buffer_load_dwordx4 v1, s[56:59], s66 offen offset:1024 sc1 lds
v_mfma_f32_16x16x32_bf16 v[120:123], a[172:175], a[108:111], v[120:123]
v_exp_f32 v141, v141
v_mfma_f32_16x16x32_bf16 v[124:127], a[176:179], a[96:99], v[184:187]
ds_read_b128 a[208:211], v12 offset:20480
ds_read_b128 a[212:215], v12 offset:21504
v_mfma_f32_16x16x32_bf16 v[124:127], a[180:183], a[100:103], v[124:127]
v_exp_f32 v142, v142
v_mfma_f32_16x16x32_bf16 v[124:127], a[184:187], a[104:107], v[124:127]
ds_read_b128 a[216:219], v12 offset:22528
ds_read_b128 a[220:223], v12 offset:23552
v_mfma_f32_16x16x32_bf16 v[124:127], a[188:191], a[108:111], v[124:127]
v_exp_f32 v143, v143
v_mfma_f32_16x16x32_bf16 v[128:131], a[128:131], a[112:115], v[188:191]
ds_read_b128 a[224:227], v12 offset:24576
v_cvt_pk_bf16_f32 v172, v136, v137
v_mfma_f32_16x16x32_bf16 v[128:131], a[132:135], a[116:119], v[128:131]
v_exp_f32 v112, v112
v_mfma_f32_16x16x32_bf16 v[128:131], a[136:139], a[120:123], v[128:131]
buffer_load_dwordx4 v1, s[56:59], s66 offen offset:2048 sc1 lds
v_mfma_f32_16x16x32_bf16 v[128:131], a[140:143], a[124:127], v[128:131]
v_exp_f32 v113, v113
v_mfma_f32_16x16x32_bf16 v[132:135], a[144:147], a[112:115], v[188:191]
ds_read_b128 a[228:231], v12 offset:25600
v_cvt_pk_bf16_f32 v173, v138, v139
v_mfma_f32_16x16x32_bf16 v[132:135], a[148:151], a[116:119], v[132:135]
v_exp_f32 v114, v114
v_mfma_f32_16x16x32_bf16 v[132:135], a[152:155], a[120:123], v[132:135]
ds_read_b128 a[232:235], v12 offset:26624
v_cvt_pk_bf16_f32 v174, v140, v141
v_mfma_f32_16x16x32_bf16 v[132:135], a[156:159], a[124:127], v[132:135]
v_exp_f32 v115, v115
v_mfma_f32_16x16x32_bf16 v[136:139], a[160:163], a[112:115], v[188:191]
ds_read_b128 a[236:239], v12 offset:27648
v_cvt_pk_bf16_f32 v175, v142, v143
v_mfma_f32_16x16x32_bf16 v[136:139], a[164:167], a[116:119], v[136:139]
v_exp_f32 v116, v116
v_mfma_f32_16x16x32_bf16 v[136:139], a[168:171], a[120:123], v[136:139]
buffer_load_dwordx4 v1, s[56:59], s66 offen offset:3072 sc1 lds
v_mfma_f32_16x16x32_bf16 v[136:139], a[172:175], a[124:127], v[136:139]
s_add_u32 m0, s68, 16384
v_exp_f32 v117, v117
v_mfma_f32_16x16x32_bf16 v[140:143], a[176:179], a[112:115], v[188:191]
s_add_u32 s66, s66, 37888
ds_read_b128 a[240:243], v12 offset:28672
ds_read_b128 a[244:247], v12 offset:29696
v_mfma_f32_16x16x32_bf16 v[140:143], a[180:183], a[116:119], v[140:143]
s_add_u32 s68, s68, 32768
v_exp_f32 v118, v118
v_mfma_f32_16x16x32_bf16 v[140:143], a[184:187], a[120:123], v[140:143]
s_and_b32 s68, s68, 131071
ds_read_b128 a[248:251], v12 offset:30720
ds_read_b128 a[252:255], v12 offset:31744
v_mfma_f32_16x16x32_bf16 v[140:143], a[188:191], a[124:127], v[140:143]
v_exp_f32 v119, v119
v_mfma_f32_16x16x32_bf16 v[200:203], v[208:211], v[160:163], v[200:203]
ds_read_b128 a[128:131], v12 offset:0
ds_read_b128 a[132:135], v12 offset:1024
v_mfma_f32_16x16x32_bf16 v[200:203], v[208:211], v[164:167], v[200:203]
s_waitcnt lgkmcnt(4)
v_exp_f32 v120, v120
v_mfma_f32_16x16x32_bf16 v[16:19], a[192:195], v[160:163], v[16:19]
buffer_load_dwordx4 v1, s[56:59], s67 offen offset:0 sc1 lds
v_mfma_f32_16x16x32_bf16 v[16:19], a[196:199], v[164:167], v[16:19]
v_exp_f32 v121, v121
v_mfma_f32_16x16x32_bf16 v[20:23], a[200:203], v[160:163], v[20:23]
ds_read_b128 a[136:139], v12 offset:2048
v_mfma_f32_16x16x32_bf16 v[20:23], a[204:207], v[164:167], v[20:23]
v_exp_f32 v122, v122
v_mfma_f32_16x16x32_bf16 v[24:27], a[208:211], v[160:163], v[24:27]
ds_read_b128 a[140:143], v12 offset:3072
ds_read_b128 a[144:147], v12 offset:4096
v_mfma_f32_16x16x32_bf16 v[24:27], a[212:215], v[164:167], v[24:27]
v_exp_f32 v123, v123
v_mfma_f32_16x16x32_bf16 v[28:31], a[216:219], v[160:163], v[28:31]
ds_read_b128 a[148:151], v12 offset:5120
ds_read_b128 a[152:155], v12 offset:6144
v_mfma_f32_16x16x32_bf16 v[28:31], a[220:223], v[164:167], v[28:31]
v_exp_f32 v124, v124
v_mfma_f32_16x16x32_bf16 v[32:35], a[224:227], v[160:163], v[32:35]
buffer_load_dwordx4 v1, s[56:59], s67 offen offset:1024 sc1 lds
v_mfma_f32_16x16x32_bf16 v[32:35], a[228:231], v[164:167], v[32:35]
v_exp_f32 v125, v125
v_mfma_f32_16x16x32_bf16 v[36:39], a[232:235], v[160:163], v[36:39]
ds_read_b128 a[156:159], v12 offset:7168
v_mfma_f32_16x16x32_bf16 v[36:39], a[236:239], v[164:167], v[36:39]
v_exp_f32 v126, v126
v_mfma_f32_16x16x32_bf16 v[40:43], a[240:243], v[160:163], v[40:43]
ds_read_b128 a[160:163], v12 offset:8192
ds_read_b128 a[164:167], v12 offset:9216
v_mfma_f32_16x16x32_bf16 v[40:43], a[244:247], v[164:167], v[40:43]
s_waitcnt lgkmcnt(3)
v_exp_f32 v127, v127
v_mfma_f32_16x16x32_bf16 v[44:47], a[248:251], v[160:163], v[44:47]
ds_read_b128 a[168:171], v12 offset:10240
ds_read_b128 a[172:175], v12 offset:11264
v_mfma_f32_16x16x32_bf16 v[44:47], a[252:255], v[164:167], v[44:47]
v_exp_f32 v128, v128
v_mfma_f32_16x16x32_bf16 v[204:207], v[208:211], v[168:171], v[204:207]
buffer_load_dwordx4 v1, s[56:59], s67 offen offset:2048 sc1 lds
v_mfma_f32_16x16x32_bf16 v[204:207], v[208:211], v[172:175], v[204:207]
v_exp_f32 v129, v129
v_mfma_f32_16x16x32_bf16 v[48:51], a[192:195], v[168:171], v[48:51]
ds_read_b128 a[176:179], v12 offset:12288
v_cvt_pk_bf16_f32 v160, v112, v113
v_mfma_f32_16x16x32_bf16 v[48:51], a[196:199], v[172:175], v[48:51]
v_exp_f32 v130, v130
v_mfma_f32_16x16x32_bf16 v[52:55], a[200:203], v[168:171], v[52:55]
ds_read_b128 a[180:183], v12 offset:13312
v_cvt_pk_bf16_f32 v161, v114, v115
v_mfma_f32_16x16x32_bf16 v[52:55], a[204:207], v[172:175], v[52:55]
v_exp_f32 v131, v131
v_mfma_f32_16x16x32_bf16 v[56:59], a[208:211], v[168:171], v[56:59]
ds_read_b128 a[184:187], v12 offset:14336
v_cvt_pk_bf16_f32 v162, v116, v117
v_mfma_f32_16x16x32_bf16 v[56:59], a[212:215], v[172:175], v[56:59]
v_exp_f32 v132, v132
v_mfma_f32_16x16x32_bf16 v[60:63], a[216:219], v[168:171], v[60:63]
buffer_load_dwordx4 v1, s[56:59], s67 offen offset:3072 sc1 lds
v_mfma_f32_16x16x32_bf16 v[60:63], a[220:223], v[172:175], v[60:63]
v_exp_f32 v133, v133
v_mfma_f32_16x16x32_bf16 v[64:67], a[224:227], v[168:171], v[64:67]
ds_read_b128 a[188:191], v12 offset:15360
v_cvt_pk_bf16_f32 v163, v118, v119
v_mfma_f32_16x16x32_bf16 v[64:67], a[228:231], v[172:175], v[64:67]
v_exp_f32 v134, v134
v_mfma_f32_16x16x32_bf16 v[68:71], a[232:235], v[168:171], v[68:71]
v_cvt_pk_bf16_f32 v164, v120, v121
v_cvt_pk_bf16_f32 v165, v122, v123
v_mfma_f32_16x16x32_bf16 v[68:71], a[236:239], v[172:175], v[68:71]
s_cmp_lg_u32 s70, 0
v_exp_f32 v135, v135
v_mfma_f32_16x16x32_bf16 v[72:75], a[240:243], v[168:171], v[72:75]
v_cvt_pk_bf16_f32 v166, v124, v125
v_cvt_pk_bf16_f32 v167, v126, v127
v_mfma_f32_16x16x32_bf16 v[72:75], a[244:247], v[172:175], v[72:75]
s_waitcnt vmcnt(16) lgkmcnt(0)
v_add_u32 v12, s69, v1
v_mfma_f32_16x16x32_bf16 v[76:79], a[248:251], v[168:171], v[76:79]
s_barrier
v_mfma_f32_16x16x32_bf16 v[76:79], a[252:255], v[172:175], v[76:79]
s_cbranch_scc1 L_attn_S
v_add_f32 v112, v212, v184
v_add_f32 v113, v213, v184
v_add_f32 v114, v214, v184
v_add_f32 v115, v215, v184
s_nop 1
v_mfma_f32_16x16x32_bf16 v[112:115], a[128:131], a[96:99], v[112:115]
s_lshl_b32 s77, s60, 13
ds_read_b128 a[192:195], v12 offset:16384
v_add_f32 v116, v216, v184
L_pad_a_2_3:
s_cbranch_execz L_padret_a
v_mfma_f32_16x16x32_bf16 v[112:115], a[132:135], a[100:103], v[112:115]
s_add_u32 s77, s77, 524288
v_add_f32 v117, v217, v184
v_add_f32 v118, v218, v184
v_mfma_f32_16x16x32_bf16 v[112:115], a[136:139], a[104:107], v[112:115]
s_add_u32 s69, s69, 32768
ds_read_b128 a[196:199], v12 offset:17408
v_add_f32 v119, v219, v184
v_mfma_f32_16x16x32_bf16 v[112:115], a[140:143], a[108:111], v[112:115]
s_and_b32 s69, s69, 131071
ds_read_b128 a[200:203], v12 offset:18432
v_add_f32 v120, v220, v184
v_mfma_f32_16x16x32_bf16 v[116:119], a[144:147], a[96:99], v[116:119]
v_add_f32 v121, v221, v184
v_add_f32 v122, v222, v184
v_mfma_f32_16x16x32_bf16 v[116:119], a[148:151], a[100:103], v[116:119]
ds_read_b128 a[204:207], v12 offset:19456
v_add_f32 v123, v223, v184
v_mfma_f32_16x16x32_bf16 v[116:119], a[152:155], a[104:107], v[116:119]
ds_read_b128 a[208:211], v12 offset:20480
v_add_f32 v124, v224, v184
v_mfma_f32_16x16x32_bf16 v[116:119], a[156:159], a[108:111], v[116:119]
v_add_f32 v125, v225, v184
v_add_f32 v126, v226, v184
v_mfma_f32_16x16x32_bf16 v[120:123], a[160:163], a[96:99], v[120:123]
ds_read_b128 a[212:215], v12 offset:21504
v_add_f32 v127, v227, v184
v_mfma_f32_16x16x32_bf16 v[120:123], a[164:167], a[100:103], v[120:123]
v_cvt_pk_bf16_f32 v168, v128, v129
v_cvt_pk_bf16_f32 v169, v130, v131
v_mfma_f32_16x16x32_bf16 v[120:123], a[168:171], a[104:107], v[120:123]
ds_read_b128 a[216:219], v12 offset:22528
v_cvt_pk_bf16_f32 v170, v132, v133
v_mfma_f32_16x16x32_bf16 v[120:123], a[172:175], a[108:111], v[120:123]
ds_read_b128 a[220:223], v12 offset:23552
v_cvt_pk_bf16_f32 v171, v134, v135
v_mfma_f32_16x16x32_bf16 v[124:127], a[176:179], a[96:99], v[124:127]
v_exp_f32 v136, v136
v_mfma_f32_16x16x32_bf16 v[124:127], a[180:183], a[100:103], v[124:127]
v_exp_f32 v137, v137
v_mfma_f32_16x16x32_bf16 v[124:127], a[184:187], a[104:107], v[124:127]
v_exp_f32 v138, v138
v_mfma_f32_16x16x32_bf16 v[124:127], a[188:191], a[108:111], v[124:127]
v_exp_f32 v139, v139
v_mfma_f32_16x16x32_bf16 v[128:131], a[128:131], a[112:115], v[188:191]
ds_read_b128 a[224:227], v12 offset:24576
v_and_b32 v6, 31, v2
v_mfma_f32_16x16x32_bf16 v[128:131], a[132:135], a[116:119], v[128:131]
ds_read_b128 a[228:231], v12 offset:25600
v_cvt_pk_bf16_f32 v172, v136, v137
v_mfma_f32_16x16x32_bf16 v[128:131], a[136:139], a[120:123], v[128:131]
v_cvt_pk_bf16_f32 v173, v138, v139
v_lshlrev_b32 v6, 8, v6
v_mfma_f32_16x16x32_bf16 v[128:131], a[140:143], a[124:127], v[128:131]
ds_read_b128 a[232:235], v12 offset:26624
v_add_u32 v6, s77, v6
v_mfma_f32_16x16x32_bf16 v[132:135], a[144:147], a[112:115], v[188:191]
buffer_load_dwordx2 v[8:9], v6, s[52:55], 0 offen sc1
v_mfma_f32_16x16x32_bf16 v[132:135], a[148:151], a[116:119], v[132:135]
v_exp_f32 v140, v140
v_mfma_f32_16x16x32_bf16 v[132:135], a[152:155], a[120:123], v[132:135]
v_exp_f32 v141, v141
v_mfma_f32_16x16x32_bf16 v[132:135], a[156:159], a[124:127], v[132:135]
v_exp_f32 v142, v142
)ASM"
R"ASM(
v_mfma_f32_16x16x32_bf16 v[136:139], a[160:163], a[112:115], v[188:191]
v_exp_f32 v143, v143
v_mfma_f32_16x16x32_bf16 v[136:139], a[164:167], a[116:119], v[136:139]
ds_read_b128 a[236:239], v12 offset:27648
v_cvt_pk_bf16_f32 v174, v140, v141
v_mfma_f32_16x16x32_bf16 v[136:139], a[168:171], a[120:123], v[136:139]
ds_read_b128 a[240:243], v12 offset:28672
v_cvt_pk_bf16_f32 v175, v142, v143
v_mfma_f32_16x16x32_bf16 v[136:139], a[172:175], a[124:127], v[136:139]
v_exp_f32 v112, v112
v_mfma_f32_16x16x32_bf16 v[140:143], a[176:179], a[112:115], v[188:191]
v_exp_f32 v113, v113
v_mfma_f32_16x16x32_bf16 v[140:143], a[180:183], a[116:119], v[140:143]
ds_read_b128 a[244:247], v12 offset:29696
ds_read_b128 a[128:131], v12 offset:0
v_mfma_f32_16x16x32_bf16 v[140:143], a[184:187], a[120:123], v[140:143]
v_exp_f32 v114, v114
v_mfma_f32_16x16x32_bf16 v[140:143], a[188:191], a[124:127], v[140:143]
ds_read_b128 a[248:251], v12 offset:30720
ds_read_b128 a[132:135], v12 offset:1024
v_mfma_f32_16x16x32_bf16 v[200:203], v[208:211], v[160:163], v[200:203]
v_exp_f32 v115, v115
v_mfma_f32_16x16x32_bf16 v[200:203], v[208:211], v[164:167], v[200:203]
s_waitcnt lgkmcnt(4)
ds_read_b128 a[252:255], v12 offset:31744
ds_read_b128 a[136:139], v12 offset:2048
v_mfma_f32_16x16x32_bf16 v[16:19], a[192:195], v[160:163], v[16:19]
v_exp_f32 v116, v116
v_mfma_f32_16x16x32_bf16 v[16:19], a[196:199], v[164:167], v[16:19]
ds_read_b128 a[140:143], v12 offset:3072
ds_read_b128 a[144:147], v12 offset:4096
v_mfma_f32_16x16x32_bf16 v[20:23], a[200:203], v[160:163], v[20:23]
v_exp_f32 v117, v117
v_mfma_f32_16x16x32_bf16 v[20:23], a[204:207], v[164:167], v[20:23]
v_exp_f32 v118, v118
v_mfma_f32_16x16x32_bf16 v[24:27], a[208:211], v[160:163], v[24:27]
ds_read_b128 a[148:151], v12 offset:5120
ds_read_b128 a[152:155], v12 offset:6144
v_mfma_f32_16x16x32_bf16 v[24:27], a[212:215], v[164:167], v[24:27]
v_exp_f32 v119, v119
v_mfma_f32_16x16x32_bf16 v[28:31], a[216:219], v[160:163], v[28:31]
v_exp_f32 v120, v120
v_mfma_f32_16x16x32_bf16 v[28:31], a[220:223], v[164:167], v[28:31]
v_exp_f32 v121, v121
v_mfma_f32_16x16x32_bf16 v[32:35], a[224:227], v[160:163], v[32:35]
ds_read_b128 a[156:159], v12 offset:7168
ds_read_b128 a[160:163], v12 offset:8192
v_mfma_f32_16x16x32_bf16 v[32:35], a[228:231], v[164:167], v[32:35]
v_exp_f32 v122, v122
v_mfma_f32_16x16x32_bf16 v[36:39], a[232:235], v[160:163], v[36:39]
v_exp_f32 v123, v123
v_mfma_f32_16x16x32_bf16 v[36:39], a[236:239], v[164:167], v[36:39]
ds_read_b128 a[164:167], v12 offset:9216
ds_read_b128 a[168:171], v12 offset:10240
v_mfma_f32_16x16x32_bf16 v[40:43], a[240:243], v[160:163], v[40:43]
s_waitcnt lgkmcnt(4)
v_exp_f32 v124, v124
v_mfma_f32_16x16x32_bf16 v[40:43], a[244:247], v[164:167], v[40:43]
v_exp_f32 v125, v125
v_mfma_f32_16x16x32_bf16 v[44:47], a[248:251], v[160:163], v[44:47]
v_exp_f32 v126, v126
v_mfma_f32_16x16x32_bf16 v[44:47], a[252:255], v[164:167], v[44:47]
ds_read_b128 a[172:175], v12 offset:11264
ds_read_b128 a[176:179], v12 offset:12288
v_mfma_f32_16x16x32_bf16 v[204:207], v[208:211], v[168:171], v[204:207]
v_exp_f32 v127, v127
v_mfma_f32_16x16x32_bf16 v[204:207], v[208:211], v[172:175], v[204:207]
v_exp_f32 v128, v128
v_mfma_f32_16x16x32_bf16 v[48:51], a[192:195], v[168:171], v[48:51]
ds_read_b128 a[180:183], v12 offset:13312
ds_read_b128 a[184:187], v12 offset:14336
v_mfma_f32_16x16x32_bf16 v[48:51], a[196:199], v[172:175], v[48:51]
v_exp_f32 v129, v129
v_mfma_f32_16x16x32_bf16 v[52:55], a[200:203], v[168:171], v[52:55]
v_exp_f32 v130, v130
v_mfma_f32_16x16x32_bf16 v[52:55], a[204:207], v[172:175], v[52:55]
v_exp_f32 v131, v131
v_mfma_f32_16x16x32_bf16 v[56:59], a[208:211], v[168:171], v[56:59]
ds_read_b128 a[188:191], v12 offset:15360
v_cvt_pk_bf16_f32 v160, v112, v113
v_mfma_f32_16x16x32_bf16 v[56:59], a[212:215], v[172:175], v[56:59]
v_exp_f32 v132, v132
v_mfma_f32_16x16x32_bf16 v[60:63], a[216:219], v[168:171], v[60:63]
v_exp_f32 v133, v133
v_mfma_f32_16x16x32_bf16 v[60:63], a[220:223], v[172:175], v[60:63]
v_exp_f32 v134, v134
v_mfma_f32_16x16x32_bf16 v[64:67], a[224:227], v[168:171], v[64:67]
v_exp_f32 v135, v135
v_mfma_f32_16x16x32_bf16 v[64:67], a[228:231], v[172:175], v[64:67]
v_cvt_pk_bf16_f32 v161, v114, v115
v_cvt_pk_bf16_f32 v162, v116, v117
v_mfma_f32_16x16x32_bf16 v[68:71], a[232:235], v[168:171], v[68:71]
v_cvt_pk_bf16_f32 v163, v118, v119
v_cvt_pk_bf16_f32 v164, v120, v121
v_mfma_f32_16x16x32_bf16 v[68:71], a[236:239], v[172:175], v[68:71]
v_cvt_pk_bf16_f32 v165, v122, v123
v_cvt_pk_bf16_f32 v166, v124, v125
v_mfma_f32_16x16x32_bf16 v[72:75], a[240:243], v[168:171], v[72:75]
v_cvt_pk_bf16_f32 v167, v126, v127
v_add_u32 v12, s69, v1
v_mfma_f32_16x16x32_bf16 v[72:75], a[244:247], v[172:175], v[72:75]
s_waitcnt vmcnt(0)
v_cmp_ne_u64 vcc, s[64:65], v[8:9]
s_nop 0
s_cbranch_vccnz L_stub_sj
L_ret_sj:
v_mfma_f32_16x16x32_bf16 v[76:79], a[248:251], v[168:171], v[76:79]
s_waitcnt lgkmcnt(0)
s_barrier
v_mfma_f32_16x16x32_bf16 v[76:79], a[252:255], v[172:175], v[76:79]
L_attn_U1:
L_pad_a_3_3:
s_cbranch_execz L_padret_a
v_add_f32 v80, v212, v188
v_add_f32 v81, v213, v188
v_add_f32 v82, v214, v188
v_add_f32 v83, v215, v188
s_nop 1
v_mfma_f32_16x16x32_bf16 v[80:83], a[128:131], a[112:115], v[80:83]
s_lshl_b32 s83, s60, 10
ds_read_b128 a[192:195], v12 offset:16384
v_add_f32 v84, v216, v188
v_mfma_f32_16x16x32_bf16 v[80:83], a[132:135], a[116:119], v[80:83]
s_add_u32 m0, s83, 147456
v_add_f32 v85, v217, v188
v_add_f32 v86, v218, v188
v_mfma_f32_16x16x32_bf16 v[80:83], a[136:139], a[120:123], v[80:83]
s_add_u32 s83, s83, 1051136
ds_read_b128 a[196:199], v12 offset:17408
v_add_f32 v87, v219, v188
v_mfma_f32_16x16x32_bf16 v[80:83], a[140:143], a[124:127], v[80:83]
buffer_load_dwordx4 v1, s[52:55], s83 offen sc1 lds
v_mfma_f32_16x16x32_bf16 v[84:87], a[144:147], a[112:115], v[84:87]
s_add_u32 m0, m0, 4096
ds_read_b128 a[200:203], v12 offset:18432
ds_read_b128 a[204:207], v12 offset:19456
v_mfma_f32_16x16x32_bf16 v[84:87], a[148:151], a[116:119], v[84:87]
s_add_u32 s83, s83, 4096
v_add_f32 v88, v220, v188
v_add_f32 v89, v221, v188
v_mfma_f32_16x16x32_bf16 v[84:87], a[152:155], a[120:123], v[84:87]
s_and_b32 s86, s63, 1
v_add_f32 v90, v222, v188
v_add_f32 v91, v223, v188
v_mfma_f32_16x16x32_bf16 v[84:87], a[156:159], a[124:127], v[84:87]
buffer_load_dwordx4 v1, s[52:55], s83 offen sc1 lds
v_mfma_f32_16x16x32_bf16 v[88:91], a[160:163], a[112:115], v[88:91]
s_lshl_b32 s86, s86, 2
ds_read_b128 a[208:211], v12 offset:20480
ds_read_b128 a[212:215], v12 offset:21504
v_mfma_f32_16x16x32_bf16 v[88:91], a[164:167], a[116:119], v[88:91]
s_and_b32 s83, s81, 1
ds_read_b128 a[216:219], v12 offset:22528
v_add_f32 v92, v224, v188
v_mfma_f32_16x16x32_bf16 v[88:91], a[168:171], a[120:123], v[88:91]
v_add_f32 v93, v225, v188
v_add_f32 v94, v226, v188
v_add_f32 v95, v227, v188
v_mfma_f32_16x16x32_bf16 v[88:91], a[172:175], a[124:127], v[88:91]
s_mul_i32 s84, s81, 5
s_add_u32 s86, s86, s60
ds_read_b128 a[220:223], v12 offset:23552
v_mfma_f32_16x16x32_bf16 v[92:95], a[176:179], a[112:115], v[92:95]
s_add_u32 s85, s84, s83
s_mul_i32 s86, s86, 16384
ds_read_b128 a[224:227], v12 offset:24576
v_mfma_f32_16x16x32_bf16 v[92:95], a[180:183], a[116:119], v[92:95]
s_mul_i32 s85, s85, 136192
s_add_u32 s86, s86, 8388608
v_add_u32 v3, 0, v1
v_mfma_f32_16x16x32_bf16 v[92:95], a[184:187], a[120:123], v[92:95]
s_add_u32 s82, s85, s86
ds_read_b128 a[228:231], v12 offset:25600
ds_read_b128 a[232:235], v12 offset:26624
v_mfma_f32_16x16x32_bf16 v[92:95], a[188:191], a[124:127], v[92:95]
buffer_load_dwordx4 a[0:3], v3, s[52:55], s82 offen offset:0 sc1
v_mfma_f32_16x16x32_bf16 v[200:203], v[208:211], v[160:163], v[200:203]
s_add_u32 s90, s82, 136192
ds_read_b128 a[236:239], v12 offset:27648
ds_read_b128 a[240:243], v12 offset:28672
v_mfma_f32_16x16x32_bf16 v[200:203], v[208:211], v[164:167], v[200:203]
s_waitcnt lgkmcnt(4)
s_add_u32 s91, s82, 272384
ds_read_b128 a[244:247], v12 offset:29696
ds_read_b128 a[248:251], v12 offset:30720
v_mfma_f32_16x16x32_bf16 v[16:19], a[192:195], v[160:163], v[16:19]
s_add_u32 s100, s82, 408576
v_exp_f32 v136, v136
v_mfma_f32_16x16x32_bf16 v[16:19], a[196:199], v[164:167], v[16:19]
buffer_load_dwordx4 a[4:7], v3, s[52:55], s90 offen offset:0 sc1
v_mfma_f32_16x16x32_bf16 v[20:23], a[200:203], v[160:163], v[20:23]
s_add_u32 s69, s69, 32768
ds_read_b128 a[252:255], v12 offset:31744
v_cvt_pk_bf16_f32 v168, v128, v129
v_mfma_f32_16x16x32_bf16 v[20:23], a[204:207], v[164:167], v[20:23]
s_and_b32 s69, s69, 131071
v_rcp_f32 v228, v200
v_mfma_f32_16x16x32_bf16 v[24:27], a[208:211], v[160:163], v[24:27]
s_lshl_b32 s70, s60, 8
v_exp_f32 v137, v137
v_mfma_f32_16x16x32_bf16 v[24:27], a[212:215], v[164:167], v[24:27]
buffer_load_dwordx4 a[8:11], v3, s[52:55], s91 offen offset:0 sc1
v_mfma_f32_16x16x32_bf16 v[28:31], a[216:219], v[160:163], v[28:31]
s_lshl_b32 s83, s83, 2
v_mul_f32 v16, v228, v16
v_mul_f32 v17, v228, v17
v_mfma_f32_16x16x32_bf16 v[28:31], a[220:223], v[164:167], v[28:31]
s_add_u32 s70, s70, 157696
v_mul_f32 v18, v228, v18
v_mul_f32 v19, v228, v19
v_mfma_f32_16x16x32_bf16 v[32:35], a[224:227], v[160:163], v[32:35]
s_waitcnt lgkmcnt(0)
s_sub_u32 s84, s84, s83
v_cvt_pk_bf16_f32 v169, v130, v131
v_cvt_pk_bf16_f32 v16, v16, v17
v_mfma_f32_16x16x32_bf16 v[32:35], a[228:231], v[164:167], v[32:35]
buffer_load_dwordx4 a[12:15], v3, s[52:55], s100 offen offset:0 sc1
v_mfma_f32_16x16x32_bf16 v[36:39], a[232:235], v[160:163], v[36:39]
s_add_u32 s84, s84, 4
v_exp_f32 v138, v138
v_mfma_f32_16x16x32_bf16 v[36:39], a[236:239], v[164:167], v[36:39]
s_mul_i32 s84, s84, 136192
v_exp_f32 v139, v139
v_mfma_f32_16x16x32_bf16 v[40:43], a[240:243], v[160:163], v[40:43]
s_waitcnt vmcnt(6)
s_barrier
v_exp_f32 v140, v140
v_mfma_f32_16x16x32_bf16 v[40:43], a[244:247], v[164:167], v[40:43]
s_add_u32 s101, s84, s86
v_exp_f32 v141, v141
v_mfma_f32_16x16x32_bf16 v[44:47], a[248:251], v[160:163], v[44:47]
v_exp_f32 v142, v142
v_exp_f32 v143, v143
v_cvt_pk_bf16_f32 v170, v132, v133
v_cvt_pk_bf16_f32 v171, v134, v135
v_mfma_f32_16x16x32_bf16 v[44:47], a[252:255], v[164:167], v[44:47]
v_cvt_pk_bf16_f32 v172, v136, v137
v_cvt_pk_bf16_f32 v173, v138, v139
v_cvt_pk_bf16_f32 v174, v140, v141
v_cvt_pk_bf16_f32 v175, v142, v143
v_mfma_f32_16x16x32_bf16 v[204:207], v[208:211], v[168:171], v[204:207]
buffer_load_dwordx4 a[16:19], v3, s[52:55], s82 offen offset:1024 sc1
v_mfma_f32_16x16x32_bf16 v[204:207], v[208:211], v[172:175], v[204:207]
v_mul_f32 v20, v228, v20
v_mul_f32 v21, v228, v21
v_mfma_f32_16x16x32_bf16 v[48:51], a[192:195], v[168:171], v[48:51]
v_mul_f32 v22, v228, v22
v_mul_f32 v23, v228, v23
v_mfma_f32_16x16x32_bf16 v[48:51], a[196:199], v[172:175], v[48:51]
v_add_u32 v12, s69, v1
v_cvt_pk_bf16_f32 v17, v18, v19
v_mfma_f32_16x16x32_bf16 v[52:55], a[200:203], v[168:171], v[52:55]
buffer_load_dwordx4 a[20:23], v3, s[52:55], s90 offen offset:1024 sc1
v_mfma_f32_16x16x32_bf16 v[52:55], a[204:207], v[172:175], v[52:55]
v_cvt_pk_bf16_f32 v18, v20, v21
v_cvt_pk_bf16_f32 v19, v22, v23
v_mfma_f32_16x16x32_bf16 v[56:59], a[208:211], v[168:171], v[56:59]
ds_read_b128 a[128:131], v12 offset:16384
ds_read_b128 a[132:135], v12 offset:17408
v_mfma_f32_16x16x32_bf16 v[56:59], a[212:215], v[172:175], v[56:59]
ds_read_b128 a[136:139], v12 offset:18432
ds_read_b128 a[140:143], v12 offset:19456
v_mfma_f32_16x16x32_bf16 v[60:63], a[216:219], v[168:171], v[60:63]
buffer_store_dwordx4 v[16:19], v1, s[52:55], s75 offen offset:0 sc1
v_mfma_f32_16x16x32_bf16 v[60:63], a[220:223], v[172:175], v[60:63]
ds_read_b128 a[144:147], v12 offset:20480
v_mul_f32 v24, v228, v24
v_mfma_f32_16x16x32_bf16 v[64:67], a[224:227], v[168:171], v[64:67]
v_mul_f32 v25, v228, v25
v_mul_f32 v26, v228, v26
v_mfma_f32_16x16x32_bf16 v[64:67], a[228:231], v[172:175], v[64:67]
v_mul_f32 v27, v228, v27
v_cvt_pk_bf16_f32 v24, v24, v25
v_mfma_f32_16x16x32_bf16 v[68:71], a[232:235], v[168:171], v[68:71]
ds_read_b128 a[148:151], v12 offset:21504
v_mul_f32 v28, v228, v28
v_mfma_f32_16x16x32_bf16 v[68:71], a[236:239], v[172:175], v[68:71]
v_mul_f32 v29, v228, v29
v_mul_f32 v30, v228, v30
v_mfma_f32_16x16x32_bf16 v[72:75], a[240:243], v[168:171], v[72:75]
v_mul_f32 v31, v228, v31
v_cvt_pk_bf16_f32 v25, v26, v27
v_mfma_f32_16x16x32_bf16 v[72:75], a[244:247], v[172:175], v[72:75]
v_exp_f32 v80, v80
v_exp_f32 v81, v81
v_exp_f32 v82, v82
v_exp_f32 v83, v83
v_exp_f32 v84, v84
v_exp_f32 v85, v85
v_exp_f32 v86, v86
v_exp_f32 v87, v87
v_mfma_f32_16x16x32_bf16 v[76:79], a[248:251], v[168:171], v[76:79]
v_exp_f32 v88, v88
v_exp_f32 v89, v89
v_exp_f32 v90, v90
v_exp_f32 v91, v91
v_exp_f32 v92, v92
v_exp_f32 v93, v93
v_exp_f32 v94, v94
v_exp_f32 v95, v95
v_cvt_pk_bf16_f32 v144, v80, v81
v_cvt_pk_bf16_f32 v145, v82, v83
v_cvt_pk_bf16_f32 v146, v84, v85
v_cvt_pk_bf16_f32 v147, v86, v87
v_mfma_f32_16x16x32_bf16 v[76:79], a[252:255], v[172:175], v[76:79]
v_cvt_pk_bf16_f32 v148, v88, v89
v_cvt_pk_bf16_f32 v149, v90, v91
v_cvt_pk_bf16_f32 v150, v92, v93
v_cvt_pk_bf16_f32 v151, v94, v95
v_mfma_f32_16x16x32_bf16 v[204:207], v[208:211], v[144:147], v[204:207]
v_cvt_pk_bf16_f32 v26, v28, v29
v_cvt_pk_bf16_f32 v27, v30, v31
v_mfma_f32_16x16x32_bf16 v[204:207], v[208:211], v[148:151], v[204:207]
s_waitcnt lgkmcnt(0)
ds_read_b128 a[152:155], v12 offset:22528
ds_read_b128 a[156:159], v12 offset:23552
v_mfma_f32_16x16x32_bf16 v[48:51], a[128:131], v[144:147], v[48:51]
buffer_store_dwordx4 v[24:27], v1, s[52:55], s75 offen offset:1024 sc1
v_mfma_f32_16x16x32_bf16 v[48:51], a[132:135], v[148:151], v[48:51]
ds_read_b128 a[160:163], v12 offset:24576
ds_read_b128 a[164:167], v12 offset:25600
v_mfma_f32_16x16x32_bf16 v[52:55], a[136:139], v[144:147], v[52:55]
ds_read_b128 a[168:171], v12 offset:26624
v_mul_f32 v32, v228, v32
v_mfma_f32_16x16x32_bf16 v[52:55], a[140:143], v[148:151], v[52:55]
v_mul_f32 v33, v228, v33
v_mul_f32 v34, v228, v34
v_mfma_f32_16x16x32_bf16 v[56:59], a[144:147], v[144:147], v[56:59]
ds_read_b128 a[172:175], v12 offset:27648
v_mul_f32 v35, v228, v35
v_mfma_f32_16x16x32_bf16 v[56:59], a[148:151], v[148:151], v[56:59]
s_waitcnt lgkmcnt(4)
ds_read_b128 a[176:179], v12 offset:28672
v_cvt_pk_bf16_f32 v32, v32, v33
v_mfma_f32_16x16x32_bf16 v[60:63], a[152:155], v[144:147], v[60:63]
ds_read_b128 a[180:183], v12 offset:29696
v_mul_f32 v36, v228, v36
v_mfma_f32_16x16x32_bf16 v[60:63], a[156:159], v[148:151], v[60:63]
s_waitcnt lgkmcnt(3)
ds_read_b128 a[184:187], v12 offset:30720
v_mul_f32 v37, v228, v37
v_mfma_f32_16x16x32_bf16 v[64:67], a[160:163], v[144:147], v[64:67]
ds_read_b128 a[188:191], v12 offset:31744
v_mul_f32 v38, v228, v38
v_mfma_f32_16x16x32_bf16 v[64:67], a[164:167], v[148:151], v[64:67]
v_mul_f32 v39, v228, v39
v_cvt_pk_bf16_f32 v33, v34, v35
v_mfma_f32_16x16x32_bf16 v[68:71], a[168:171], v[144:147], v[68:71]
s_waitcnt lgkmcnt(3)
v_cvt_pk_bf16_f32 v34, v36, v37
v_cvt_pk_bf16_f32 v35, v38, v39
v_mfma_f32_16x16x32_bf16 v[68:71], a[172:175], v[148:151], v[68:71]
buffer_store_dwordx4 v[32:35], v1, s[52:55], s75 offen offset:2048 sc1
v_mfma_f32_16x16x32_bf16 v[72:75], a[176:179], v[144:147], v[72:75]
s_waitcnt lgkmcnt(1)
v_mul_f32 v40, v228, v40
v_mul_f32 v41, v228, v41
v_mfma_f32_16x16x32_bf16 v[72:75], a[180:183], v[148:151], v[72:75]
v_mul_f32 v42, v228, v42
v_mul_f32 v43, v228, v43
v_mfma_f32_16x16x32_bf16 v[76:79], a[184:187], v[144:147], v[76:79]
v_max_u32 v13, v13, v200
v_mul_f32 v44, v228, v44
v_mul_f32 v45, v228, v45
v_mul_f32 v46, v228, v46
v_mul_f32 v47, v228, v47
v_cvt_pk_bf16_f32 v40, v40, v41
v_cvt_pk_bf16_f32 v41, v42, v43
v_cvt_pk_bf16_f32 v42, v44, v45
v_cvt_pk_bf16_f32 v43, v46, v47
v_rcp_f32 v230, v204
v_max_u32 v13, v13, v204
v_lshl_add_u32 v232, v2, 2, s70
s_waitcnt lgkmcnt(0)
v_mfma_f32_16x16x32_bf16 v[76:79], a[188:191], v[148:151], v[76:79]
buffer_store_dwordx4 v[40:43], v1, s[52:55], s75 offen offset:3072 sc1
ds_write_b32 v232, v13
v_mov_b32 v231, v230
v_add_u32 v233, 157696, v1
v_pk_mul_f32 v[48:49], v[48:49], v[230:231]
v_pk_mul_f32 v[50:51], v[50:51], v[230:231]
v_pk_mul_f32 v[52:53], v[52:53], v[230:231]
v_pk_mul_f32 v[54:55], v[54:55], v[230:231]
v_cvt_pk_bf16_f32 v48, v48, v49
v_cvt_pk_bf16_f32 v49, v50, v51
v_cvt_pk_bf16_f32 v50, v52, v53
v_cvt_pk_bf16_f32 v51, v54, v55
buffer_store_dwordx4 v[48:51], v1, s[52:55], s76 offen offset:0 sc1
v_pk_mul_f32 v[56:57], v[56:57], v[230:231]
v_pk_mul_f32 v[58:59], v[58:59], v[230:231]
v_pk_mul_f32 v[60:61], v[60:61], v[230:231]
v_pk_mul_f32 v[62:63], v[62:63], v[230:231]
v_cvt_pk_bf16_f32 v56, v56, v57
v_cvt_pk_bf16_f32 v57, v58, v59
v_cvt_pk_bf16_f32 v58, v60, v61
v_cvt_pk_bf16_f32 v59, v62, v63
buffer_store_dwordx4 v[56:59], v1, s[52:55], s76 offen offset:1024 sc1
v_pk_mul_f32 v[64:65], v[64:65], v[230:231]
v_pk_mul_f32 v[66:67], v[66:67], v[230:231]
v_pk_mul_f32 v[68:69], v[68:69], v[230:231]
)ASM"
R"ASM(
v_pk_mul_f32 v[70:71], v[70:71], v[230:231]
v_cvt_pk_bf16_f32 v64, v64, v65
v_cvt_pk_bf16_f32 v65, v66, v67
v_cvt_pk_bf16_f32 v66, v68, v69
v_cvt_pk_bf16_f32 v67, v70, v71
buffer_store_dwordx4 v[64:67], v1, s[52:55], s76 offen offset:2048 sc1
v_pk_mul_f32 v[72:73], v[72:73], v[230:231]
v_pk_mul_f32 v[74:75], v[74:75], v[230:231]
v_pk_mul_f32 v[76:77], v[76:77], v[230:231]
v_pk_mul_f32 v[78:79], v[78:79], v[230:231]
v_cvt_pk_bf16_f32 v72, v72, v73
v_cvt_pk_bf16_f32 v73, v74, v75
v_cvt_pk_bf16_f32 v74, v76, v77
v_cvt_pk_bf16_f32 v75, v78, v79
buffer_store_dwordx4 v[72:75], v1, s[52:55], s76 offen offset:3072 sc1
s_waitcnt lgkmcnt(0)
s_barrier
ds_read_b128 v[234:237], v233
s_waitcnt lgkmcnt(0)
v_max3_u32 v234, v234, v235, v236
v_max_u32 v234, v234, v237
v_cmp_lt_u32 vcc, s94, v234
s_nop 0
s_cbranch_vccnz L_attn_redo
s_lshl_b32 s83, s60, 12
s_add_u32 m0, s83, 0
s_add_u32 s83, s101, 0
buffer_load_dwordx4 v1, s[52:55], s83 offen offset:0 sc1 lds
buffer_load_dwordx4 v1, s[52:55], s83 offen offset:1024 sc1 lds
buffer_load_dwordx4 v1, s[52:55], s83 offen offset:2048 sc1 lds
buffer_load_dwordx4 v1, s[52:55], s83 offen offset:3072 sc1 lds
s_and_b32 s86, s63, 1
s_lshl_b32 s86, s86, 2
s_add_u32 s86, s86, s60
s_lshl_b32 s86, s86, 14
s_add_u32 s86, s86, 29360128
s_and_b32 s2, s63, 1
s_mul_i32 s2, s2, 4358144
s_sub_u32 s2, s2, 2179072
s_add_u32 s83, s60, 1
s_and_b32 s83, s83, 3
s_lshl_b32 s84, s80, 2
s_add_u32 s84, s84, s83
s_xor_b32 s84, s84, 16
s_mul_i32 s84, s84, 136192
s_add_u32 s66, s84, s86
s_mul_i32 s83, s83, 15360
s_add_u32 s83, s83, 86016
v_add_u32 v4, s83, v1
s_add_u32 s83, s60, 2
s_and_b32 s83, s83, 3
s_lshl_b32 s84, s80, 2
s_add_u32 s84, s84, s83
s_xor_b32 s84, s84, 16
s_mul_i32 s84, s84, 136192
s_add_u32 s67, s84, s86
s_mul_i32 s83, s83, 15360
s_add_u32 s83, s83, 91136
v_add_u32 v7, s83, v1
s_add_u32 s83, s60, 3
s_and_b32 s83, s83, 3
s_lshl_b32 s84, s80, 2
s_add_u32 s84, s84, s83
s_xor_b32 s84, s84, 16
s_mul_i32 s84, s84, 136192
s_add_u32 s68, s84, s86
s_mul_i32 s83, s83, 15360
s_add_u32 s83, s83, 96256
v_add_u32 v8, s83, v1
s_add_u32 s83, s60, 4
s_and_b32 s83, s83, 3
s_lshl_b32 s84, s80, 2
s_add_u32 s84, s84, s83
s_xor_b32 s84, s84, 16
s_mul_i32 s84, s84, 136192
s_add_u32 s69, s84, s86
s_mul_i32 s83, s60, 15360
s_add_u32 s83, s83, 86016
v_add_u32 v9, s83, v1
s_and_b32 s83, s81, 1
s_mul_i32 s84, s81, 160
s_lshl_b32 s85, s83, 5
s_add_u32 s70, s84, s85
s_lshl_b32 s85, s83, 7
s_sub_u32 s85, 128, s85
s_add_u32 s71, s84, s85
s_lshl_b32 s75, s80, 2
s_add_u32 s75, s75, s60
s_mul_i32 s84, s75, 20480
s_add_u32 s84, s84, 5242880
s_lshr_b32 s85, s81, 1
s_mul_i32 s85, s85, 5120
s_add_u32 s84, s84, s85
s_mul_i32 s85, s83, 3072
s_add_u32 s72, s84, s85
s_lshl_b32 s85, s83, 9
s_add_u32 s85, s85, 2048
s_add_u32 s73, s84, s85
s_lshl_b32 s84, s75, 10
s_lshl_b32 s85, s81, 7
s_add_u32 s84, s84, s85
s_add_u32 s74, s84, 1179648
s_mul_i32 s75, s75, 20480
s_lshr_b32 s84, s81, 1
s_mul_i32 s84, s84, 320
s_add_u32 s84, s84, 147456
s_mul_i32 s85, s83, 192
s_add_u32 s92, s84, s85
s_lshl_b32 s85, s83, 3
s_add_u32 s85, s85, 128
s_add_u32 s93, s84, s85
v_lshrrev_b32 v12, 4, v2
v_lshlrev_b32 v12, 3, v12
v_lshlrev_b32 v13, 1, v12
s_lshl_b32 s85, s60, 12
v_add_u32 v127, s85, v1
v_and_b32 v0, 15, v2
v_mul_u32_u24 v0, 1280, v0
v_add_u32 v0, v0, v12
s_cmp_eq_u32 s60, 3
s_cselect_b32 s83, 0, 0x40000000
v_add_u32 v5, s83, v5
s_lshr_b32 s83, s79, 1
s_mul_i32 s84, s83, 7
s_and_b32 s85, s79, 1
s_lshl_b32 s86, s85, 2
s_add_u32 s86, s86, s84
s_add_u32 s84, s84, 3
s_mul_i32 s83, s79, 3
s_add_u32 s83, s83, 6
s_cmp_lt_u32 s79, 12
s_cselect_b32 s86, s86, s83
s_mul_i32 s76, s86, 40960
s_add_u32 s76, s76, 14680064
s_lshl_b32 s88, s86, 10
s_lshl_b32 s83, s84, 1
s_add_u32 s83, s83, s85
s_mul_i32 s77, s83, 20480
s_add_u32 s77, s77, 14680064
s_lshl_b32 s89, s84, 10
s_lshl_b32 s85, s85, 9
s_add_u32 s89, s89, s85
s_lshl_b32 s83, s78, 3
s_lshl_b32 s84, s60, 1
s_add_u32 s83, s83, s84
s_mul_i32 s83, s83, 55296
s_add_u32 s83, s83, 20971520
s_add_u32 s88, s88, s83
s_add_u32 s89, s89, s83
s_cmp_lt_u32 s79, 12
s_cselect_b32 s77, s77, 100663296
s_cselect_b32 s89, s89, 100663296
s_lshl_b32 s87, s78, 4
s_add_u32 s87, s87, s79
s_lshl_b32 s87, s87, 8
s_and_b32 s83, s81, 1
s_lshl_b32 s84, s60, 12
s_add_u32 s84, s84, 131072
s_lshl_b32 s85, s83, 10
s_add_u32 s85, s85, s84
s_mul_i32 s83, s83, 2016
s_sub_u32 s84, s84, s83
v_and_b32 v156, 15, v2
v_lshlrev_b32 v156, 6, v156
v_add_u32 v156, v156, v12
v_add_u32 v157, s85, v156
v_add_u32 v156, s84, v156
ds_read_b64 v[148:149], v157 offset:0
ds_read_b64 v[150:151], v157 offset:32
ds_read_b64 v[152:153], v157 offset:1024
ds_read_b64 v[154:155], v157 offset:1056
ds_read_b64 v[156:157], v156 offset:2048
s_lshl_b32 s83, s61, 3
s_add_u32 s83, s83, s62
s_lshl_b32 s83, s83, 8
s_add_u32 s83, s83, 327680
s_cmp_eq_u32 s63, 0
s_cselect_b32 s84, 0, 1073741824
s_waitcnt vmcnt(4) lgkmcnt(0)
s_barrier
buffer_store_dwordx2 v[10:11], v5, s[52:55], s83 offen sc1
buffer_store_dwordx2 v[10:11], v5, s[52:55], s84 offen sc1
s_lshl_b32 s83, s60, 12
s_add_u32 m0, s83, 16384
s_add_u32 s83, s101, 4096
buffer_load_dwordx4 v1, s[52:55], s83 offen offset:0 sc1 lds
buffer_load_dwordx4 v1, s[52:55], s83 offen offset:1024 sc1 lds
buffer_load_dwordx4 v1, s[52:55], s83 offen offset:2048 sc1 lds
L_pad_a_0_4:
s_cbranch_execz L_padret_a
buffer_load_dwordx4 v1, s[52:55], s83 offen offset:3072 sc1 lds
s_lshl_b32 s83, s60, 12
s_add_u32 m0, s83, 32768
s_add_u32 s83, s101, 8192
buffer_load_dwordx4 v1, s[52:55], s83 offen offset:0 sc1 lds
buffer_load_dwordx4 v1, s[52:55], s83 offen offset:1024 sc1 lds
buffer_load_dwordx4 v1, s[52:55], s83 offen offset:2048 sc1 lds
buffer_load_dwordx4 v1, s[52:55], s83 offen offset:3072 sc1 lds
s_lshl_b32 s83, s60, 12
s_add_u32 m0, s83, 49152
s_add_u32 s83, s101, 12288
buffer_load_dwordx4 v1, s[52:55], s83 offen offset:0 sc1 lds
buffer_load_dwordx4 v1, s[52:55], s83 offen offset:1024 sc1 lds
buffer_load_dwordx4 v1, s[52:55], s83 offen offset:2048 sc1 lds
buffer_load_dwordx4 v1, s[52:55], s83 offen offset:3072 sc1 lds
s_sub_u32 s83, 31, s80
s_min_u32 s83, s83, s80
s_lshr_b32 s83, s83, 1
s_and_b32 s84, s63, 1
s_lshl_b32 s84, s84, 2
s_add_u32 s84, s84, s60
s_lshl_b32 s84, s84, 5
s_add_u32 s83, s83, s84
v_and_b32 v6, 3, v2
v_lshlrev_b32 v6, 3, v6
v_add_u32 v6, s83, v6
v_and_b32 v126, 4, v2
v_lshrrev_b32 v126, 1, v126
v_xor_b32 v6, v6, v126
v_lshlrev_b32 v6, 8, v6
v_add_u32 v6, 327680, v6
s_mov_b64 exec, 0
s_cmp_eq_u32 s60, 0
s_cbranch_scc1 L_pad_b_0
s_cmp_eq_u32 s60, 1
s_cbranch_scc1 L_pad_b_1
s_cmp_eq_u32 s60, 2
s_cbranch_scc1 L_pad_b_2
s_branch L_pad_b_3
L_padret_b:
s_mov_b64 exec, -1
L_ch_b0:
buffer_load_dwordx4 a[24:27], v3, s[52:55], s91 offen offset:1024 sc1
buffer_load_dwordx4 a[28:31], v3, s[52:55], s100 offen offset:1024 sc1
buffer_load_dwordx4 a[32:35], v3, s[52:55], s82 offen offset:2048 sc1
buffer_load_dwordx4 a[36:39], v3, s[52:55], s90 offen offset:2048 sc1
buffer_load_dwordx4 a[40:43], v3, s[52:55], s91 offen offset:2048 sc1
buffer_load_dwordx4 a[44:47], v3, s[52:55], s100 offen offset:2048 sc1
buffer_load_dwordx4 a[48:51], v3, s[52:55], s82 offen offset:3072 sc1
buffer_load_dwordx4 a[52:55], v3, s[52:55], s90 offen offset:3072 sc1
buffer_load_dwordx4 a[56:59], v3, s[52:55], s91 offen offset:3072 sc1
buffer_load_dwordx4 a[60:63], v3, s[52:55], s100 offen offset:3072 sc1
v_add_u32 v3, 4096, v1
buffer_load_dwordx4 a[192:195], v3, s[52:55], s82 offen offset:0 sc1
buffer_load_dwordx4 a[196:199], v3, s[52:55], s90 offen offset:0 sc1
buffer_load_dwordx4 a[200:203], v3, s[52:55], s91 offen offset:0 sc1
buffer_load_dwordx4 a[204:207], v3, s[52:55], s100 offen offset:0 sc1
buffer_load_dwordx4 a[208:211], v3, s[52:55], s82 offen offset:1024 sc1
buffer_load_dwordx4 a[212:215], v3, s[52:55], s90 offen offset:1024 sc1
buffer_load_dwordx4 a[216:219], v3, s[52:55], s91 offen offset:1024 sc1
buffer_load_dwordx4 a[220:223], v3, s[52:55], s100 offen offset:1024 sc1
buffer_load_dwordx4 a[224:227], v3, s[52:55], s82 offen offset:2048 sc1
buffer_load_dwordx4 a[228:231], v3, s[52:55], s90 offen offset:2048 sc1
buffer_load_dwordx4 a[232:235], v3, s[52:55], s91 offen offset:2048 sc1
buffer_load_dwordx4 a[236:239], v3, s[52:55], s100 offen offset:2048 sc1
buffer_load_dwordx4 a[240:243], v3, s[52:55], s82 offen offset:3072 sc1
buffer_load_dwordx4 a[244:247], v3, s[52:55], s90 offen offset:3072 sc1
buffer_load_dwordx4 a[248:251], v3, s[52:55], s91 offen offset:3072 sc1
buffer_load_dwordx4 a[252:255], v3, s[52:55], s100 offen offset:3072 sc1
v_add_u32 v3, 8192, v1
buffer_load_dwordx4 a[128:131], v3, s[52:55], s82 offen offset:0 sc1
buffer_load_dwordx4 a[132:135], v3, s[52:55], s90 offen offset:0 sc1
buffer_load_dwordx4 a[136:139], v3, s[52:55], s91 offen offset:0 sc1
buffer_load_dwordx4 a[140:143], v3, s[52:55], s100 offen offset:0 sc1
buffer_load_dwordx4 a[144:147], v3, s[52:55], s82 offen offset:1024 sc1
buffer_load_dwordx4 a[148:151], v3, s[52:55], s90 offen offset:1024 sc1
buffer_load_dwordx4 a[152:155], v3, s[52:55], s91 offen offset:1024 sc1
buffer_load_dwordx4 a[156:159], v3, s[52:55], s100 offen offset:1024 sc1
buffer_load_dwordx4 a[160:163], v3, s[52:55], s82 offen offset:2048 sc1
buffer_load_dwordx4 a[164:167], v3, s[52:55], s90 offen offset:2048 sc1
buffer_load_dwordx4 a[168:171], v3, s[52:55], s91 offen offset:2048 sc1
buffer_load_dwordx4 a[172:175], v3, s[52:55], s100 offen offset:2048 sc1
buffer_load_dwordx4 a[176:179], v3, s[52:55], s82 offen offset:3072 sc1
buffer_load_dwordx4 a[180:183], v3, s[52:55], s90 offen offset:3072 sc1
buffer_load_dwordx2 v[10:11], v6, s[52:55], 0 offen sc1
buffer_load_dwordx4 a[184:187], v3, s[52:55], s91 offen offset:3072 sc1
buffer_load_dwordx4 a[188:191], v3, s[52:55], s100 offen offset:3072 sc1
v_add_u32 v3, 12288, v1
buffer_load_dwordx4 a[64:67], v3, s[52:55], s82 offen offset:0 sc1
s_waitcnt vmcnt(56)
buffer_load_dwordx4 a[68:71], v3, s[52:55], s90 offen offset:0 sc1
buffer_load_dwordx4 a[72:75], v3, s[52:55], s91 offen offset:0 sc1
buffer_load_dwordx4 a[76:79], v3, s[52:55], s100 offen offset:0 sc1
buffer_load_dwordx4 a[80:83], v3, s[52:55], s82 offen offset:1024 sc1
buffer_load_dwordx4 a[84:87], v3, s[52:55], s90 offen offset:1024 sc1
buffer_load_dwordx4 a[88:91], v3, s[52:55], s91 offen offset:1024 sc1
s_waitcnt vmcnt(56)
buffer_load_dwordx4 a[92:95], v3, s[52:55], s100 offen offset:1024 sc1
buffer_load_dwordx4 a[96:99], v3, s[52:55], s82 offen offset:2048 sc1
buffer_load_dwordx4 a[100:103], v3, s[52:55], s90 offen offset:2048 sc1
buffer_load_dwordx4 a[104:107], v3, s[52:55], s91 offen offset:2048 sc1
buffer_load_dwordx4 a[108:111], v3, s[52:55], s100 offen offset:2048 sc1
buffer_load_dwordx4 a[112:115], v3, s[52:55], s82 offen offset:3072 sc1
s_waitcnt vmcnt(15)
buffer_load_dwordx4 a[116:119], v3, s[52:55], s90 offen offset:3072 sc1
buffer_load_dwordx4 a[120:123], v3, s[52:55], s91 offen offset:3072 sc1
buffer_load_dwordx4 a[124:127], v3, s[52:55], s100 offen offset:3072 sc1
v_cmp_ne_u64 vcc, s[64:65], v[10:11]
s_nop 0
s_cbranch_vccnz L_stub_ctx
L_ret_ctx:
s_add_u32 s0, s66, 0
buffer_load_dwordx4 v[168:171], v1, s[52:55], s0 offen offset:0 sc1
buffer_load_dwordx4 v[172:175], v1, s[52:55], s0 offen offset:1024 sc1
buffer_load_dwordx4 v[176:179], v1, s[52:55], s0 offen offset:2048 sc1
buffer_load_dwordx4 v[180:183], v1, s[52:55], s0 offen offset:3072 sc1
s_add_u32 s0, s66, 4096
buffer_load_dwordx4 v[184:187], v1, s[52:55], s0 offen offset:0 sc1
buffer_load_dwordx4 v[188:191], v1, s[52:55], s0 offen offset:1024 sc1
buffer_load_dwordx4 v[192:195], v1, s[52:55], s0 offen offset:2048 sc1
buffer_load_dwordx4 v[196:199], v1, s[52:55], s0 offen offset:3072 sc1
s_add_u32 s0, s66, 8192
buffer_load_dwordx4 v[200:203], v1, s[52:55], s0 offen offset:0 sc1
buffer_load_dwordx4 v[204:207], v1, s[52:55], s0 offen offset:1024 sc1
buffer_load_dwordx4 v[208:211], v1, s[52:55], s0 offen offset:2048 sc1
buffer_load_dwordx4 v[212:215], v1, s[52:55], s0 offen offset:3072 sc1
s_waitcnt vmcnt(10)
v_mfma_f32_16x16x32_bf16 v[16:19], a[0:3], v[168:171], 0
s_add_u32 s0, s66, 12288
buffer_load_dwordx4 v[216:219], v1, s[52:55], s0 offen offset:0 sc1
v_mfma_f32_16x16x32_bf16 v[16:19], a[16:19], v[172:175], v[16:19]
buffer_load_dwordx4 v[220:223], v1, s[52:55], s0 offen offset:1024 sc1
ds_read_b128 v[240:243], v127 offset:0
v_mfma_f32_16x16x32_bf16 v[20:23], a[4:7], v[168:171], 0
s_add_u32 s66, s66, s2
ds_read_b128 v[244:247], v127 offset:1024
v_add_u32 v128, s92, v13
v_mfma_f32_16x16x32_bf16 v[20:23], a[20:23], v[172:175], v[20:23]
s_xor_b32 s1, s63, 1
ds_read_b128 v[248:251], v127 offset:2048
v_add_u32 v129, s93, v13
v_mfma_f32_16x16x32_bf16 v[24:27], a[8:11], v[168:171], 0
s_lshl_b32 s1, s1, 2
s_add_u32 s1, s1, s60
ds_read_b128 v[252:255], v127 offset:3072
v_mfma_f32_16x16x32_bf16 v[24:27], a[24:27], v[172:175], v[24:27]
buffer_load_dwordx4 v[224:227], v1, s[52:55], s0 offen offset:2048 sc1
v_mfma_f32_16x16x32_bf16 v[28:31], a[12:15], v[168:171], 0
s_mul_i32 s1, s1, 5120
s_add_u32 s1, s1, 92274688
v_mov_b32 v10, s64
v_mfma_f32_16x16x32_bf16 v[28:31], a[28:31], v[172:175], v[28:31]
s_waitcnt lgkmcnt(2)
v_mov_b32 v11, s65
v_mfma_f32_16x16x32_bf16 v[32:35], v[240:243], v[168:171], 0
s_waitcnt vmcnt(12)
ds_read_b128 v[240:243], v127 offset:16384
v_mfma_f32_16x16x32_bf16 v[32:35], v[244:247], v[172:175], v[32:35]
buffer_load_dwordx4 v[228:231], v1, s[52:55], s0 offen offset:3072 sc1
v_mfma_f32_16x16x32_bf16 v[16:19], a[32:35], v[176:179], v[16:19]
s_waitcnt vmcnt(12)
s_add_u32 s0, s67, 0
ds_read_b128 v[244:247], v127 offset:17408
v_mfma_f32_16x16x32_bf16 v[16:19], a[48:51], v[180:183], v[16:19]
v_mfma_f32_16x16x32_bf16 v[20:23], a[36:39], v[176:179], v[20:23]
v_mfma_f32_16x16x32_bf16 v[20:23], a[52:55], v[180:183], v[20:23]
buffer_load_dwordx4 v[232:235], v1, s[52:55], s0 offen offset:0 sc1
v_mfma_f32_16x16x32_bf16 v[24:27], a[40:43], v[176:179], v[24:27]
v_mfma_f32_16x16x32_bf16 v[24:27], a[56:59], v[180:183], v[24:27]
v_mfma_f32_16x16x32_bf16 v[28:31], a[44:47], v[176:179], v[28:31]
s_waitcnt lgkmcnt(0)
v_mfma_f32_16x16x32_bf16 v[28:31], a[60:63], v[180:183], v[28:31]
buffer_load_dwordx4 v[236:239], v1, s[52:55], s0 offen offset:1024 sc1
v_mfma_f32_16x16x32_bf16 v[32:35], v[248:251], v[176:179], v[32:35]
ds_read_b128 v[248:251], v127 offset:18432
v_mfma_f32_16x16x32_bf16 v[32:35], v[252:255], v[180:183], v[32:35]
s_waitcnt vmcnt(13)
ds_read_b128 v[252:255], v127 offset:19456
v_mfma_f32_16x16x32_bf16 v[16:19], a[192:195], v[184:187], v[16:19]
s_waitcnt vmcnt(12)
v_mfma_f32_16x16x32_bf16 v[16:19], a[208:211], v[188:191], v[16:19]
buffer_load_dwordx4 v[168:171], v1, s[52:55], s0 offen offset:2048 sc1
v_mfma_f32_16x16x32_bf16 v[20:23], a[196:199], v[184:187], v[20:23]
v_mfma_f32_16x16x32_bf16 v[20:23], a[212:215], v[188:191], v[20:23]
v_mfma_f32_16x16x32_bf16 v[24:27], a[200:203], v[184:187], v[24:27]
v_mfma_f32_16x16x32_bf16 v[24:27], a[216:219], v[188:191], v[24:27]
buffer_load_dwordx4 v[172:175], v1, s[52:55], s0 offen offset:3072 sc1
v_mfma_f32_16x16x32_bf16 v[28:31], a[204:207], v[184:187], v[28:31]
s_add_u32 s0, s67, 4096
v_mfma_f32_16x16x32_bf16 v[28:31], a[220:223], v[188:191], v[28:31]
v_mfma_f32_16x16x32_bf16 v[32:35], v[240:243], v[184:187], v[32:35]
s_waitcnt vmcnt(13)
ds_read_b128 v[240:243], v127 offset:32768
v_mfma_f32_16x16x32_bf16 v[32:35], v[244:247], v[188:191], v[32:35]
buffer_load_dwordx4 v[176:179], v1, s[52:55], s0 offen offset:0 sc1
v_mfma_f32_16x16x32_bf16 v[16:19], a[224:227], v[192:195], v[16:19]
s_waitcnt vmcnt(13)
ds_read_b128 v[244:247], v127 offset:33792
v_mfma_f32_16x16x32_bf16 v[16:19], a[240:243], v[196:199], v[16:19]
v_mfma_f32_16x16x32_bf16 v[20:23], a[228:231], v[192:195], v[20:23]
v_mfma_f32_16x16x32_bf16 v[20:23], a[244:247], v[196:199], v[20:23]
buffer_load_dwordx4 v[180:183], v1, s[52:55], s0 offen offset:1024 sc1
v_mfma_f32_16x16x32_bf16 v[24:27], a[232:235], v[192:195], v[24:27]
v_mfma_f32_16x16x32_bf16 v[24:27], a[248:251], v[196:199], v[24:27]
v_mfma_f32_16x16x32_bf16 v[28:31], a[236:239], v[192:195], v[28:31]
s_waitcnt lgkmcnt(0)
v_mfma_f32_16x16x32_bf16 v[28:31], a[252:255], v[196:199], v[28:31]
buffer_load_dwordx4 v[184:187], v1, s[52:55], s0 offen offset:2048 sc1
v_mfma_f32_16x16x32_bf16 v[32:35], v[248:251], v[192:195], v[32:35]
ds_read_b128 v[248:251], v127 offset:34816
)ASM"
R"ASM(
v_mfma_f32_16x16x32_bf16 v[32:35], v[252:255], v[196:199], v[32:35]
s_waitcnt vmcnt(14)
ds_read_b128 v[252:255], v127 offset:35840
v_mfma_f32_16x16x32_bf16 v[16:19], a[128:131], v[200:203], v[16:19]
s_waitcnt vmcnt(13)
ds_read_b128 v[158:161], v128 offset:0
v_mfma_f32_16x16x32_bf16 v[16:19], a[144:147], v[204:207], v[16:19]
buffer_load_dwordx4 v[188:191], v1, s[52:55], s0 offen offset:3072 sc1
v_mfma_f32_16x16x32_bf16 v[20:23], a[132:135], v[200:203], v[20:23]
s_add_u32 s0, s67, 8192
ds_read_b128 v[162:165], v128 offset:64
v_mfma_f32_16x16x32_bf16 v[20:23], a[148:151], v[204:207], v[20:23]
ds_read_b64 v[166:167], v129 offset:0
v_mfma_f32_16x16x32_bf16 v[24:27], a[136:139], v[200:203], v[24:27]
ds_read_b128 v[116:119], v128 offset:3840
v_mfma_f32_16x16x32_bf16 v[24:27], a[152:155], v[204:207], v[24:27]
buffer_load_dwordx4 v[192:195], v1, s[52:55], s0 offen offset:0 sc1
v_mfma_f32_16x16x32_bf16 v[28:31], a[140:143], v[200:203], v[28:31]
ds_read_b128 v[120:123], v128 offset:3904
v_mfma_f32_16x16x32_bf16 v[28:31], a[156:159], v[204:207], v[28:31]
ds_read_b64 v[124:125], v129 offset:3840
v_mfma_f32_16x16x32_bf16 v[32:35], v[240:243], v[200:203], v[32:35]
s_waitcnt vmcnt(14)
ds_read_b128 v[106:109], v128 offset:2560
v_mfma_f32_16x16x32_bf16 v[32:35], v[244:247], v[204:207], v[32:35]
buffer_load_dwordx4 v[196:199], v1, s[52:55], s0 offen offset:1024 sc1
v_mfma_f32_16x16x32_bf16 v[16:19], a[160:163], v[208:211], v[16:19]
s_waitcnt vmcnt(14)
ds_read_b128 v[110:113], v128 offset:2624
v_mfma_f32_16x16x32_bf16 v[16:19], a[176:179], v[212:215], v[16:19]
ds_read_b64 v[114:115], v129 offset:2560
v_mfma_f32_16x16x32_bf16 v[20:23], a[164:167], v[208:211], v[20:23]
ds_read_b128 v[240:243], v127 offset:49152
v_mfma_f32_16x16x32_bf16 v[20:23], a[180:183], v[212:215], v[20:23]
buffer_load_dwordx4 v[200:203], v1, s[52:55], s0 offen offset:2048 sc1
v_mfma_f32_16x16x32_bf16 v[24:27], a[168:171], v[208:211], v[24:27]
ds_read_b128 v[244:247], v127 offset:50176
v_mfma_f32_16x16x32_bf16 v[24:27], a[184:187], v[212:215], v[24:27]
v_mfma_f32_16x16x32_bf16 v[28:31], a[172:175], v[208:211], v[28:31]
s_waitcnt lgkmcnt(1)
v_mfma_f32_16x16x32_bf16 v[28:31], a[188:191], v[212:215], v[28:31]
buffer_load_dwordx4 v[204:207], v1, s[52:55], s0 offen offset:3072 sc1
v_mfma_f32_16x16x32_bf16 v[32:35], v[248:251], v[208:211], v[32:35]
s_add_u32 s0, s67, 12288
s_add_u32 s67, s67, s2
ds_read_b128 v[248:251], v127 offset:51200
v_mfma_f32_16x16x32_bf16 v[32:35], v[252:255], v[212:215], v[32:35]
s_waitcnt vmcnt(15)
ds_read_b128 v[252:255], v127 offset:52224
v_mfma_f32_16x16x32_bf16 v[16:19], a[64:67], v[216:219], v[16:19]
s_waitcnt vmcnt(14)
v_mfma_f32_16x16x32_bf16 v[16:19], a[80:83], v[220:223], v[16:19]
buffer_load_dwordx4 v[208:211], v1, s[52:55], s0 offen offset:0 sc1
v_mfma_f32_16x16x32_bf16 v[20:23], a[68:71], v[216:219], v[20:23]
v_mfma_f32_16x16x32_bf16 v[20:23], a[84:87], v[220:223], v[20:23]
v_mfma_f32_16x16x32_bf16 v[24:27], a[72:75], v[216:219], v[24:27]
v_mfma_f32_16x16x32_bf16 v[24:27], a[88:91], v[220:223], v[24:27]
buffer_load_dwordx4 v[212:215], v1, s[52:55], s0 offen offset:1024 sc1
v_mfma_f32_16x16x32_bf16 v[28:31], a[76:79], v[216:219], v[28:31]
v_mfma_f32_16x16x32_bf16 v[28:31], a[92:95], v[220:223], v[28:31]
v_mfma_f32_16x16x32_bf16 v[32:35], v[240:243], v[216:219], v[32:35]
s_waitcnt vmcnt(15) lgkmcnt(0)
ds_read_b128 v[240:243], v127 offset:0
v_mfma_f32_16x16x32_bf16 v[32:35], v[244:247], v[220:223], v[32:35]
buffer_load_dwordx4 v[216:219], v1, s[52:55], s0 offen offset:2048 sc1
v_mfma_f32_16x16x32_bf16 v[16:19], a[96:99], v[224:227], v[16:19]
s_waitcnt vmcnt(15)
ds_read_b128 v[244:247], v127 offset:1024
v_mfma_f32_16x16x32_bf16 v[16:19], a[112:115], v[228:231], v[16:19]
v_mfma_f32_16x16x32_bf16 v[20:23], a[100:103], v[224:227], v[20:23]
v_mfma_f32_16x16x32_bf16 v[20:23], a[116:119], v[228:231], v[20:23]
buffer_load_dwordx4 v[220:223], v1, s[52:55], s0 offen offset:3072 sc1
v_mfma_f32_16x16x32_bf16 v[24:27], a[104:107], v[224:227], v[24:27]
s_add_u32 s0, s68, 0
v_mfma_f32_16x16x32_bf16 v[24:27], a[120:123], v[228:231], v[24:27]
v_mfma_f32_16x16x32_bf16 v[28:31], a[108:111], v[224:227], v[28:31]
v_mfma_f32_16x16x32_bf16 v[28:31], a[124:127], v[228:231], v[28:31]
v_mfma_f32_16x16x32_bf16 v[32:35], v[248:251], v[224:227], v[32:35]
buffer_load_dwordx4 v[224:227], v1, s[52:55], s0 offen offset:0 sc1
v_mfma_f32_16x16x32_bf16 v[32:35], v[252:255], v[228:231], v[32:35]
s_waitcnt vmcnt(16)
ds_write_b128 v4, v[16:19] offset:0
v_mfma_f32_16x16x32_bf16 v[36:39], a[0:3], v[232:235], 0
s_waitcnt vmcnt(15)
ds_read_b128 v[248:251], v127 offset:2048
v_and_b32 v129, 0xffff0000, v158
v_mfma_f32_16x16x32_bf16 v[36:39], a[16:19], v[236:239], v[36:39]
ds_write_b128 v4, v[20:23] offset:1024
v_mfma_f32_16x16x32_bf16 v[40:43], a[4:7], v[232:235], 0
buffer_load_dwordx4 v[228:231], v1, s[52:55], s0 offen offset:1024 sc1
v_mfma_f32_16x16x32_bf16 v[40:43], a[20:23], v[236:239], v[40:43]
ds_write_b128 v4, v[24:27] offset:2048
v_mfma_f32_16x16x32_bf16 v[44:47], a[8:11], v[232:235], 0
ds_read_b128 v[252:255], v127 offset:3072
v_lshlrev_b32 v128, 16, v158
v_mfma_f32_16x16x32_bf16 v[44:47], a[24:27], v[236:239], v[44:47]
ds_write_b128 v4, v[28:31] offset:3072
v_mfma_f32_16x16x32_bf16 v[48:51], a[12:15], v[232:235], 0
v_and_b32 v131, 0xffff0000, v159
v_lshlrev_b32 v130, 16, v159
v_mfma_f32_16x16x32_bf16 v[48:51], a[28:31], v[236:239], v[48:51]
s_waitcnt lgkmcnt(3)
ds_write_b128 v4, v[32:35] offset:4096
v_mfma_f32_16x16x32_bf16 v[52:55], v[240:243], v[232:235], 0
buffer_load_dwordx4 v[232:235], v1, s[52:55], s0 offen offset:2048 sc1
v_mfma_f32_16x16x32_bf16 v[52:55], v[244:247], v[236:239], v[52:55]
s_waitcnt vmcnt(16)
ds_read_b128 v[240:243], v127 offset:16384
v_and_b32 v97, 0xffff0000, v148
v_mfma_f32_16x16x32_bf16 v[36:39], a[32:35], v[168:171], v[36:39]
s_waitcnt vmcnt(15)
ds_read_b128 v[244:247], v127 offset:17408
v_lshlrev_b32 v96, 16, v148
v_mfma_f32_16x16x32_bf16 v[36:39], a[48:51], v[172:175], v[36:39]
v_and_b32 v99, 0xffff0000, v149
v_lshlrev_b32 v98, 16, v149
v_mfma_f32_16x16x32_bf16 v[40:43], a[36:39], v[168:171], v[40:43]
buffer_load_dwordx4 v[236:239], v1, s[52:55], s0 offen offset:3072 sc1
v_mfma_f32_16x16x32_bf16 v[40:43], a[52:55], v[172:175], v[40:43]
s_add_u32 s0, s68, 4096
v_and_b32 v133, 0xffff0000, v160
v_lshlrev_b32 v132, 16, v160
v_mfma_f32_16x16x32_bf16 v[44:47], a[40:43], v[168:171], v[44:47]
v_and_b32 v135, 0xffff0000, v161
v_lshlrev_b32 v134, 16, v161
v_mfma_f32_16x16x32_bf16 v[44:47], a[56:59], v[172:175], v[44:47]
v_and_b32 v101, 0xffff0000, v150
v_lshlrev_b32 v100, 16, v150
v_mfma_f32_16x16x32_bf16 v[48:51], a[44:47], v[168:171], v[48:51]
v_and_b32 v103, 0xffff0000, v151
v_lshlrev_b32 v102, 16, v151
v_mfma_f32_16x16x32_bf16 v[48:51], a[60:63], v[172:175], v[48:51]
s_waitcnt lgkmcnt(0)
v_mov_b32 v148, v106
v_mov_b32 v149, v107
v_mfma_f32_16x16x32_bf16 v[52:55], v[248:251], v[168:171], v[52:55]
buffer_load_dwordx4 v[168:171], v1, s[52:55], s0 offen offset:0 sc1
v_mfma_f32_16x16x32_bf16 v[52:55], v[252:255], v[172:175], v[52:55]
s_waitcnt vmcnt(16)
ds_read_b128 v[248:251], v127 offset:18432
v_and_b32 v137, 0xffff0000, v162
v_mfma_f32_16x16x32_bf16 v[36:39], a[192:195], v[176:179], v[36:39]
s_waitcnt vmcnt(15)
ds_read_b128 v[252:255], v127 offset:19456
v_lshlrev_b32 v136, 16, v162
v_mfma_f32_16x16x32_bf16 v[36:39], a[208:211], v[180:183], v[36:39]
v_and_b32 v139, 0xffff0000, v163
v_lshlrev_b32 v138, 16, v163
v_mfma_f32_16x16x32_bf16 v[40:43], a[196:199], v[176:179], v[40:43]
buffer_load_dwordx4 v[172:175], v1, s[52:55], s0 offen offset:1024 sc1
v_mfma_f32_16x16x32_bf16 v[40:43], a[212:215], v[180:183], v[40:43]
v_and_b32 v105, 0xffff0000, v152
v_lshlrev_b32 v104, 16, v152
v_mfma_f32_16x16x32_bf16 v[44:47], a[200:203], v[176:179], v[44:47]
v_and_b32 v107, 0xffff0000, v153
v_lshlrev_b32 v106, 16, v153
v_mfma_f32_16x16x32_bf16 v[44:47], a[216:219], v[180:183], v[44:47]
v_and_b32 v159, 0xffff0000, v111
v_lshlrev_b32 v158, 16, v111
v_mfma_f32_16x16x32_bf16 v[48:51], a[204:207], v[176:179], v[48:51]
v_mov_b32 v150, v108
v_mov_b32 v151, v109
v_mfma_f32_16x16x32_bf16 v[48:51], a[220:223], v[180:183], v[48:51]
v_and_b32 v141, 0xffff0000, v164
v_mov_b32 v152, v110
v_mfma_f32_16x16x32_bf16 v[52:55], v[240:243], v[176:179], v[52:55]
buffer_load_dwordx4 v[176:179], v1, s[52:55], s0 offen offset:2048 sc1
v_mfma_f32_16x16x32_bf16 v[52:55], v[244:247], v[180:183], v[52:55]
s_waitcnt vmcnt(16)
ds_read_b128 v[240:243], v127 offset:32768
v_lshlrev_b32 v140, 16, v164
v_mfma_f32_16x16x32_bf16 v[36:39], a[224:227], v[184:187], v[36:39]
s_waitcnt vmcnt(15)
ds_read_b128 v[244:247], v127 offset:33792
v_and_b32 v143, 0xffff0000, v165
v_mfma_f32_16x16x32_bf16 v[36:39], a[240:243], v[188:191], v[36:39]
v_lshlrev_b32 v142, 16, v165
v_and_b32 v109, 0xffff0000, v154
v_mfma_f32_16x16x32_bf16 v[40:43], a[228:231], v[184:187], v[40:43]
buffer_load_dwordx4 v[180:183], v1, s[52:55], s0 offen offset:3072 sc1
v_mfma_f32_16x16x32_bf16 v[40:43], a[244:247], v[188:191], v[40:43]
s_add_u32 s0, s68, 8192
v_lshlrev_b32 v108, 16, v154
v_and_b32 v111, 0xffff0000, v155
v_mfma_f32_16x16x32_bf16 v[44:47], a[232:235], v[184:187], v[44:47]
v_and_b32 v145, 0xffff0000, v166
v_lshlrev_b32 v110, 16, v155
v_mfma_f32_16x16x32_bf16 v[44:47], a[248:251], v[188:191], v[44:47]
v_lshlrev_b32 v144, 16, v166
v_and_b32 v147, 0xffff0000, v167
v_mfma_f32_16x16x32_bf16 v[48:51], a[236:239], v[184:187], v[48:51]
v_lshlrev_b32 v146, 16, v167
v_and_b32 v161, 0xffff0000, v112
v_mfma_f32_16x16x32_bf16 v[48:51], a[252:255], v[188:191], v[48:51]
s_waitcnt lgkmcnt(0)
v_lshlrev_b32 v160, 16, v112
v_and_b32 v163, 0xffff0000, v113
v_mfma_f32_16x16x32_bf16 v[52:55], v[248:251], v[184:187], v[52:55]
buffer_load_dwordx4 v[184:187], v1, s[52:55], s0 offen offset:0 sc1
v_mfma_f32_16x16x32_bf16 v[52:55], v[252:255], v[188:191], v[52:55]
s_waitcnt vmcnt(16)
ds_read_b128 v[248:251], v127 offset:34816
v_lshlrev_b32 v162, 16, v113
v_mfma_f32_16x16x32_bf16 v[36:39], a[128:131], v[192:195], v[36:39]
s_waitcnt vmcnt(15)
ds_read_b128 v[252:255], v127 offset:35840
v_and_b32 v165, 0xffff0000, v114
v_mfma_f32_16x16x32_bf16 v[36:39], a[144:147], v[196:199], v[36:39]
v_lshlrev_b32 v164, 16, v114
v_and_b32 v167, 0xffff0000, v115
v_mfma_f32_16x16x32_bf16 v[40:43], a[132:135], v[192:195], v[40:43]
buffer_load_dwordx4 v[188:191], v1, s[52:55], s0 offen offset:1024 sc1
v_mfma_f32_16x16x32_bf16 v[40:43], a[148:151], v[196:199], v[40:43]
v_lshlrev_b32 v166, 16, v115
v_and_b32 v113, 0xffff0000, v156
v_mfma_f32_16x16x32_bf16 v[44:47], a[136:139], v[192:195], v[44:47]
v_lshlrev_b32 v112, 16, v156
v_and_b32 v115, 0xffff0000, v157
v_mfma_f32_16x16x32_bf16 v[44:47], a[152:155], v[196:199], v[44:47]
v_lshlrev_b32 v114, 16, v157
v_and_b32 v157, 0xffff0000, v152
v_mfma_f32_16x16x32_bf16 v[48:51], a[140:143], v[192:195], v[48:51]
v_lshlrev_b32 v156, 16, v152
v_and_b32 v155, 0xffff0000, v151
v_mfma_f32_16x16x32_bf16 v[48:51], a[156:159], v[196:199], v[48:51]
v_lshlrev_b32 v154, 16, v151
v_and_b32 v153, 0xffff0000, v150
v_mfma_f32_16x16x32_bf16 v[52:55], v[240:243], v[192:195], v[52:55]
buffer_load_dwordx4 v[192:195], v1, s[52:55], s0 offen offset:2048 sc1
v_mfma_f32_16x16x32_bf16 v[52:55], v[244:247], v[196:199], v[52:55]
s_waitcnt vmcnt(16)
ds_read_b128 v[240:243], v127 offset:49152
v_lshlrev_b32 v152, 16, v150
v_mfma_f32_16x16x32_bf16 v[36:39], a[160:163], v[200:203], v[36:39]
s_waitcnt vmcnt(15)
ds_read_b128 v[244:247], v127 offset:50176
v_and_b32 v151, 0xffff0000, v149
v_mfma_f32_16x16x32_bf16 v[36:39], a[176:179], v[204:207], v[36:39]
v_lshlrev_b32 v150, 16, v149
v_and_b32 v149, 0xffff0000, v148
v_mfma_f32_16x16x32_bf16 v[40:43], a[164:167], v[200:203], v[40:43]
buffer_load_dwordx4 v[196:199], v1, s[52:55], s0 offen offset:3072 sc1
v_mfma_f32_16x16x32_bf16 v[40:43], a[180:183], v[204:207], v[40:43]
s_add_u32 s0, s68, 12288
s_add_u32 s68, s68, s2
v_lshlrev_b32 v148, 16, v148
v_mfma_f32_16x16x32_bf16 v[44:47], a[168:171], v[200:203], v[44:47]
v_mfma_f32_16x16x32_bf16 v[44:47], a[184:187], v[204:207], v[44:47]
v_mfma_f32_16x16x32_bf16 v[48:51], a[172:175], v[200:203], v[48:51]
v_mfma_f32_16x16x32_bf16 v[48:51], a[188:191], v[204:207], v[48:51]
s_waitcnt lgkmcnt(0)
v_mfma_f32_16x16x32_bf16 v[52:55], v[248:251], v[200:203], v[52:55]
buffer_load_dwordx4 v[200:203], v1, s[52:55], s0 offen offset:0 sc1
v_mfma_f32_16x16x32_bf16 v[52:55], v[252:255], v[204:207], v[52:55]
s_waitcnt vmcnt(16)
ds_read_b128 v[248:251], v127 offset:51200
v_mfma_f32_16x16x32_bf16 v[36:39], a[64:67], v[208:211], v[36:39]
s_waitcnt vmcnt(15)
ds_read_b128 v[252:255], v127 offset:52224
v_mfma_f32_16x16x32_bf16 v[36:39], a[80:83], v[212:215], v[36:39]
v_mfma_f32_16x16x32_bf16 v[40:43], a[68:71], v[208:211], v[40:43]
buffer_load_dwordx4 v[204:207], v1, s[52:55], s0 offen offset:1024 sc1
v_mfma_f32_16x16x32_bf16 v[40:43], a[84:87], v[212:215], v[40:43]
v_mfma_f32_16x16x32_bf16 v[44:47], a[72:75], v[208:211], v[44:47]
v_mfma_f32_16x16x32_bf16 v[44:47], a[88:91], v[212:215], v[44:47]
v_mfma_f32_16x16x32_bf16 v[48:51], a[76:79], v[208:211], v[48:51]
v_mfma_f32_16x16x32_bf16 v[48:51], a[92:95], v[212:215], v[48:51]
v_mfma_f32_16x16x32_bf16 v[52:55], v[240:243], v[208:211], v[52:55]
buffer_load_dwordx4 v[208:211], v1, s[52:55], s0 offen offset:2048 sc1
v_mfma_f32_16x16x32_bf16 v[52:55], v[244:247], v[212:215], v[52:55]
s_waitcnt vmcnt(16)
ds_read_b128 v[240:243], v127 offset:0
v_mfma_f32_16x16x32_bf16 v[36:39], a[96:99], v[216:219], v[36:39]
s_waitcnt vmcnt(15)
ds_read_b128 v[244:247], v127 offset:1024
v_mfma_f32_16x16x32_bf16 v[36:39], a[112:115], v[220:223], v[36:39]
v_mfma_f32_16x16x32_bf16 v[40:43], a[100:103], v[216:219], v[40:43]
buffer_load_dwordx4 v[212:215], v1, s[52:55], s0 offen offset:3072 sc1
v_mfma_f32_16x16x32_bf16 v[40:43], a[116:119], v[220:223], v[40:43]
s_add_u32 s0, s69, 0
v_mfma_f32_16x16x32_bf16 v[44:47], a[104:107], v[216:219], v[44:47]
v_mfma_f32_16x16x32_bf16 v[44:47], a[120:123], v[220:223], v[44:47]
v_mfma_f32_16x16x32_bf16 v[48:51], a[108:111], v[216:219], v[48:51]
v_mfma_f32_16x16x32_bf16 v[48:51], a[124:127], v[220:223], v[48:51]
s_waitcnt lgkmcnt(0)
v_mfma_f32_16x16x32_bf16 v[52:55], v[248:251], v[216:219], v[52:55]
buffer_load_dwordx4 v[216:219], v1, s[52:55], s0 offen offset:0 sc1
v_mfma_f32_16x16x32_bf16 v[52:55], v[252:255], v[220:223], v[52:55]
s_waitcnt vmcnt(16)
ds_write_b128 v7, v[36:39] offset:0
v_mfma_f32_16x16x32_bf16 v[16:19], a[0:3], v[224:227], 0
s_waitcnt vmcnt(15)
ds_read_b128 v[248:251], v127 offset:2048
v_mfma_f32_16x16x32_bf16 v[16:19], a[16:19], v[228:231], v[16:19]
ds_write_b128 v7, v[40:43] offset:1024
v_mfma_f32_16x16x32_bf16 v[20:23], a[4:7], v[224:227], 0
buffer_load_dwordx4 v[220:223], v1, s[52:55], s0 offen offset:1024 sc1
v_mfma_f32_16x16x32_bf16 v[20:23], a[20:23], v[228:231], v[20:23]
ds_write_b128 v7, v[44:47] offset:2048
v_mfma_f32_16x16x32_bf16 v[24:27], a[8:11], v[224:227], 0
ds_read_b128 v[252:255], v127 offset:3072
v_mfma_f32_16x16x32_bf16 v[24:27], a[24:27], v[228:231], v[24:27]
ds_write_b128 v7, v[48:51] offset:3072
v_mfma_f32_16x16x32_bf16 v[28:31], a[12:15], v[224:227], 0
v_mfma_f32_16x16x32_bf16 v[28:31], a[28:31], v[228:231], v[28:31]
ds_write_b128 v7, v[52:55] offset:4096
v_mfma_f32_16x16x32_bf16 v[32:35], v[240:243], v[224:227], 0
buffer_load_dwordx4 v[224:227], v1, s[52:55], s0 offen offset:2048 sc1
v_mfma_f32_16x16x32_bf16 v[32:35], v[244:247], v[228:231], v[32:35]
s_waitcnt vmcnt(16)
ds_read_b128 v[240:243], v127 offset:16384
v_mfma_f32_16x16x32_bf16 v[16:19], a[32:35], v[232:235], v[16:19]
s_waitcnt vmcnt(15)
ds_read_b128 v[244:247], v127 offset:17408
v_mfma_f32_16x16x32_bf16 v[16:19], a[48:51], v[236:239], v[16:19]
v_mfma_f32_16x16x32_bf16 v[20:23], a[36:39], v[232:235], v[20:23]
buffer_load_dwordx4 v[228:231], v1, s[52:55], s0 offen offset:3072 sc1
v_mfma_f32_16x16x32_bf16 v[20:23], a[52:55], v[236:239], v[20:23]
s_add_u32 s0, s69, 4096
v_mfma_f32_16x16x32_bf16 v[24:27], a[40:43], v[232:235], v[24:27]
v_mfma_f32_16x16x32_bf16 v[24:27], a[56:59], v[236:239], v[24:27]
v_mfma_f32_16x16x32_bf16 v[28:31], a[44:47], v[232:235], v[28:31]
v_mfma_f32_16x16x32_bf16 v[28:31], a[60:63], v[236:239], v[28:31]
s_waitcnt lgkmcnt(0)
v_mfma_f32_16x16x32_bf16 v[32:35], v[248:251], v[232:235], v[32:35]
buffer_load_dwordx4 v[232:235], v1, s[52:55], s0 offen offset:0 sc1
v_mfma_f32_16x16x32_bf16 v[32:35], v[252:255], v[236:239], v[32:35]
s_waitcnt vmcnt(16)
ds_read_b128 v[248:251], v127 offset:18432
v_mfma_f32_16x16x32_bf16 v[16:19], a[192:195], v[168:171], v[16:19]
s_waitcnt vmcnt(15)
ds_read_b128 v[252:255], v127 offset:19456
v_mfma_f32_16x16x32_bf16 v[16:19], a[208:211], v[172:175], v[16:19]
v_mfma_f32_16x16x32_bf16 v[20:23], a[196:199], v[168:171], v[20:23]
buffer_load_dwordx4 v[236:239], v1, s[52:55], s0 offen offset:1024 sc1
v_mfma_f32_16x16x32_bf16 v[20:23], a[212:215], v[172:175], v[20:23]
v_mfma_f32_16x16x32_bf16 v[24:27], a[200:203], v[168:171], v[24:27]
v_mfma_f32_16x16x32_bf16 v[24:27], a[216:219], v[172:175], v[24:27]
v_mfma_f32_16x16x32_bf16 v[28:31], a[204:207], v[168:171], v[28:31]
v_mfma_f32_16x16x32_bf16 v[28:31], a[220:223], v[172:175], v[28:31]
v_mfma_f32_16x16x32_bf16 v[32:35], v[240:243], v[168:171], v[32:35]
buffer_load_dwordx4 v[168:171], v1, s[52:55], s0 offen offset:2048 sc1
v_mfma_f32_16x16x32_bf16 v[32:35], v[244:247], v[172:175], v[32:35]
s_waitcnt vmcnt(16)
ds_read_b128 v[240:243], v127 offset:32768
v_mfma_f32_16x16x32_bf16 v[16:19], a[224:227], v[176:179], v[16:19]
s_waitcnt vmcnt(15)
ds_read_b128 v[244:247], v127 offset:33792
v_mfma_f32_16x16x32_bf16 v[16:19], a[240:243], v[180:183], v[16:19]
v_mfma_f32_16x16x32_bf16 v[20:23], a[228:231], v[176:179], v[20:23]
buffer_load_dwordx4 v[172:175], v1, s[52:55], s0 offen offset:3072 sc1
v_mfma_f32_16x16x32_bf16 v[20:23], a[244:247], v[180:183], v[20:23]
s_add_u32 s0, s69, 8192
v_mfma_f32_16x16x32_bf16 v[24:27], a[232:235], v[176:179], v[24:27]
v_mfma_f32_16x16x32_bf16 v[24:27], a[248:251], v[180:183], v[24:27]
v_mfma_f32_16x16x32_bf16 v[28:31], a[236:239], v[176:179], v[28:31]
v_mfma_f32_16x16x32_bf16 v[28:31], a[252:255], v[180:183], v[28:31]
s_waitcnt lgkmcnt(0)
v_mfma_f32_16x16x32_bf16 v[32:35], v[248:251], v[176:179], v[32:35]
buffer_load_dwordx4 v[176:179], v1, s[52:55], s0 offen offset:0 sc1
v_mfma_f32_16x16x32_bf16 v[32:35], v[252:255], v[180:183], v[32:35]
s_waitcnt vmcnt(16)
ds_read_b128 v[248:251], v127 offset:34816
v_mfma_f32_16x16x32_bf16 v[16:19], a[128:131], v[184:187], v[16:19]
s_waitcnt vmcnt(15)
ds_read_b128 v[252:255], v127 offset:35840
v_mfma_f32_16x16x32_bf16 v[16:19], a[144:147], v[188:191], v[16:19]
L_pad_b_0:
s_cbranch_execz L_pad_b_0_1
v_mfma_f32_16x16x32_bf16 v[20:23], a[132:135], v[184:187], v[20:23]
buffer_load_dwordx4 v[180:183], v1, s[52:55], s0 offen offset:1024 sc1
v_mfma_f32_16x16x32_bf16 v[20:23], a[148:151], v[188:191], v[20:23]
v_mfma_f32_16x16x32_bf16 v[24:27], a[136:139], v[184:187], v[24:27]
v_mfma_f32_16x16x32_bf16 v[24:27], a[152:155], v[188:191], v[24:27]
v_mfma_f32_16x16x32_bf16 v[28:31], a[140:143], v[184:187], v[28:31]
v_mfma_f32_16x16x32_bf16 v[28:31], a[156:159], v[188:191], v[28:31]
v_mfma_f32_16x16x32_bf16 v[32:35], v[240:243], v[184:187], v[32:35]
buffer_load_dwordx4 v[184:187], v1, s[52:55], s0 offen offset:2048 sc1
v_mfma_f32_16x16x32_bf16 v[32:35], v[244:247], v[188:191], v[32:35]
s_waitcnt vmcnt(16)
ds_read_b128 v[240:243], v127 offset:49152
v_mfma_f32_16x16x32_bf16 v[16:19], a[160:163], v[192:195], v[16:19]
s_waitcnt vmcnt(15)
ds_read_b128 v[244:247], v127 offset:50176
v_mfma_f32_16x16x32_bf16 v[16:19], a[176:179], v[196:199], v[16:19]
v_mfma_f32_16x16x32_bf16 v[20:23], a[164:167], v[192:195], v[20:23]
buffer_load_dwordx4 v[188:191], v1, s[52:55], s0 offen offset:3072 sc1
v_mfma_f32_16x16x32_bf16 v[20:23], a[180:183], v[196:199], v[20:23]
s_add_u32 s0, s69, 12288
s_add_u32 s69, s69, s2
v_mfma_f32_16x16x32_bf16 v[24:27], a[168:171], v[192:195], v[24:27]
v_mfma_f32_16x16x32_bf16 v[24:27], a[184:187], v[196:199], v[24:27]
v_mfma_f32_16x16x32_bf16 v[28:31], a[172:175], v[192:195], v[28:31]
v_mfma_f32_16x16x32_bf16 v[28:31], a[188:191], v[196:199], v[28:31]
s_waitcnt lgkmcnt(0)
v_mfma_f32_16x16x32_bf16 v[32:35], v[248:251], v[192:195], v[32:35]
buffer_load_dwordx4 v[192:195], v1, s[52:55], s0 offen offset:0 sc1
v_mfma_f32_16x16x32_bf16 v[32:35], v[252:255], v[196:199], v[32:35]
s_waitcnt vmcnt(16)
ds_read_b128 v[248:251], v127 offset:51200
)ASM"
R"ASM(
v_mfma_f32_16x16x32_bf16 v[16:19], a[64:67], v[200:203], v[16:19]
s_waitcnt vmcnt(15)
ds_read_b128 v[252:255], v127 offset:52224
v_mfma_f32_16x16x32_bf16 v[16:19], a[80:83], v[204:207], v[16:19]
v_mfma_f32_16x16x32_bf16 v[20:23], a[68:71], v[200:203], v[20:23]
buffer_load_dwordx4 v[196:199], v1, s[52:55], s0 offen offset:1024 sc1
v_mfma_f32_16x16x32_bf16 v[20:23], a[84:87], v[204:207], v[20:23]
v_mfma_f32_16x16x32_bf16 v[24:27], a[72:75], v[200:203], v[24:27]
v_mfma_f32_16x16x32_bf16 v[24:27], a[88:91], v[204:207], v[24:27]
v_mfma_f32_16x16x32_bf16 v[28:31], a[76:79], v[200:203], v[28:31]
v_mfma_f32_16x16x32_bf16 v[28:31], a[92:95], v[204:207], v[28:31]
v_mfma_f32_16x16x32_bf16 v[32:35], v[240:243], v[200:203], v[32:35]
buffer_load_dwordx4 v[200:203], v1, s[52:55], s0 offen offset:2048 sc1
v_mfma_f32_16x16x32_bf16 v[32:35], v[244:247], v[204:207], v[32:35]
s_waitcnt vmcnt(16)
ds_read_b128 v[240:243], v127 offset:0
v_mfma_f32_16x16x32_bf16 v[16:19], a[96:99], v[208:211], v[16:19]
s_waitcnt vmcnt(15)
ds_read_b128 v[244:247], v127 offset:1024
v_mfma_f32_16x16x32_bf16 v[16:19], a[112:115], v[212:215], v[16:19]
v_mfma_f32_16x16x32_bf16 v[20:23], a[100:103], v[208:211], v[20:23]
buffer_load_dwordx4 v[204:207], v1, s[52:55], s0 offen offset:3072 sc1
v_mfma_f32_16x16x32_bf16 v[20:23], a[116:119], v[212:215], v[20:23]
s_add_u32 s0, s66, 0
v_mfma_f32_16x16x32_bf16 v[24:27], a[104:107], v[208:211], v[24:27]
v_mfma_f32_16x16x32_bf16 v[24:27], a[120:123], v[212:215], v[24:27]
v_mfma_f32_16x16x32_bf16 v[28:31], a[108:111], v[208:211], v[28:31]
v_mfma_f32_16x16x32_bf16 v[28:31], a[124:127], v[212:215], v[28:31]
s_waitcnt lgkmcnt(0)
v_mfma_f32_16x16x32_bf16 v[32:35], v[248:251], v[208:211], v[32:35]
buffer_load_dwordx4 v[208:211], v1, s[52:55], s0 offen offset:0 sc1
v_mfma_f32_16x16x32_bf16 v[32:35], v[252:255], v[212:215], v[32:35]
s_waitcnt vmcnt(16)
ds_write_b128 v8, v[16:19] offset:0
v_mfma_f32_16x16x32_bf16 v[36:39], a[0:3], v[216:219], 0
s_waitcnt vmcnt(15)
ds_read_b128 v[248:251], v127 offset:2048
v_mfma_f32_16x16x32_bf16 v[36:39], a[16:19], v[220:223], v[36:39]
ds_write_b128 v8, v[20:23] offset:1024
v_mfma_f32_16x16x32_bf16 v[40:43], a[4:7], v[216:219], 0
buffer_load_dwordx4 v[212:215], v1, s[52:55], s0 offen offset:1024 sc1
v_mfma_f32_16x16x32_bf16 v[40:43], a[20:23], v[220:223], v[40:43]
ds_write_b128 v8, v[24:27] offset:2048
v_mfma_f32_16x16x32_bf16 v[44:47], a[8:11], v[216:219], 0
ds_read_b128 v[252:255], v127 offset:3072
v_mfma_f32_16x16x32_bf16 v[44:47], a[24:27], v[220:223], v[44:47]
ds_write_b128 v8, v[28:31] offset:3072
v_mfma_f32_16x16x32_bf16 v[48:51], a[12:15], v[216:219], 0
v_mfma_f32_16x16x32_bf16 v[48:51], a[28:31], v[220:223], v[48:51]
ds_write_b128 v8, v[32:35] offset:4096
v_mfma_f32_16x16x32_bf16 v[52:55], v[240:243], v[216:219], 0
buffer_load_dwordx4 v[216:219], v1, s[52:55], s0 offen offset:2048 sc1
v_mfma_f32_16x16x32_bf16 v[52:55], v[244:247], v[220:223], v[52:55]
s_waitcnt vmcnt(16)
ds_read_b128 v[240:243], v127 offset:16384
v_mfma_f32_16x16x32_bf16 v[36:39], a[32:35], v[224:227], v[36:39]
s_waitcnt vmcnt(15)
ds_read_b128 v[244:247], v127 offset:17408
v_mfma_f32_16x16x32_bf16 v[36:39], a[48:51], v[228:231], v[36:39]
v_mfma_f32_16x16x32_bf16 v[40:43], a[36:39], v[224:227], v[40:43]
buffer_load_dwordx4 v[220:223], v1, s[52:55], s0 offen offset:3072 sc1
v_mfma_f32_16x16x32_bf16 v[40:43], a[52:55], v[228:231], v[40:43]
s_add_u32 s0, s66, 4096
v_mfma_f32_16x16x32_bf16 v[44:47], a[40:43], v[224:227], v[44:47]
v_mfma_f32_16x16x32_bf16 v[44:47], a[56:59], v[228:231], v[44:47]
v_mfma_f32_16x16x32_bf16 v[48:51], a[44:47], v[224:227], v[48:51]
v_mfma_f32_16x16x32_bf16 v[48:51], a[60:63], v[228:231], v[48:51]
s_waitcnt lgkmcnt(0)
v_mfma_f32_16x16x32_bf16 v[52:55], v[248:251], v[224:227], v[52:55]
buffer_load_dwordx4 v[224:227], v1, s[52:55], s0 offen offset:0 sc1
v_mfma_f32_16x16x32_bf16 v[52:55], v[252:255], v[228:231], v[52:55]
s_waitcnt vmcnt(16)
ds_read_b128 v[248:251], v127 offset:18432
v_mfma_f32_16x16x32_bf16 v[36:39], a[192:195], v[232:235], v[36:39]
s_waitcnt vmcnt(15)
ds_read_b128 v[252:255], v127 offset:19456
v_mfma_f32_16x16x32_bf16 v[36:39], a[208:211], v[236:239], v[36:39]
s_waitcnt lgkmcnt(0)
v_mfma_f32_16x16x32_bf16 v[40:43], a[196:199], v[232:235], v[40:43]
s_barrier
ds_read_b128 v[56:59], v9 offset:0
v_mfma_f32_16x16x32_bf16 v[40:43], a[212:215], v[236:239], v[40:43]
buffer_load_dwordx4 v[228:231], v1, s[52:55], s0 offen offset:1024 sc1
v_mfma_f32_16x16x32_bf16 v[44:47], a[200:203], v[232:235], v[44:47]
ds_read_b128 v[60:63], v9 offset:1024
v_mfma_f32_16x16x32_bf16 v[44:47], a[216:219], v[236:239], v[44:47]
ds_read_b128 v[64:67], v9 offset:2048
v_mfma_f32_16x16x32_bf16 v[48:51], a[204:207], v[232:235], v[48:51]
ds_read_b128 v[68:71], v9 offset:3072
v_mfma_f32_16x16x32_bf16 v[48:51], a[220:223], v[236:239], v[48:51]
ds_read_b128 v[72:75], v9 offset:4096
v_mfma_f32_16x16x32_bf16 v[52:55], v[240:243], v[232:235], v[52:55]
buffer_load_dwordx4 v[232:235], v1, s[52:55], s0 offen offset:2048 sc1
v_mfma_f32_16x16x32_bf16 v[52:55], v[244:247], v[236:239], v[52:55]
s_waitcnt vmcnt(16)
ds_read_b128 v[76:79], v9 offset:5120
v_mfma_f32_16x16x32_bf16 v[36:39], a[224:227], v[168:171], v[36:39]
s_waitcnt vmcnt(15)
ds_read_b128 v[80:83], v9 offset:6144
v_mfma_f32_16x16x32_bf16 v[36:39], a[240:243], v[172:175], v[36:39]
ds_read_b128 v[84:87], v9 offset:7168
v_mfma_f32_16x16x32_bf16 v[40:43], a[228:231], v[168:171], v[40:43]
buffer_load_dwordx4 v[236:239], v1, s[52:55], s0 offen offset:3072 sc1
v_mfma_f32_16x16x32_bf16 v[40:43], a[244:247], v[172:175], v[40:43]
s_add_u32 s0, s66, 8192
ds_read_b128 v[88:91], v9 offset:8192
v_mfma_f32_16x16x32_bf16 v[44:47], a[232:235], v[168:171], v[44:47]
ds_read_b128 v[240:243], v127 offset:32768
v_mfma_f32_16x16x32_bf16 v[44:47], a[248:251], v[172:175], v[44:47]
ds_read_b128 v[92:95], v9 offset:9216
v_mfma_f32_16x16x32_bf16 v[48:51], a[236:239], v[168:171], v[48:51]
s_waitcnt lgkmcnt(3)
ds_read_b128 v[244:247], v127 offset:33792
v_add_f32 v56, v56, v76
v_mfma_f32_16x16x32_bf16 v[48:51], a[252:255], v[172:175], v[48:51]
v_add_f32 v57, v57, v77
v_add_f32 v58, v58, v78
v_mfma_f32_16x16x32_bf16 v[52:55], v[248:251], v[168:171], v[52:55]
buffer_load_dwordx4 v[168:171], v1, s[52:55], s0 offen offset:0 sc1
v_mfma_f32_16x16x32_bf16 v[52:55], v[252:255], v[172:175], v[52:55]
s_waitcnt vmcnt(16)
v_add_f32 v59, v59, v79
v_add_f32 v60, v60, v80
v_mfma_f32_16x16x32_bf16 v[36:39], a[128:131], v[176:179], v[36:39]
s_waitcnt vmcnt(15)
ds_read_b128 v[76:79], v9 offset:10240
v_add_f32 v61, v61, v81
v_mfma_f32_16x16x32_bf16 v[36:39], a[144:147], v[180:183], v[36:39]
v_add_f32 v62, v62, v82
v_add_f32 v63, v63, v83
v_mfma_f32_16x16x32_bf16 v[40:43], a[132:135], v[176:179], v[40:43]
buffer_load_dwordx4 v[172:175], v1, s[52:55], s0 offen offset:1024 sc1
v_mfma_f32_16x16x32_bf16 v[40:43], a[148:151], v[180:183], v[40:43]
ds_read_b128 v[80:83], v9 offset:11264
v_add_f32 v64, v64, v84
v_mfma_f32_16x16x32_bf16 v[44:47], a[136:139], v[176:179], v[44:47]
v_add_f32 v65, v65, v85
v_add_f32 v66, v66, v86
v_mfma_f32_16x16x32_bf16 v[44:47], a[152:155], v[180:183], v[44:47]
s_waitcnt lgkmcnt(1)
v_add_f32 v67, v67, v87
v_add_f32 v68, v68, v88
v_mfma_f32_16x16x32_bf16 v[48:51], a[140:143], v[176:179], v[48:51]
ds_read_b128 v[84:87], v9 offset:12288
v_add_f32 v69, v69, v89
v_mfma_f32_16x16x32_bf16 v[48:51], a[156:159], v[180:183], v[48:51]
v_add_f32 v70, v70, v90
v_add_f32 v71, v71, v91
v_mfma_f32_16x16x32_bf16 v[52:55], v[240:243], v[176:179], v[52:55]
buffer_load_dwordx4 v[176:179], v1, s[52:55], s0 offen offset:2048 sc1
v_mfma_f32_16x16x32_bf16 v[52:55], v[244:247], v[180:183], v[52:55]
s_waitcnt vmcnt(16)
ds_read_b128 v[88:91], v9 offset:13312
v_add_f32 v72, v72, v92
v_mfma_f32_16x16x32_bf16 v[36:39], a[160:163], v[184:187], v[36:39]
s_waitcnt vmcnt(15)
v_add_f32 v73, v73, v93
v_add_f32 v74, v74, v94
v_mfma_f32_16x16x32_bf16 v[36:39], a[176:179], v[188:191], v[36:39]
ds_read_b128 v[248:251], v127 offset:34816
v_add_f32 v75, v75, v95
v_mfma_f32_16x16x32_bf16 v[40:43], a[164:167], v[184:187], v[40:43]
ds_read_b128 v[252:255], v127 offset:35840
v_add_f32 v56, v56, v76
v_mfma_f32_16x16x32_bf16 v[40:43], a[180:183], v[188:191], v[40:43]
buffer_load_dwordx4 v[180:183], v1, s[52:55], s0 offen offset:3072 sc1
v_mfma_f32_16x16x32_bf16 v[44:47], a[168:171], v[184:187], v[44:47]
s_add_u32 s0, s66, 12288
ds_read_b128 v[92:95], v9 offset:14336
v_add_f32 v57, v57, v77
v_mfma_f32_16x16x32_bf16 v[44:47], a[184:187], v[188:191], v[44:47]
ds_read_b128 v[240:243], v127 offset:49152
v_add_f32 v58, v58, v78
v_mfma_f32_16x16x32_bf16 v[48:51], a[172:175], v[184:187], v[48:51]
ds_read_b128 v[244:247], v127 offset:50176
v_add_f32 v59, v59, v79
v_mfma_f32_16x16x32_bf16 v[48:51], a[188:191], v[188:191], v[48:51]
s_waitcnt lgkmcnt(3)
v_add_f32 v60, v60, v80
v_add_f32 v61, v61, v81
v_mfma_f32_16x16x32_bf16 v[52:55], v[248:251], v[184:187], v[52:55]
buffer_load_dwordx4 v[184:187], v1, s[52:55], s0 offen offset:0 sc1
v_mfma_f32_16x16x32_bf16 v[52:55], v[252:255], v[188:191], v[52:55]
s_waitcnt vmcnt(16)
ds_read_b128 v[248:251], v127 offset:51200
v_add_f32 v62, v62, v82
v_mfma_f32_16x16x32_bf16 v[36:39], a[64:67], v[192:195], v[36:39]
s_waitcnt vmcnt(15)
ds_read_b128 v[252:255], v127 offset:52224
v_add_f32 v63, v63, v83
v_mfma_f32_16x16x32_bf16 v[36:39], a[80:83], v[196:199], v[36:39]
v_add_f32 v64, v64, v84
v_add_f32 v65, v65, v85
v_mfma_f32_16x16x32_bf16 v[40:43], a[68:71], v[192:195], v[40:43]
buffer_load_dwordx4 v[188:191], v1, s[52:55], s0 offen offset:1024 sc1
v_mfma_f32_16x16x32_bf16 v[40:43], a[84:87], v[196:199], v[40:43]
v_add_f32 v66, v66, v86
v_add_f32 v67, v67, v87
v_mfma_f32_16x16x32_bf16 v[44:47], a[72:75], v[192:195], v[44:47]
v_add_f32 v68, v68, v88
v_add_f32 v69, v69, v89
v_mfma_f32_16x16x32_bf16 v[44:47], a[88:91], v[196:199], v[44:47]
v_add_f32 v70, v70, v90
v_add_f32 v71, v71, v91
v_mfma_f32_16x16x32_bf16 v[48:51], a[76:79], v[192:195], v[48:51]
s_waitcnt lgkmcnt(0)
v_add_f32 v72, v72, v92
v_add_f32 v73, v73, v93
v_mfma_f32_16x16x32_bf16 v[48:51], a[92:95], v[196:199], v[48:51]
v_add_f32 v74, v74, v94
v_add_f32 v75, v75, v95
v_mfma_f32_16x16x32_bf16 v[52:55], v[240:243], v[192:195], v[52:55]
buffer_load_dwordx4 v[192:195], v1, s[52:55], s0 offen offset:2048 sc1
v_mfma_f32_16x16x32_bf16 v[52:55], v[244:247], v[196:199], v[52:55]
s_waitcnt vmcnt(16)
ds_read_b128 v[240:243], v127 offset:0
v_mfma_f32_16x16x32_bf16 v[36:39], a[96:99], v[200:203], v[36:39]
s_waitcnt vmcnt(15)
ds_read_b128 v[244:247], v127 offset:1024
v_mfma_f32_16x16x32_bf16 v[36:39], a[112:115], v[204:207], v[36:39]
v_mfma_f32_16x16x32_bf16 v[40:43], a[100:103], v[200:203], v[40:43]
buffer_load_dwordx4 v[196:199], v1, s[52:55], s0 offen offset:3072 sc1
v_mfma_f32_16x16x32_bf16 v[40:43], a[116:119], v[204:207], v[40:43]
s_add_u32 s0, s67, 0
v_mfma_f32_16x16x32_bf16 v[44:47], a[104:107], v[200:203], v[44:47]
v_mfma_f32_16x16x32_bf16 v[44:47], a[120:123], v[204:207], v[44:47]
v_mfma_f32_16x16x32_bf16 v[48:51], a[108:111], v[200:203], v[48:51]
v_mfma_f32_16x16x32_bf16 v[48:51], a[124:127], v[204:207], v[48:51]
v_mfma_f32_16x16x32_bf16 v[52:55], v[248:251], v[200:203], v[52:55]
buffer_load_dwordx4 v[200:203], v1, s[52:55], s0 offen offset:0 sc1
v_mfma_f32_16x16x32_bf16 v[52:55], v[252:255], v[204:207], v[52:55]
s_waitcnt vmcnt(16)
v_add_f32 v56, v56, v36
v_add_f32 v57, v57, v37
v_mfma_f32_16x16x32_bf16 v[16:19], a[0:3], v[208:211], 0
s_waitcnt vmcnt(15)
v_add_f32 v58, v58, v38
v_add_f32 v59, v59, v39
v_mfma_f32_16x16x32_bf16 v[16:19], a[16:19], v[212:215], v[16:19]
ds_read_b128 v[248:251], v127 offset:2048
v_add_f32 v60, v60, v40
v_mfma_f32_16x16x32_bf16 v[20:23], a[4:7], v[208:211], 0
buffer_store_dwordx4 v[56:59], v1, s[52:55], s1 offen offset:0 sc1
v_mfma_f32_16x16x32_bf16 v[20:23], a[20:23], v[212:215], v[20:23]
ds_read_b128 v[252:255], v127 offset:3072
v_add_f32 v61, v61, v41
v_mfma_f32_16x16x32_bf16 v[24:27], a[8:11], v[208:211], 0
v_add_f32 v62, v62, v42
v_add_f32 v63, v63, v43
v_mfma_f32_16x16x32_bf16 v[24:27], a[24:27], v[212:215], v[24:27]
v_add_f32 v64, v64, v44
v_add_f32 v65, v65, v45
v_mfma_f32_16x16x32_bf16 v[28:31], a[12:15], v[208:211], 0
v_add_f32 v66, v66, v46
v_add_f32 v67, v67, v47
v_mfma_f32_16x16x32_bf16 v[28:31], a[28:31], v[212:215], v[28:31]
s_waitcnt lgkmcnt(1)
v_add_f32 v68, v68, v48
v_add_f32 v69, v69, v49
v_mfma_f32_16x16x32_bf16 v[32:35], v[240:243], v[208:211], 0
buffer_load_dwordx4 v[204:207], v1, s[52:55], s0 offen offset:1024 sc1
v_mfma_f32_16x16x32_bf16 v[32:35], v[244:247], v[212:215], v[32:35]
s_waitcnt vmcnt(16)
ds_read_b128 v[240:243], v127 offset:16384
v_add_f32 v70, v70, v50
v_mfma_f32_16x16x32_bf16 v[16:19], a[32:35], v[216:219], v[16:19]
s_waitcnt vmcnt(15)
ds_read_b128 v[244:247], v127 offset:17408
v_add_f32 v71, v71, v51
v_mfma_f32_16x16x32_bf16 v[16:19], a[48:51], v[220:223], v[16:19]
v_add_f32 v72, v72, v52
v_add_f32 v73, v73, v53
v_mfma_f32_16x16x32_bf16 v[20:23], a[36:39], v[216:219], v[20:23]
buffer_load_dwordx4 v[208:211], v1, s[52:55], s0 offen offset:2048 sc1
v_mfma_f32_16x16x32_bf16 v[20:23], a[52:55], v[220:223], v[20:23]
v_add_f32 v74, v74, v54
v_add_f32 v75, v75, v55
v_mfma_f32_16x16x32_bf16 v[24:27], a[40:43], v[216:219], v[24:27]
v_mfma_f32_16x16x32_bf16 v[24:27], a[56:59], v[220:223], v[24:27]
v_mfma_f32_16x16x32_bf16 v[28:31], a[44:47], v[216:219], v[28:31]
buffer_store_dwordx4 v[60:63], v1, s[52:55], s1 offen offset:1024 sc1
v_mfma_f32_16x16x32_bf16 v[28:31], a[60:63], v[220:223], v[28:31]
v_mfma_f32_16x16x32_bf16 v[32:35], v[248:251], v[216:219], v[32:35]
s_waitcnt lgkmcnt(0)
ds_read_b128 v[248:251], v127 offset:18432
v_mfma_f32_16x16x32_bf16 v[32:35], v[252:255], v[220:223], v[32:35]
s_waitcnt vmcnt(16)
ds_read_b128 v[252:255], v127 offset:19456
v_mfma_f32_16x16x32_bf16 v[16:19], a[192:195], v[224:227], v[16:19]
s_waitcnt vmcnt(15)
v_mfma_f32_16x16x32_bf16 v[16:19], a[208:211], v[228:231], v[16:19]
v_mfma_f32_16x16x32_bf16 v[20:23], a[196:199], v[224:227], v[20:23]
buffer_load_dwordx4 v[212:215], v1, s[52:55], s0 offen offset:3072 sc1
v_mfma_f32_16x16x32_bf16 v[20:23], a[212:215], v[228:231], v[20:23]
s_add_u32 s0, s67, 4096
v_mfma_f32_16x16x32_bf16 v[24:27], a[200:203], v[224:227], v[24:27]
v_mfma_f32_16x16x32_bf16 v[24:27], a[216:219], v[228:231], v[24:27]
v_mfma_f32_16x16x32_bf16 v[28:31], a[204:207], v[224:227], v[28:31]
buffer_store_dwordx4 v[64:67], v1, s[52:55], s1 offen offset:2048 sc1
v_mfma_f32_16x16x32_bf16 v[28:31], a[220:223], v[228:231], v[28:31]
v_mfma_f32_16x16x32_bf16 v[32:35], v[240:243], v[224:227], v[32:35]
ds_read_b128 v[240:243], v127 offset:32768
v_mfma_f32_16x16x32_bf16 v[32:35], v[244:247], v[228:231], v[32:35]
s_waitcnt vmcnt(16)
ds_read_b128 v[244:247], v127 offset:33792
v_mfma_f32_16x16x32_bf16 v[16:19], a[224:227], v[232:235], v[16:19]
s_waitcnt lgkmcnt(0)
s_barrier
s_waitcnt vmcnt(15)
v_mfma_f32_16x16x32_bf16 v[16:19], a[240:243], v[236:239], v[16:19]
v_mfma_f32_16x16x32_bf16 v[20:23], a[228:231], v[232:235], v[20:23]
buffer_load_dwordx4 v[216:219], v1, s[52:55], s0 offen offset:0 sc1
v_mfma_f32_16x16x32_bf16 v[20:23], a[244:247], v[236:239], v[20:23]
v_mfma_f32_16x16x32_bf16 v[24:27], a[232:235], v[232:235], v[24:27]
v_mfma_f32_16x16x32_bf16 v[24:27], a[248:251], v[236:239], v[24:27]
v_mfma_f32_16x16x32_bf16 v[28:31], a[236:239], v[232:235], v[28:31]
buffer_load_dwordx4 v[220:223], v1, s[52:55], s0 offen offset:1024 sc1
v_mfma_f32_16x16x32_bf16 v[28:31], a[252:255], v[236:239], v[28:31]
v_mfma_f32_16x16x32_bf16 v[32:35], v[248:251], v[232:235], v[32:35]
ds_read_b128 v[248:251], v127 offset:34816
v_mfma_f32_16x16x32_bf16 v[32:35], v[252:255], v[236:239], v[32:35]
s_waitcnt vmcnt(15)
ds_read_b128 v[252:255], v127 offset:35840
v_mfma_f32_16x16x32_bf16 v[16:19], a[128:131], v[168:171], v[16:19]
buffer_store_dwordx4 v[68:71], v1, s[52:55], s1 offen offset:3072 sc1
v_mfma_f32_16x16x32_bf16 v[16:19], a[144:147], v[172:175], v[16:19]
s_add_u32 s1, s1, 4096
v_mfma_f32_16x16x32_bf16 v[20:23], a[132:135], v[168:171], v[20:23]
v_mfma_f32_16x16x32_bf16 v[20:23], a[148:151], v[172:175], v[20:23]
v_mfma_f32_16x16x32_bf16 v[24:27], a[136:139], v[168:171], v[24:27]
v_mfma_f32_16x16x32_bf16 v[24:27], a[152:155], v[172:175], v[24:27]
v_mfma_f32_16x16x32_bf16 v[28:31], a[140:143], v[168:171], v[28:31]
buffer_load_dwordx4 v[224:227], v1, s[52:55], s0 offen offset:2048 sc1
v_mfma_f32_16x16x32_bf16 v[28:31], a[156:159], v[172:175], v[28:31]
v_mfma_f32_16x16x32_bf16 v[32:35], v[240:243], v[168:171], v[32:35]
ds_read_b128 v[240:243], v127 offset:49152
v_mfma_f32_16x16x32_bf16 v[32:35], v[244:247], v[172:175], v[32:35]
s_waitcnt vmcnt(15)
ds_read_b128 v[244:247], v127 offset:50176
v_mfma_f32_16x16x32_bf16 v[16:19], a[160:163], v[176:179], v[16:19]
buffer_store_dwordx4 v[72:75], v1, s[52:55], s1 offen offset:0 sc1
v_mfma_f32_16x16x32_bf16 v[16:19], a[176:179], v[180:183], v[16:19]
buffer_load_dwordx4 v[228:231], v1, s[52:55], s0 offen offset:3072 sc1
v_mfma_f32_16x16x32_bf16 v[20:23], a[164:167], v[176:179], v[20:23]
s_lshl_b32 s1, s63, 8
s_add_u32 s1, s1, 655360
s_add_u32 s0, s67, 8192
v_mfma_f32_16x16x32_bf16 v[20:23], a[180:183], v[180:183], v[20:23]
v_mfma_f32_16x16x32_bf16 v[24:27], a[168:171], v[176:179], v[24:27]
v_mfma_f32_16x16x32_bf16 v[24:27], a[184:187], v[180:183], v[24:27]
buffer_load_dwordx4 v[232:235], v1, s[52:55], s0 offen offset:0 sc1
v_mfma_f32_16x16x32_bf16 v[28:31], a[172:175], v[176:179], v[28:31]
v_mfma_f32_16x16x32_bf16 v[28:31], a[188:191], v[180:183], v[28:31]
s_waitcnt lgkmcnt(0)
v_mfma_f32_16x16x32_bf16 v[32:35], v[248:251], v[176:179], v[32:35]
s_waitcnt vmcnt(17)
ds_read_b128 v[248:251], v127 offset:51200
v_mfma_f32_16x16x32_bf16 v[32:35], v[252:255], v[180:183], v[32:35]
buffer_load_dwordx4 v[236:239], v1, s[52:55], s0 offen offset:1024 sc1
v_mfma_f32_16x16x32_bf16 v[16:19], a[64:67], v[184:187], v[16:19]
s_waitcnt vmcnt(17)
ds_read_b128 v[252:255], v127 offset:52224
v_mfma_f32_16x16x32_bf16 v[16:19], a[80:83], v[188:191], v[16:19]
v_mfma_f32_16x16x32_bf16 v[20:23], a[68:71], v[184:187], v[20:23]
v_mfma_f32_16x16x32_bf16 v[20:23], a[84:87], v[188:191], v[20:23]
buffer_load_dwordx4 v[168:171], v1, s[52:55], s0 offen offset:2048 sc1
v_mfma_f32_16x16x32_bf16 v[24:27], a[72:75], v[184:187], v[24:27]
v_mfma_f32_16x16x32_bf16 v[24:27], a[88:91], v[188:191], v[24:27]
v_mfma_f32_16x16x32_bf16 v[28:31], a[76:79], v[184:187], v[28:31]
v_mfma_f32_16x16x32_bf16 v[28:31], a[92:95], v[188:191], v[28:31]
buffer_load_dwordx4 v[172:175], v1, s[52:55], s0 offen offset:3072 sc1
v_mfma_f32_16x16x32_bf16 v[32:35], v[240:243], v[184:187], v[32:35]
s_add_u32 s0, s67, 12288
ds_read_b128 v[240:243], v127 offset:0
v_mfma_f32_16x16x32_bf16 v[32:35], v[244:247], v[188:191], v[32:35]
s_waitcnt vmcnt(18)
ds_read_b128 v[244:247], v127 offset:1024
v_mfma_f32_16x16x32_bf16 v[16:19], a[96:99], v[192:195], v[16:19]
s_waitcnt vmcnt(17)
v_mfma_f32_16x16x32_bf16 v[16:19], a[112:115], v[196:199], v[16:19]
buffer_load_dwordx4 v[176:179], v1, s[52:55], s0 offen offset:0 sc1
v_mfma_f32_16x16x32_bf16 v[20:23], a[100:103], v[192:195], v[20:23]
v_mfma_f32_16x16x32_bf16 v[20:23], a[116:119], v[196:199], v[20:23]
v_mfma_f32_16x16x32_bf16 v[24:27], a[104:107], v[192:195], v[24:27]
v_mfma_f32_16x16x32_bf16 v[24:27], a[120:123], v[196:199], v[24:27]
buffer_load_dwordx4 v[180:183], v1, s[52:55], s0 offen offset:1024 sc1
v_mfma_f32_16x16x32_bf16 v[28:31], a[108:111], v[192:195], v[28:31]
v_mfma_f32_16x16x32_bf16 v[28:31], a[124:127], v[196:199], v[28:31]
s_waitcnt lgkmcnt(0)
v_mfma_f32_16x16x32_bf16 v[32:35], v[248:251], v[192:195], v[32:35]
s_waitcnt vmcnt(18)
ds_read_b128 v[248:251], v127 offset:2048
v_mfma_f32_16x16x32_bf16 v[32:35], v[252:255], v[196:199], v[32:35]
buffer_load_dwordx4 v[184:187], v1, s[52:55], s0 offen offset:2048 sc1
v_mfma_f32_16x16x32_bf16 v[36:39], a[0:3], v[200:203], 0
s_waitcnt vmcnt(17)
ds_write_b128 v4, v[16:19] offset:0
v_mfma_f32_16x16x32_bf16 v[36:39], a[16:19], v[204:207], v[36:39]
ds_write_b128 v4, v[20:23] offset:1024
v_mfma_f32_16x16x32_bf16 v[40:43], a[4:7], v[200:203], 0
)ASM"
R"ASM(
ds_read_b128 v[252:255], v127 offset:3072
v_mfma_f32_16x16x32_bf16 v[40:43], a[20:23], v[204:207], v[40:43]
buffer_load_dwordx4 v[188:191], v1, s[52:55], s0 offen offset:3072 sc1
v_mfma_f32_16x16x32_bf16 v[44:47], a[8:11], v[200:203], 0
s_add_u32 s0, s68, 0
ds_write_b128 v4, v[24:27] offset:2048
v_mfma_f32_16x16x32_bf16 v[44:47], a[24:27], v[204:207], v[44:47]
ds_write_b128 v4, v[28:31] offset:3072
v_mfma_f32_16x16x32_bf16 v[48:51], a[12:15], v[200:203], 0
v_mfma_f32_16x16x32_bf16 v[48:51], a[28:31], v[204:207], v[48:51]
buffer_load_dwordx4 v[192:195], v1, s[52:55], s0 offen offset:0 sc1
v_mfma_f32_16x16x32_bf16 v[52:55], v[240:243], v[200:203], 0
ds_write_b128 v4, v[32:35] offset:4096
v_mfma_f32_16x16x32_bf16 v[52:55], v[244:247], v[204:207], v[52:55]
s_waitcnt vmcnt(18)
ds_read_b128 v[240:243], v127 offset:16384
v_mfma_f32_16x16x32_bf16 v[36:39], a[32:35], v[208:211], v[36:39]
s_waitcnt vmcnt(16)
ds_read_b128 v[244:247], v127 offset:17408
v_mfma_f32_16x16x32_bf16 v[36:39], a[48:51], v[212:215], v[36:39]
buffer_load_dwordx4 v[196:199], v1, s[52:55], s0 offen offset:1024 sc1
v_mfma_f32_16x16x32_bf16 v[40:43], a[36:39], v[208:211], v[40:43]
v_mfma_f32_16x16x32_bf16 v[40:43], a[52:55], v[212:215], v[40:43]
v_mfma_f32_16x16x32_bf16 v[44:47], a[40:43], v[208:211], v[44:47]
v_mfma_f32_16x16x32_bf16 v[44:47], a[56:59], v[212:215], v[44:47]
buffer_load_dwordx4 v[200:203], v1, s[52:55], s0 offen offset:2048 sc1
v_mfma_f32_16x16x32_bf16 v[48:51], a[44:47], v[208:211], v[48:51]
v_mfma_f32_16x16x32_bf16 v[48:51], a[60:63], v[212:215], v[48:51]
s_waitcnt lgkmcnt(0)
v_mfma_f32_16x16x32_bf16 v[52:55], v[248:251], v[208:211], v[52:55]
s_waitcnt vmcnt(16)
ds_read_b128 v[248:251], v127 offset:18432
v_mfma_f32_16x16x32_bf16 v[52:55], v[252:255], v[212:215], v[52:55]
buffer_load_dwordx4 v[204:207], v1, s[52:55], s0 offen offset:3072 sc1
v_mfma_f32_16x16x32_bf16 v[36:39], a[192:195], v[216:219], v[36:39]
s_waitcnt vmcnt(13) lgkmcnt(0)
s_barrier
s_add_u32 s0, s68, 4096
ds_read_b128 v[252:255], v127 offset:19456
v_mfma_f32_16x16x32_bf16 v[36:39], a[208:211], v[220:223], v[36:39]
v_mfma_f32_16x16x32_bf16 v[40:43], a[196:199], v[216:219], v[40:43]
v_mfma_f32_16x16x32_bf16 v[40:43], a[212:215], v[220:223], v[40:43]
buffer_store_dwordx2 v[10:11], v5, s[52:55], s1 offen sc1
v_mfma_f32_16x16x32_bf16 v[44:47], a[200:203], v[216:219], v[44:47]
s_xor_b32 s1, s1, 256
v_mov_b32 v6, s1
v_mfma_f32_16x16x32_bf16 v[44:47], a[216:219], v[220:223], v[44:47]
s_lshl_b32 s1, s63, 2
s_add_u32 s1, s1, s60
s_mul_i32 s1, s1, 5120
v_mfma_f32_16x16x32_bf16 v[48:51], a[204:207], v[216:219], v[48:51]
s_add_u32 s1, s1, 92274688
v_mfma_f32_16x16x32_bf16 v[48:51], a[220:223], v[220:223], v[48:51]
buffer_load_dwordx4 v[208:211], v1, s[52:55], s0 offen offset:0 sc1
v_mfma_f32_16x16x32_bf16 v[52:55], v[240:243], v[216:219], v[52:55]
ds_read_b128 v[240:243], v127 offset:32768
v_mfma_f32_16x16x32_bf16 v[52:55], v[244:247], v[220:223], v[52:55]
ds_read_b128 v[244:247], v127 offset:33792
v_mfma_f32_16x16x32_bf16 v[36:39], a[224:227], v[224:227], v[36:39]
s_waitcnt vmcnt(14)
v_mfma_f32_16x16x32_bf16 v[36:39], a[240:243], v[228:231], v[36:39]
buffer_load_dwordx4 v[212:215], v1, s[52:55], s0 offen offset:1024 sc1
v_mfma_f32_16x16x32_bf16 v[40:43], a[228:231], v[224:227], v[40:43]
v_mfma_f32_16x16x32_bf16 v[40:43], a[244:247], v[228:231], v[40:43]
v_mfma_f32_16x16x32_bf16 v[44:47], a[232:235], v[224:227], v[44:47]
v_mfma_f32_16x16x32_bf16 v[44:47], a[248:251], v[228:231], v[44:47]
buffer_load_dwordx4 v[216:219], v1, s[52:55], s0 offen offset:2048 sc1
v_mfma_f32_16x16x32_bf16 v[48:51], a[236:239], v[224:227], v[48:51]
v_mfma_f32_16x16x32_bf16 v[48:51], a[252:255], v[228:231], v[48:51]
v_mfma_f32_16x16x32_bf16 v[52:55], v[248:251], v[224:227], v[52:55]
s_waitcnt vmcnt(15) lgkmcnt(0)
ds_read_b128 v[248:251], v127 offset:34816
v_mfma_f32_16x16x32_bf16 v[52:55], v[252:255], v[228:231], v[52:55]
buffer_load_dwordx4 v[220:223], v1, s[52:55], s0 offen offset:3072 sc1
v_mfma_f32_16x16x32_bf16 v[36:39], a[128:131], v[232:235], v[36:39]
s_waitcnt vmcnt(15)
s_add_u32 s0, s68, 8192
ds_read_b128 v[252:255], v127 offset:35840
v_mfma_f32_16x16x32_bf16 v[36:39], a[144:147], v[236:239], v[36:39]
v_mfma_f32_16x16x32_bf16 v[40:43], a[132:135], v[232:235], v[40:43]
v_mfma_f32_16x16x32_bf16 v[40:43], a[148:151], v[236:239], v[40:43]
buffer_load_dwordx4 v[224:227], v1, s[52:55], s0 offen offset:0 sc1
v_mfma_f32_16x16x32_bf16 v[44:47], a[136:139], v[232:235], v[44:47]
v_mfma_f32_16x16x32_bf16 v[44:47], a[152:155], v[236:239], v[44:47]
v_mfma_f32_16x16x32_bf16 v[48:51], a[140:143], v[232:235], v[48:51]
v_mfma_f32_16x16x32_bf16 v[48:51], a[156:159], v[236:239], v[48:51]
buffer_load_dwordx4 v[228:231], v1, s[52:55], s0 offen offset:1024 sc1
v_mfma_f32_16x16x32_bf16 v[52:55], v[240:243], v[232:235], v[52:55]
ds_read_b128 v[240:243], v127 offset:49152
v_mfma_f32_16x16x32_bf16 v[52:55], v[244:247], v[236:239], v[52:55]
s_waitcnt vmcnt(16)
ds_read_b128 v[244:247], v127 offset:50176
v_mfma_f32_16x16x32_bf16 v[36:39], a[160:163], v[168:171], v[36:39]
s_waitcnt vmcnt(15)
v_mfma_f32_16x16x32_bf16 v[36:39], a[176:179], v[172:175], v[36:39]
buffer_load_dwordx4 v[232:235], v1, s[52:55], s0 offen offset:2048 sc1
v_mfma_f32_16x16x32_bf16 v[40:43], a[164:167], v[168:171], v[40:43]
v_mfma_f32_16x16x32_bf16 v[40:43], a[180:183], v[172:175], v[40:43]
v_mfma_f32_16x16x32_bf16 v[44:47], a[168:171], v[168:171], v[44:47]
v_mfma_f32_16x16x32_bf16 v[44:47], a[184:187], v[172:175], v[44:47]
buffer_load_dwordx4 v[236:239], v1, s[52:55], s0 offen offset:3072 sc1
v_mfma_f32_16x16x32_bf16 v[48:51], a[172:175], v[168:171], v[48:51]
s_add_u32 s0, s68, 12288
v_mfma_f32_16x16x32_bf16 v[48:51], a[188:191], v[172:175], v[48:51]
s_waitcnt lgkmcnt(0)
v_mfma_f32_16x16x32_bf16 v[52:55], v[248:251], v[168:171], v[52:55]
s_waitcnt vmcnt(16)
ds_read_b128 v[248:251], v127 offset:51200
v_mfma_f32_16x16x32_bf16 v[52:55], v[252:255], v[172:175], v[52:55]
buffer_load_dwordx4 v[168:171], v1, s[52:55], s0 offen offset:0 sc1
v_mfma_f32_16x16x32_bf16 v[36:39], a[64:67], v[176:179], v[36:39]
s_waitcnt vmcnt(16)
ds_read_b128 v[252:255], v127 offset:52224
v_mfma_f32_16x16x32_bf16 v[36:39], a[80:83], v[180:183], v[36:39]
v_mfma_f32_16x16x32_bf16 v[40:43], a[68:71], v[176:179], v[40:43]
v_mfma_f32_16x16x32_bf16 v[40:43], a[84:87], v[180:183], v[40:43]
buffer_load_dwordx4 v[172:175], v1, s[52:55], s0 offen offset:1024 sc1
v_mfma_f32_16x16x32_bf16 v[44:47], a[72:75], v[176:179], v[44:47]
v_mfma_f32_16x16x32_bf16 v[44:47], a[88:91], v[180:183], v[44:47]
buffer_load_dwordx2 v[10:11], v6, s[52:55], 0 offen sc1
v_mfma_f32_16x16x32_bf16 v[48:51], a[76:79], v[176:179], v[48:51]
v_mfma_f32_16x16x32_bf16 v[48:51], a[92:95], v[180:183], v[48:51]
v_mfma_f32_16x16x32_bf16 v[52:55], v[240:243], v[176:179], v[52:55]
s_waitcnt vmcnt(17)
ds_read_b128 v[240:243], v127 offset:0
v_mfma_f32_16x16x32_bf16 v[52:55], v[244:247], v[180:183], v[52:55]
buffer_load_dwordx4 v[176:179], v1, s[52:55], s0 offen offset:2048 sc1
v_mfma_f32_16x16x32_bf16 v[36:39], a[96:99], v[184:187], v[36:39]
s_waitcnt vmcnt(17)
ds_read_b128 v[244:247], v127 offset:1024
v_mfma_f32_16x16x32_bf16 v[36:39], a[112:115], v[188:191], v[36:39]
v_mfma_f32_16x16x32_bf16 v[40:43], a[100:103], v[184:187], v[40:43]
v_mfma_f32_16x16x32_bf16 v[40:43], a[116:119], v[188:191], v[40:43]
buffer_load_dwordx4 v[180:183], v1, s[52:55], s0 offen offset:3072 sc1
v_mfma_f32_16x16x32_bf16 v[44:47], a[104:107], v[184:187], v[44:47]
s_add_u32 s0, s69, 0
v_mfma_f32_16x16x32_bf16 v[44:47], a[120:123], v[188:191], v[44:47]
v_mfma_f32_16x16x32_bf16 v[48:51], a[108:111], v[184:187], v[48:51]
v_mfma_f32_16x16x32_bf16 v[48:51], a[124:127], v[188:191], v[48:51]
s_waitcnt lgkmcnt(0)
v_mfma_f32_16x16x32_bf16 v[52:55], v[248:251], v[184:187], v[52:55]
buffer_load_dwordx4 v[184:187], v1, s[52:55], s0 offen offset:0 sc1
v_mfma_f32_16x16x32_bf16 v[52:55], v[252:255], v[188:191], v[52:55]
s_waitcnt vmcnt(18)
ds_write_b128 v7, v[36:39] offset:0
v_mfma_f32_16x16x32_bf16 v[16:19], a[0:3], v[192:195], 0
s_waitcnt vmcnt(17)
ds_read_b128 v[248:251], v127 offset:2048
v_mfma_f32_16x16x32_bf16 v[16:19], a[16:19], v[196:199], v[16:19]
L_pad_b_1:
s_cbranch_execz L_padret_b
ds_write_b128 v7, v[40:43] offset:1024
v_mfma_f32_16x16x32_bf16 v[20:23], a[4:7], v[192:195], 0
buffer_load_dwordx4 v[188:191], v1, s[52:55], s0 offen offset:1024 sc1
v_mfma_f32_16x16x32_bf16 v[20:23], a[20:23], v[196:199], v[20:23]
ds_write_b128 v7, v[44:47] offset:2048
v_mfma_f32_16x16x32_bf16 v[24:27], a[8:11], v[192:195], 0
ds_read_b128 v[252:255], v127 offset:3072
v_mfma_f32_16x16x32_bf16 v[24:27], a[24:27], v[196:199], v[24:27]
ds_write_b128 v7, v[48:51] offset:3072
v_mfma_f32_16x16x32_bf16 v[28:31], a[12:15], v[192:195], 0
v_mfma_f32_16x16x32_bf16 v[28:31], a[28:31], v[196:199], v[28:31]
ds_write_b128 v7, v[52:55] offset:4096
v_mfma_f32_16x16x32_bf16 v[32:35], v[240:243], v[192:195], 0
buffer_load_dwordx4 v[192:195], v1, s[52:55], s0 offen offset:2048 sc1
v_mfma_f32_16x16x32_bf16 v[32:35], v[244:247], v[196:199], v[32:35]
s_waitcnt vmcnt(18)
ds_read_b128 v[240:243], v127 offset:16384
v_mfma_f32_16x16x32_bf16 v[16:19], a[32:35], v[200:203], v[16:19]
s_waitcnt vmcnt(17)
ds_read_b128 v[244:247], v127 offset:17408
v_mfma_f32_16x16x32_bf16 v[16:19], a[48:51], v[204:207], v[16:19]
v_mfma_f32_16x16x32_bf16 v[20:23], a[36:39], v[200:203], v[20:23]
buffer_load_dwordx4 v[196:199], v1, s[52:55], s0 offen offset:3072 sc1
v_mfma_f32_16x16x32_bf16 v[20:23], a[52:55], v[204:207], v[20:23]
s_add_u32 s0, s69, 4096
v_mfma_f32_16x16x32_bf16 v[24:27], a[40:43], v[200:203], v[24:27]
v_mfma_f32_16x16x32_bf16 v[24:27], a[56:59], v[204:207], v[24:27]
v_mfma_f32_16x16x32_bf16 v[28:31], a[44:47], v[200:203], v[28:31]
v_mfma_f32_16x16x32_bf16 v[28:31], a[60:63], v[204:207], v[28:31]
s_waitcnt lgkmcnt(0)
v_mfma_f32_16x16x32_bf16 v[32:35], v[248:251], v[200:203], v[32:35]
buffer_load_dwordx4 v[200:203], v1, s[52:55], s0 offen offset:0 sc1
v_mfma_f32_16x16x32_bf16 v[32:35], v[252:255], v[204:207], v[32:35]
s_waitcnt vmcnt(17)
ds_read_b128 v[248:251], v127 offset:18432
v_mfma_f32_16x16x32_bf16 v[16:19], a[192:195], v[208:211], v[16:19]
s_waitcnt vmcnt(16)
ds_read_b128 v[252:255], v127 offset:19456
v_mfma_f32_16x16x32_bf16 v[16:19], a[208:211], v[212:215], v[16:19]
v_mfma_f32_16x16x32_bf16 v[20:23], a[196:199], v[208:211], v[20:23]
buffer_load_dwordx4 v[204:207], v1, s[52:55], s0 offen offset:1024 sc1
v_mfma_f32_16x16x32_bf16 v[20:23], a[212:215], v[212:215], v[20:23]
v_mfma_f32_16x16x32_bf16 v[24:27], a[200:203], v[208:211], v[24:27]
v_mfma_f32_16x16x32_bf16 v[24:27], a[216:219], v[212:215], v[24:27]
v_mfma_f32_16x16x32_bf16 v[28:31], a[204:207], v[208:211], v[28:31]
v_mfma_f32_16x16x32_bf16 v[28:31], a[220:223], v[212:215], v[28:31]
v_mfma_f32_16x16x32_bf16 v[32:35], v[240:243], v[208:211], v[32:35]
buffer_load_dwordx4 v[208:211], v1, s[52:55], s0 offen offset:2048 sc1
v_mfma_f32_16x16x32_bf16 v[32:35], v[244:247], v[212:215], v[32:35]
s_waitcnt vmcnt(17)
ds_read_b128 v[240:243], v127 offset:32768
v_mfma_f32_16x16x32_bf16 v[16:19], a[224:227], v[216:219], v[16:19]
s_waitcnt vmcnt(16)
ds_read_b128 v[244:247], v127 offset:33792
v_mfma_f32_16x16x32_bf16 v[16:19], a[240:243], v[220:223], v[16:19]
v_mfma_f32_16x16x32_bf16 v[20:23], a[228:231], v[216:219], v[20:23]
buffer_load_dwordx4 v[212:215], v1, s[52:55], s0 offen offset:3072 sc1
v_mfma_f32_16x16x32_bf16 v[20:23], a[244:247], v[220:223], v[20:23]
s_add_u32 s0, s69, 8192
v_mfma_f32_16x16x32_bf16 v[24:27], a[232:235], v[216:219], v[24:27]
v_mfma_f32_16x16x32_bf16 v[24:27], a[248:251], v[220:223], v[24:27]
v_mfma_f32_16x16x32_bf16 v[28:31], a[236:239], v[216:219], v[28:31]
v_mfma_f32_16x16x32_bf16 v[28:31], a[252:255], v[220:223], v[28:31]
s_waitcnt lgkmcnt(0)
v_mfma_f32_16x16x32_bf16 v[32:35], v[248:251], v[216:219], v[32:35]
buffer_load_dwordx4 v[216:219], v1, s[52:55], s0 offen offset:0 sc1
v_mfma_f32_16x16x32_bf16 v[32:35], v[252:255], v[220:223], v[32:35]
s_waitcnt vmcnt(17)
ds_read_b128 v[248:251], v127 offset:34816
v_mfma_f32_16x16x32_bf16 v[16:19], a[128:131], v[224:227], v[16:19]
s_waitcnt vmcnt(16)
ds_read_b128 v[252:255], v127 offset:35840
v_mfma_f32_16x16x32_bf16 v[16:19], a[144:147], v[228:231], v[16:19]
v_mfma_f32_16x16x32_bf16 v[20:23], a[132:135], v[224:227], v[20:23]
buffer_load_dwordx4 v[220:223], v1, s[52:55], s0 offen offset:1024 sc1
v_mfma_f32_16x16x32_bf16 v[20:23], a[148:151], v[228:231], v[20:23]
v_mfma_f32_16x16x32_bf16 v[24:27], a[136:139], v[224:227], v[24:27]
v_mfma_f32_16x16x32_bf16 v[24:27], a[152:155], v[228:231], v[24:27]
s_waitcnt vmcnt(12)
v_cmp_ne_u64 vcc, s[64:65], v[10:11]
s_nop 0
s_cbranch_vccnz L_stub_ex
L_ret_ex:
v_mfma_f32_16x16x32_bf16 v[28:31], a[140:143], v[224:227], v[28:31]
buffer_load_dwordx4 v[56:59], v1, s[52:55], s1 offen offset:0 sc1
v_mfma_f32_16x16x32_bf16 v[28:31], a[156:159], v[228:231], v[28:31]
v_mfma_f32_16x16x32_bf16 v[32:35], v[240:243], v[224:227], v[32:35]
ds_read_b128 v[240:243], v127 offset:49152
v_mfma_f32_16x16x32_bf16 v[32:35], v[244:247], v[228:231], v[32:35]
ds_read_b128 v[244:247], v127 offset:50176
v_mfma_f32_16x16x32_bf16 v[16:19], a[160:163], v[232:235], v[16:19]
buffer_load_dwordx4 v[224:227], v1, s[52:55], s0 offen offset:2048 sc1
v_mfma_f32_16x16x32_bf16 v[16:19], a[176:179], v[236:239], v[16:19]
v_mfma_f32_16x16x32_bf16 v[20:23], a[164:167], v[232:235], v[20:23]
v_mfma_f32_16x16x32_bf16 v[20:23], a[180:183], v[236:239], v[20:23]
v_mfma_f32_16x16x32_bf16 v[24:27], a[168:171], v[232:235], v[24:27]
buffer_load_dwordx4 v[60:63], v1, s[52:55], s1 offen offset:1024 sc1
v_mfma_f32_16x16x32_bf16 v[24:27], a[184:187], v[236:239], v[24:27]
v_mfma_f32_16x16x32_bf16 v[28:31], a[172:175], v[232:235], v[28:31]
v_mfma_f32_16x16x32_bf16 v[28:31], a[188:191], v[236:239], v[28:31]
s_waitcnt lgkmcnt(0)
v_mfma_f32_16x16x32_bf16 v[32:35], v[248:251], v[232:235], v[32:35]
buffer_load_dwordx4 v[228:231], v1, s[52:55], s0 offen offset:3072 sc1
v_mfma_f32_16x16x32_bf16 v[32:35], v[252:255], v[236:239], v[32:35]
s_add_u32 s0, s69, 12288
ds_read_b128 v[248:251], v127 offset:51200
v_mfma_f32_16x16x32_bf16 v[16:19], a[64:67], v[168:171], v[16:19]
ds_read_b128 v[252:255], v127 offset:52224
v_mfma_f32_16x16x32_bf16 v[16:19], a[80:83], v[172:175], v[16:19]
v_mfma_f32_16x16x32_bf16 v[20:23], a[68:71], v[168:171], v[20:23]
buffer_load_dwordx4 v[64:67], v1, s[52:55], s1 offen offset:2048 sc1
v_mfma_f32_16x16x32_bf16 v[20:23], a[84:87], v[172:175], v[20:23]
v_mfma_f32_16x16x32_bf16 v[24:27], a[72:75], v[168:171], v[24:27]
s_lshl_b32 s83, s60, 12
s_add_u32 m0, s83, 65536
s_add_u32 s83, s83, 65536
v_mfma_f32_16x16x32_bf16 v[24:27], a[88:91], v[172:175], v[24:27]
s_add_u32 s83, s83, s76
v_mfma_f32_16x16x32_bf16 v[28:31], a[76:79], v[168:171], v[28:31]
buffer_load_dwordx4 v[232:235], v1, s[52:55], s0 offen offset:0 sc1
v_mfma_f32_16x16x32_bf16 v[28:31], a[92:95], v[172:175], v[28:31]
v_mfma_f32_16x16x32_bf16 v[32:35], v[240:243], v[168:171], v[32:35]
ds_read_b128 v[240:243], v127 offset:0
v_mfma_f32_16x16x32_bf16 v[32:35], v[244:247], v[172:175], v[32:35]
s_waitcnt vmcnt(16)
ds_read_b128 v[244:247], v127 offset:1024
v_mfma_f32_16x16x32_bf16 v[16:19], a[96:99], v[176:179], v[16:19]
buffer_load_dwordx4 v1, s[52:55], s83 offen offset:0 sc1 lds
v_mfma_f32_16x16x32_bf16 v[16:19], a[112:115], v[180:183], v[16:19]
v_mfma_f32_16x16x32_bf16 v[20:23], a[100:103], v[176:179], v[20:23]
v_mfma_f32_16x16x32_bf16 v[20:23], a[116:119], v[180:183], v[20:23]
v_mfma_f32_16x16x32_bf16 v[24:27], a[104:107], v[176:179], v[24:27]
buffer_load_dwordx4 v1, s[52:55], s83 offen offset:1024 sc1 lds
v_mfma_f32_16x16x32_bf16 v[24:27], a[120:123], v[180:183], v[24:27]
v_mfma_f32_16x16x32_bf16 v[28:31], a[108:111], v[176:179], v[28:31]
v_mfma_f32_16x16x32_bf16 v[28:31], a[124:127], v[180:183], v[28:31]
s_waitcnt lgkmcnt(0)
v_mfma_f32_16x16x32_bf16 v[32:35], v[248:251], v[176:179], v[32:35]
buffer_load_dwordx4 v1, s[52:55], s83 offen offset:2048 sc1 lds
v_mfma_f32_16x16x32_bf16 v[32:35], v[252:255], v[180:183], v[32:35]
s_waitcnt vmcnt(18)
ds_write_b128 v8, v[16:19] offset:0
v_mfma_f32_16x16x32_bf16 v[36:39], a[0:3], v[184:187], 0
s_waitcnt vmcnt(17)
ds_read_b128 v[248:251], v127 offset:2048
v_mfma_f32_16x16x32_bf16 v[36:39], a[16:19], v[188:191], v[36:39]
buffer_load_dwordx4 v[236:239], v1, s[52:55], s0 offen offset:1024 sc1
v_mfma_f32_16x16x32_bf16 v[40:43], a[4:7], v[184:187], 0
ds_write_b128 v8, v[20:23] offset:1024
v_mfma_f32_16x16x32_bf16 v[40:43], a[20:23], v[188:191], v[40:43]
ds_write_b128 v8, v[24:27] offset:2048
v_mfma_f32_16x16x32_bf16 v[44:47], a[8:11], v[184:187], 0
ds_read_b128 v[252:255], v127 offset:3072
v_mfma_f32_16x16x32_bf16 v[44:47], a[24:27], v[188:191], v[44:47]
buffer_load_dwordx4 v1, s[52:55], s83 offen offset:3072 sc1 lds
v_mfma_f32_16x16x32_bf16 v[48:51], a[12:15], v[184:187], 0
s_lshl_b32 s83, s60, 12
s_add_u32 m0, s83, 0
s_add_u32 s83, s83, 0
v_mfma_f32_16x16x32_bf16 v[48:51], a[28:31], v[188:191], v[48:51]
s_add_u32 s83, s83, s76
ds_write_b128 v8, v[28:31] offset:3072
v_mfma_f32_16x16x32_bf16 v[52:55], v[240:243], v[184:187], 0
s_waitcnt vmcnt(17)
ds_write_b128 v8, v[32:35] offset:4096
v_mfma_f32_16x16x32_bf16 v[52:55], v[244:247], v[188:191], v[52:55]
buffer_load_dwordx4 v[68:71], v1, s[52:55], s1 offen offset:3072 sc1
v_mfma_f32_16x16x32_bf16 v[36:39], a[32:35], v[192:195], v[36:39]
buffer_load_dwordx4 v[168:171], v1, s[52:55], s0 offen offset:2048 sc1
v_mfma_f32_16x16x32_bf16 v[36:39], a[48:51], v[196:199], v[36:39]
buffer_load_dwordx4 v[172:175], v1, s[52:55], s0 offen offset:3072 sc1
v_mfma_f32_16x16x32_bf16 v[40:43], a[36:39], v[192:195], v[40:43]
s_add_u32 s1, s1, 4096
ds_read_b128 v[240:243], v127 offset:16384
v_mfma_f32_16x16x32_bf16 v[40:43], a[52:55], v[196:199], v[40:43]
ds_read_b128 v[244:247], v127 offset:17408
v_mfma_f32_16x16x32_bf16 v[44:47], a[40:43], v[192:195], v[44:47]
v_mfma_f32_16x16x32_bf16 v[44:47], a[56:59], v[196:199], v[44:47]
buffer_load_dwordx4 v[72:75], v1, s[52:55], s1 offen offset:0 sc1
v_mfma_f32_16x16x32_bf16 v[48:51], a[44:47], v[192:195], v[48:51]
v_mfma_f32_16x16x32_bf16 v[48:51], a[60:63], v[196:199], v[48:51]
s_waitcnt lgkmcnt(1)
v_mfma_f32_16x16x32_bf16 v[52:55], v[248:251], v[192:195], v[52:55]
ds_read_b128 v[248:251], v127 offset:18432
v_mfma_f32_16x16x32_bf16 v[52:55], v[252:255], v[196:199], v[52:55]
s_waitcnt vmcnt(20)
ds_read_b128 v[252:255], v127 offset:19456
v_mfma_f32_16x16x32_bf16 v[36:39], a[192:195], v[200:203], v[36:39]
s_waitcnt vmcnt(19)
v_mfma_f32_16x16x32_bf16 v[36:39], a[208:211], v[204:207], v[36:39]
s_waitcnt lgkmcnt(0)
v_mfma_f32_16x16x32_bf16 v[40:43], a[196:199], v[200:203], v[40:43]
s_barrier
ds_read_b128 v[76:79], v9 offset:0
v_mfma_f32_16x16x32_bf16 v[40:43], a[212:215], v[204:207], v[40:43]
ds_read_b128 v[80:83], v9 offset:1024
v_mfma_f32_16x16x32_bf16 v[44:47], a[200:203], v[200:203], v[44:47]
ds_read_b128 v[84:87], v9 offset:2048
v_mfma_f32_16x16x32_bf16 v[44:47], a[216:219], v[204:207], v[44:47]
ds_read_b128 v[88:91], v9 offset:3072
v_mfma_f32_16x16x32_bf16 v[48:51], a[204:207], v[200:203], v[48:51]
ds_read_b128 v[92:95], v9 offset:4096
v_mfma_f32_16x16x32_bf16 v[48:51], a[220:223], v[204:207], v[48:51]
buffer_load_dwordx4 v1, s[52:55], s83 offen offset:0 sc1 lds
v_mfma_f32_16x16x32_bf16 v[52:55], v[240:243], v[200:203], v[52:55]
ds_read_b128 v[240:243], v127 offset:32768
v_mfma_f32_16x16x32_bf16 v[52:55], v[244:247], v[204:207], v[52:55]
s_waitcnt vmcnt(19) lgkmcnt(3)
v_fmac_f32 v96, v128, v76
v_fmac_f32 v97, v129, v77
v_mfma_f32_16x16x32_bf16 v[36:39], a[224:227], v[208:211], v[36:39]
s_waitcnt vmcnt(18)
v_fmac_f32 v98, v130, v78
v_fmac_f32 v99, v131, v79
v_mfma_f32_16x16x32_bf16 v[36:39], a[240:243], v[212:215], v[36:39]
buffer_load_dwordx4 v1, s[52:55], s83 offen offset:1024 sc1 lds
v_mfma_f32_16x16x32_bf16 v[40:43], a[228:231], v[208:211], v[40:43]
ds_read_b128 v[76:79], v9 offset:5120
v_fmac_f32 v100, v132, v80
v_mfma_f32_16x16x32_bf16 v[40:43], a[244:247], v[212:215], v[40:43]
v_fmac_f32 v101, v133, v81
v_fmac_f32 v102, v134, v82
v_mfma_f32_16x16x32_bf16 v[44:47], a[232:235], v[208:211], v[44:47]
v_fmac_f32 v103, v135, v83
v_fmac_f32 v104, v136, v84
v_mfma_f32_16x16x32_bf16 v[44:47], a[248:251], v[212:215], v[44:47]
buffer_load_dwordx4 v1, s[52:55], s83 offen offset:2048 sc1 lds
v_mfma_f32_16x16x32_bf16 v[48:51], a[236:239], v[208:211], v[48:51]
ds_read_b128 v[80:83], v9 offset:6144
v_fmac_f32 v105, v137, v85
v_mfma_f32_16x16x32_bf16 v[48:51], a[252:255], v[212:215], v[48:51]
v_fmac_f32 v106, v138, v86
v_fmac_f32 v107, v139, v87
v_mfma_f32_16x16x32_bf16 v[52:55], v[248:251], v[208:211], v[52:55]
s_waitcnt vmcnt(18) lgkmcnt(1)
ds_read_b128 v[84:87], v9 offset:7168
v_fmac_f32 v108, v140, v88
v_mfma_f32_16x16x32_bf16 v[52:55], v[252:255], v[212:215], v[52:55]
buffer_load_dwordx4 v1, s[52:55], s83 offen offset:3072 sc1 lds
v_mfma_f32_16x16x32_bf16 v[36:39], a[128:131], v[216:219], v[36:39]
s_lshl_b32 s83, s60, 12
s_add_u32 m0, s83, 16384
s_add_u32 s83, s83, 16384
v_mfma_f32_16x16x32_bf16 v[36:39], a[144:147], v[220:223], v[36:39]
s_add_u32 s83, s83, s76
v_fmac_f32 v109, v141, v89
v_fmac_f32 v110, v142, v90
v_mfma_f32_16x16x32_bf16 v[40:43], a[132:135], v[216:219], v[40:43]
ds_read_b128 v[244:247], v127 offset:33792
)ASM"
R"ASM(
v_fmac_f32 v111, v143, v91
v_mfma_f32_16x16x32_bf16 v[40:43], a[148:151], v[220:223], v[40:43]
ds_read_b128 v[88:91], v9 offset:8192
v_fmac_f32 v112, v144, v92
v_mfma_f32_16x16x32_bf16 v[44:47], a[136:139], v[216:219], v[44:47]
v_fmac_f32 v113, v145, v93
v_fmac_f32 v114, v146, v94
v_mfma_f32_16x16x32_bf16 v[44:47], a[152:155], v[220:223], v[44:47]
v_fmac_f32 v115, v147, v95
v_fmac_f32 v96, v128, v76
v_mfma_f32_16x16x32_bf16 v[48:51], a[140:143], v[216:219], v[48:51]
ds_read_b128 v[92:95], v9 offset:9216
v_fmac_f32 v97, v129, v77
v_mfma_f32_16x16x32_bf16 v[48:51], a[156:159], v[220:223], v[48:51]
buffer_load_dwordx4 v1, s[52:55], s83 offen offset:0 sc1 lds
v_mfma_f32_16x16x32_bf16 v[52:55], v[240:243], v[216:219], v[52:55]
s_waitcnt lgkmcnt(1)
v_fmac_f32 v98, v130, v78
v_fmac_f32 v99, v131, v79
v_mfma_f32_16x16x32_bf16 v[52:55], v[244:247], v[220:223], v[52:55]
s_waitcnt vmcnt(18)
ds_read_b128 v[76:79], v9 offset:10240
v_fmac_f32 v100, v132, v80
v_mfma_f32_16x16x32_bf16 v[36:39], a[160:163], v[224:227], v[36:39]
s_waitcnt vmcnt(16)
v_fmac_f32 v101, v133, v81
v_fmac_f32 v102, v134, v82
v_mfma_f32_16x16x32_bf16 v[36:39], a[176:179], v[228:231], v[36:39]
ds_read_b128 v[248:251], v127 offset:34816
v_fmac_f32 v103, v135, v83
v_mfma_f32_16x16x32_bf16 v[40:43], a[164:167], v[224:227], v[40:43]
ds_read_b128 v[252:255], v127 offset:35840
v_fmac_f32 v104, v136, v84
v_mfma_f32_16x16x32_bf16 v[40:43], a[180:183], v[228:231], v[40:43]
buffer_load_dwordx4 v1, s[52:55], s83 offen offset:1024 sc1 lds
v_mfma_f32_16x16x32_bf16 v[44:47], a[168:171], v[224:227], v[44:47]
ds_read_b128 v[80:83], v9 offset:11264
v_fmac_f32 v105, v137, v85
v_mfma_f32_16x16x32_bf16 v[44:47], a[184:187], v[228:231], v[44:47]
v_fmac_f32 v106, v138, v86
v_fmac_f32 v107, v139, v87
v_mfma_f32_16x16x32_bf16 v[48:51], a[172:175], v[224:227], v[48:51]
s_waitcnt lgkmcnt(1)
ds_read_b128 v[84:87], v9 offset:12288
v_fmac_f32 v108, v140, v88
v_mfma_f32_16x16x32_bf16 v[48:51], a[188:191], v[228:231], v[48:51]
buffer_load_dwordx4 v1, s[52:55], s83 offen offset:2048 sc1 lds
v_mfma_f32_16x16x32_bf16 v[52:55], v[248:251], v[224:227], v[52:55]
v_fmac_f32 v109, v141, v89
v_fmac_f32 v110, v142, v90
v_mfma_f32_16x16x32_bf16 v[52:55], v[252:255], v[228:231], v[52:55]
s_waitcnt vmcnt(16)
v_fmac_f32 v111, v143, v91
v_fmac_f32 v112, v144, v92
v_mfma_f32_16x16x32_bf16 v[36:39], a[64:67], v[232:235], v[36:39]
s_waitcnt vmcnt(12)
ds_read_b128 v[88:91], v9 offset:13312
v_fmac_f32 v113, v145, v93
v_mfma_f32_16x16x32_bf16 v[36:39], a[80:83], v[236:239], v[36:39]
ds_read_b128 v[240:243], v127 offset:49152
v_fmac_f32 v114, v146, v94
v_mfma_f32_16x16x32_bf16 v[40:43], a[68:71], v[232:235], v[40:43]
ds_read_b128 v[244:247], v127 offset:50176
v_fmac_f32 v115, v147, v95
v_mfma_f32_16x16x32_bf16 v[40:43], a[84:87], v[236:239], v[40:43]
buffer_load_dwordx4 v1, s[52:55], s83 offen offset:3072 sc1 lds
v_mfma_f32_16x16x32_bf16 v[44:47], a[72:75], v[232:235], v[44:47]
s_lshl_b32 s83, s60, 12
s_add_u32 m0, s83, 32768
s_add_u32 s83, s83, 32768
v_mfma_f32_16x16x32_bf16 v[44:47], a[88:91], v[236:239], v[44:47]
s_add_u32 s83, s83, s76
ds_read_b128 v[92:95], v9 offset:14336
v_fmac_f32 v96, v128, v76
v_mfma_f32_16x16x32_bf16 v[48:51], a[76:79], v[232:235], v[48:51]
s_waitcnt lgkmcnt(1)
ds_read_b128 v[248:251], v127 offset:51200
v_fmac_f32 v97, v129, v77
v_mfma_f32_16x16x32_bf16 v[48:51], a[92:95], v[236:239], v[48:51]
buffer_load_dwordx4 v1, s[52:55], s83 offen offset:0 sc1 lds
v_mfma_f32_16x16x32_bf16 v[52:55], v[240:243], v[232:235], v[52:55]
ds_read_b128 v[252:255], v127 offset:52224
v_fmac_f32 v98, v130, v78
v_mfma_f32_16x16x32_bf16 v[52:55], v[244:247], v[236:239], v[52:55]
s_waitcnt vmcnt(11)
v_fmac_f32 v99, v131, v79
v_fmac_f32 v100, v132, v80
v_mfma_f32_16x16x32_bf16 v[36:39], a[96:99], v[168:171], v[36:39]
s_waitcnt vmcnt(10)
v_fmac_f32 v101, v133, v81
v_fmac_f32 v102, v134, v82
v_mfma_f32_16x16x32_bf16 v[36:39], a[112:115], v[172:175], v[36:39]
buffer_load_dwordx4 v1, s[52:55], s83 offen offset:1024 sc1 lds
v_mfma_f32_16x16x32_bf16 v[40:43], a[100:103], v[168:171], v[40:43]
v_fmac_f32 v103, v135, v83
v_fmac_f32 v104, v136, v84
v_mfma_f32_16x16x32_bf16 v[40:43], a[116:119], v[172:175], v[40:43]
v_fmac_f32 v105, v137, v85
v_fmac_f32 v106, v138, v86
v_mfma_f32_16x16x32_bf16 v[44:47], a[104:107], v[168:171], v[44:47]
v_fmac_f32 v107, v139, v87
v_fmac_f32 v108, v140, v88
v_mfma_f32_16x16x32_bf16 v[44:47], a[120:123], v[172:175], v[44:47]
buffer_load_dwordx4 v1, s[52:55], s83 offen offset:2048 sc1 lds
v_mfma_f32_16x16x32_bf16 v[48:51], a[108:111], v[168:171], v[48:51]
s_waitcnt lgkmcnt(0)
v_fmac_f32 v109, v141, v89
v_fmac_f32 v110, v142, v90
v_mfma_f32_16x16x32_bf16 v[48:51], a[124:127], v[172:175], v[48:51]
buffer_load_dwordx4 v1, s[52:55], s83 offen offset:3072 sc1 lds
v_mfma_f32_16x16x32_bf16 v[52:55], v[248:251], v[168:171], v[52:55]
s_lshl_b32 s83, s60, 12
s_add_u32 m0, s83, 49152
s_add_u32 s83, s83, 49152
v_mfma_f32_16x16x32_bf16 v[52:55], v[252:255], v[172:175], v[52:55]
v_fmac_f32 v111, v143, v91
v_fmac_f32 v112, v144, v92
v_fmac_f32 v113, v145, v93
v_fmac_f32 v114, v146, v94
v_fmac_f32 v115, v147, v95
s_add_u32 s83, s83, s76
buffer_load_dwordx4 v1, s[52:55], s83 offen offset:0 sc1 lds
buffer_load_dwordx4 v1, s[52:55], s83 offen offset:1024 sc1 lds
buffer_load_dwordx4 v1, s[52:55], s83 offen offset:2048 sc1 lds
buffer_load_dwordx4 v1, s[52:55], s83 offen offset:3072 sc1 lds
s_barrier
v_lshlrev_b32 v127, 3, v2
v_pk_fma_f32 v[96:97], v[128:129], v[56:57], v[96:97]
v_pk_fma_f32 v[98:99], v[130:131], v[58:59], v[98:99]
v_pk_fma_f32 v[96:97], v[128:129], v[36:37], v[96:97]
v_pk_fma_f32 v[98:99], v[130:131], v[38:39], v[98:99]
v_pk_mul_f32 v[148:149], v[148:149], v[96:97]
v_pk_mul_f32 v[170:171], v[96:97], v[96:97]
v_pk_mul_f32 v[150:151], v[150:151], v[98:99]
v_pk_fma_f32 v[170:171], v[98:99], v[98:99], v[170:171]
v_pk_add_f32 v[168:169], v[96:97], v[98:99]
v_cvt_pk_bf16_f32 v16, v148, v149
v_cvt_pk_bf16_f32 v17, v150, v151
v_pk_fma_f32 v[100:101], v[132:133], v[60:61], v[100:101]
v_pk_fma_f32 v[102:103], v[134:135], v[62:63], v[102:103]
v_pk_fma_f32 v[100:101], v[132:133], v[40:41], v[100:101]
v_pk_fma_f32 v[102:103], v[134:135], v[42:43], v[102:103]
v_pk_mul_f32 v[152:153], v[152:153], v[100:101]
v_pk_fma_f32 v[170:171], v[100:101], v[100:101], v[170:171]
v_pk_add_f32 v[168:169], v[168:169], v[100:101]
v_pk_mul_f32 v[154:155], v[154:155], v[102:103]
v_pk_fma_f32 v[170:171], v[102:103], v[102:103], v[170:171]
v_pk_add_f32 v[168:169], v[168:169], v[102:103]
v_cvt_pk_bf16_f32 v18, v152, v153
v_cvt_pk_bf16_f32 v19, v154, v155
buffer_store_dwordx4 v[16:19], v1, s[52:55], s72 offen offset:0 sc1
v_pk_fma_f32 v[104:105], v[136:137], v[64:65], v[104:105]
v_pk_fma_f32 v[106:107], v[138:139], v[66:67], v[106:107]
v_pk_fma_f32 v[104:105], v[136:137], v[44:45], v[104:105]
v_pk_fma_f32 v[106:107], v[138:139], v[46:47], v[106:107]
v_pk_mul_f32 v[156:157], v[156:157], v[104:105]
v_pk_fma_f32 v[170:171], v[104:105], v[104:105], v[170:171]
v_pk_add_f32 v[168:169], v[168:169], v[104:105]
v_pk_mul_f32 v[158:159], v[158:159], v[106:107]
v_pk_fma_f32 v[170:171], v[106:107], v[106:107], v[170:171]
v_pk_add_f32 v[168:169], v[168:169], v[106:107]
v_cvt_pk_bf16_f32 v20, v156, v157
v_cvt_pk_bf16_f32 v21, v158, v159
v_pk_fma_f32 v[108:109], v[140:141], v[68:69], v[108:109]
v_pk_fma_f32 v[110:111], v[142:143], v[70:71], v[110:111]
v_pk_fma_f32 v[108:109], v[140:141], v[48:49], v[108:109]
v_pk_fma_f32 v[110:111], v[142:143], v[50:51], v[110:111]
v_pk_mul_f32 v[160:161], v[160:161], v[108:109]
v_pk_fma_f32 v[170:171], v[108:109], v[108:109], v[170:171]
v_pk_add_f32 v[168:169], v[168:169], v[108:109]
v_pk_mul_f32 v[162:163], v[162:163], v[110:111]
v_pk_fma_f32 v[170:171], v[110:111], v[110:111], v[170:171]
v_pk_add_f32 v[168:169], v[168:169], v[110:111]
v_cvt_pk_bf16_f32 v22, v160, v161
v_cvt_pk_bf16_f32 v23, v162, v163
buffer_store_dwordx4 v[20:23], v1, s[52:55], s72 offen offset:1024 sc1
s_waitcnt vmcnt(18)
v_pk_fma_f32 v[112:113], v[144:145], v[72:73], v[112:113]
v_pk_fma_f32 v[114:115], v[146:147], v[74:75], v[114:115]
v_pk_fma_f32 v[112:113], v[144:145], v[52:53], v[112:113]
v_pk_fma_f32 v[114:115], v[146:147], v[54:55], v[114:115]
v_pk_mul_f32 v[164:165], v[164:165], v[112:113]
v_pk_fma_f32 v[170:171], v[112:113], v[112:113], v[170:171]
v_pk_add_f32 v[168:169], v[168:169], v[112:113]
v_pk_mul_f32 v[166:167], v[166:167], v[114:115]
v_pk_fma_f32 v[170:171], v[114:115], v[114:115], v[170:171]
v_pk_add_f32 v[168:169], v[168:169], v[114:115]
v_cvt_pk_bf16_f32 v24, v164, v165
v_cvt_pk_bf16_f32 v25, v166, v167
buffer_store_dwordx2 v[24:25], v127, s[52:55], s73 offen sc1
v_add_f32 v168, v168, v169
v_add_f32 v169, v170, v171
v_lshlrev_b32 v172, 3, v2
v_lshlrev_b32 v173, 26, v12
v_permlane32_swap_b32 v168, v169
v_or_b32 v172, v172, v173
v_add_f32 v168, v168, v169
v_mov_b32 v169, v168
s_nop 1
v_permlane16_swap_b32 v168, v169
v_add_f32 v168, v168, v169
v_mov_b32 v169, v168
s_nop 1
v_permlane32_swap_b32 v168, v169
buffer_store_dwordx2 v[168:169], v172, s[52:55], s74 offen sc1
s_lshl_b32 s83, s60, 12
s_add_u32 m0, s83, 81920
s_add_u32 s83, s83, 81920
s_add_u32 s83, s83, s76
buffer_load_dwordx4 v1, s[52:55], s83 offen offset:0 sc1 lds
buffer_load_dwordx4 v1, s[52:55], s83 offen offset:1024 sc1 lds
buffer_load_dwordx4 v1, s[52:55], s83 offen offset:2048 sc1 lds
buffer_load_dwordx4 v1, s[52:55], s83 offen offset:3072 sc1 lds
s_lshl_b32 s83, s60, 12
s_add_u32 m0, s83, 98304
s_add_u32 s83, s83, 98304
s_add_u32 s83, s83, s76
buffer_load_dwordx4 v1, s[52:55], s83 offen offset:0 sc1 lds
buffer_load_dwordx4 v1, s[52:55], s83 offen offset:1024 sc1 lds
buffer_load_dwordx4 v1, s[52:55], s83 offen offset:2048 sc1 lds
buffer_load_dwordx4 v1, s[52:55], s83 offen offset:3072 sc1 lds
s_lshl_b32 s83, s78, 3
s_lshl_b32 s84, s60, 1
s_add_u32 s83, s83, s84
s_mul_i32 s66, s83, 20480
s_add_u32 s66, s66, 5242880
s_add_u32 s67, s66, 20480
s_lshl_b32 s68, s83, 10
s_add_u32 s68, s68, 1179648
v_and_b32 v216, 15, v2
v_lshlrev_b32 v216, 3, v216
v_lshl_add_u32 v216, v13, 4, v216
s_lshl_b32 s83, s78, 4
s_lshr_b32 s84, s60, 1
s_lshl_b32 s84, s84, 3
s_add_u32 s83, s83, s84
s_lshl_b32 s83, s83, 8
s_add_u32 s83, s83, 393216
v_and_b32 v6, 7, v2
v_lshlrev_b32 v6, 8, v6
v_add_u32 v6, s83, v6
s_mul_i32 s83, s60, 20480
v_add_u32 v220, s83, v1
v_add_u32 v221, 81920, v220
v_and_b32 v223, 1, v2
v_mul_u32_u24 v222, 1280, v223
v_add_u32 v222, v222, v13
v_add_u32 v222, 148736, v222
s_lshl_b32 s83, s60, 7
s_add_u32 s83, s83, 155648
v_lshl_add_u32 v223, v223, 6, v13
v_add_u32 v223, s83, v223
v_add_u32 v217, 155648, v13
s_and_b32 s83, s81, 1
s_mul_i32 s84, s81, 5
s_add_u32 s85, s84, s83
s_mul_i32 s85, s85, 57344
s_and_b32 s86, s63, 0
s_lshl_b32 s86, s86, 2
s_add_u32 s86, s86, s60
s_mul_i32 s86, s86, 14336
s_add_u32 s86, s86, 17825792
s_add_u32 s82, s85, s86
s_add_u32 s90, s82, 57344
s_add_u32 s91, s82, 114688
s_add_u32 s100, s82, 172032
s_lshl_b32 s83, s83, 2
s_sub_u32 s84, s84, s83
s_add_u32 s84, s84, 4
s_mul_i32 s84, s84, 57344
s_add_u32 s101, s84, s86
v_mov_b32 v10, s64
v_mov_b32 v11, s65
s_add_u32 s86, s87, 393216
s_waitcnt vmcnt(8)
s_barrier
buffer_store_dwordx2 v[10:11], v5, s[52:55], s86 offen sc1
s_lshl_b32 s83, s60, 12
s_add_u32 m0, s83, 114688
s_add_u32 s84, s76, 114688
s_sub_u32 s85, s77, 8192
s_cmp_lt_u32 s60, 2
s_cselect_b32 s84, s84, s85
s_add_u32 s83, s83, s84
buffer_load_dwordx4 v1, s[52:55], s83 offen offset:0 sc1 lds
buffer_load_dwordx4 v1, s[52:55], s83 offen offset:1024 sc1 lds
buffer_load_dwordx4 v1, s[52:55], s83 offen offset:2048 sc1 lds
buffer_load_dwordx4 v1, s[52:55], s83 offen offset:3072 sc1 lds
s_lshl_b32 s83, s60, 12
s_add_u32 m0, s83, 131072
s_add_u32 s83, s83, 8192
s_add_u32 s83, s83, s77
s_cmp_lt_u32 s60, 3
s_cselect_b32 s83, s83, 100663296
buffer_load_dwordx4 v1, s[52:55], s83 offen offset:0 sc1 lds
buffer_load_dwordx4 v1, s[52:55], s83 offen offset:1024 sc1 lds
buffer_load_dwordx4 v1, s[52:55], s83 offen offset:2048 sc1 lds
buffer_load_dwordx4 v1, s[52:55], s83 offen offset:3072 sc1 lds
ds_read_b128 a[0:3], v222 offset:0
ds_read_b128 a[80:83], v220 offset:0
ds_read_b128 a[4:7], v222 offset:64
ds_read_b128 a[84:87], v220 offset:1024
ds_read_b128 a[8:11], v222 offset:128
ds_read_b128 a[88:91], v220 offset:2048
ds_read_b128 a[12:15], v222 offset:192
ds_read_b128 a[92:95], v220 offset:3072
ds_read_b128 a[16:19], v222 offset:256
ds_read_b128 a[96:99], v220 offset:4096
ds_read_b128 a[20:23], v222 offset:320
ds_read_b128 a[100:103], v220 offset:5120
ds_read_b128 a[24:27], v222 offset:384
ds_read_b128 a[104:107], v220 offset:6144
s_waitcnt lgkmcnt(12)
v_mfma_f32_16x16x32_bf16 v[224:227], a[80:83], a[0:3], 0
s_waitcnt lgkmcnt(10)
ds_read_b128 a[28:31], v222 offset:448
ds_read_b128 a[108:111], v220 offset:7168
v_mfma_f32_16x16x32_bf16 v[224:227], a[84:87], a[4:7], v[224:227]
s_waitcnt lgkmcnt(10)
ds_read_b128 a[32:35], v222 offset:512
ds_read_b128 a[112:115], v220 offset:8192
v_mfma_f32_16x16x32_bf16 v[224:227], a[88:91], a[8:11], v[224:227]
s_waitcnt lgkmcnt(10)
ds_read_b128 a[36:39], v222 offset:576
ds_read_b128 a[116:119], v220 offset:9216
v_mfma_f32_16x16x32_bf16 v[224:227], a[92:95], a[12:15], v[224:227]
s_waitcnt lgkmcnt(10)
ds_read_b128 a[40:43], v222 offset:640
ds_read_b128 a[120:123], v220 offset:10240
v_mfma_f32_16x16x32_bf16 v[224:227], a[96:99], a[16:19], v[224:227]
s_waitcnt lgkmcnt(8)
ds_read_b128 a[44:47], v222 offset:704
ds_read_b128 a[124:127], v220 offset:11264
v_mfma_f32_16x16x32_bf16 v[224:227], a[100:103], a[20:23], v[224:227]
ds_read_b128 a[48:51], v222 offset:768
ds_read_b128 a[128:131], v220 offset:12288
v_mfma_f32_16x16x32_bf16 v[224:227], a[104:107], a[24:27], v[224:227]
s_waitcnt lgkmcnt(8)
ds_read_b128 a[52:55], v222 offset:832
ds_read_b128 a[132:135], v220 offset:13312
v_mfma_f32_16x16x32_bf16 v[224:227], a[108:111], a[28:31], v[224:227]
ds_read_b128 a[56:59], v222 offset:896
ds_read_b128 a[136:139], v220 offset:14336
v_mfma_f32_16x16x32_bf16 v[224:227], a[112:115], a[32:35], v[224:227]
s_waitcnt lgkmcnt(8)
ds_read_b128 a[60:63], v222 offset:960
ds_read_b128 a[140:143], v220 offset:15360
v_mfma_f32_16x16x32_bf16 v[224:227], a[116:119], a[36:39], v[224:227]
ds_read_b128 a[64:67], v222 offset:1024
ds_read_b128 a[144:147], v220 offset:16384
v_mfma_f32_16x16x32_bf16 v[224:227], a[120:123], a[40:43], v[224:227]
s_waitcnt lgkmcnt(8)
ds_read_b128 a[68:71], v222 offset:1088
ds_read_b128 a[148:151], v220 offset:17408
v_mfma_f32_16x16x32_bf16 v[224:227], a[124:127], a[44:47], v[224:227]
L_pad_b_2:
s_cbranch_execz L_padret_b
ds_read_b128 a[72:75], v222 offset:1152
ds_read_b128 a[152:155], v220 offset:18432
v_mfma_f32_16x16x32_bf16 v[224:227], a[128:131], a[48:51], v[224:227]
s_waitcnt lgkmcnt(8)
ds_read_b128 a[76:79], v222 offset:1216
ds_read_b128 a[156:159], v220 offset:19456
v_mfma_f32_16x16x32_bf16 v[224:227], a[132:135], a[52:55], v[224:227]
v_mfma_f32_16x16x32_bf16 v[224:227], a[136:139], a[56:59], v[224:227]
s_waitcnt lgkmcnt(6)
v_mfma_f32_16x16x32_bf16 v[224:227], a[140:143], a[60:63], v[224:227]
v_mfma_f32_16x16x32_bf16 v[224:227], a[144:147], a[64:67], v[224:227]
s_waitcnt lgkmcnt(2)
v_mfma_f32_16x16x32_bf16 v[224:227], a[148:151], a[68:71], v[224:227]
v_mfma_f32_16x16x32_bf16 v[224:227], a[152:155], a[72:75], v[224:227]
s_waitcnt lgkmcnt(0)
v_mfma_f32_16x16x32_bf16 v[224:227], a[156:159], a[76:79], v[224:227]
s_nop 7
ds_write_b128 v223, v[224:227] offset:0
s_waitcnt vmcnt(0) lgkmcnt(0)
s_barrier
buffer_load_dwordx2 v[10:11], v6, s[52:55], 0 offen sc1
v_add_u32 v3, 0, v1
buffer_load_dwordx4 a[160:163], v3, s[52:55], s82 offen offset:0 sc1
buffer_load_dwordx4 a[164:167], v3, s[52:55], s90 offen offset:0 sc1
buffer_load_dwordx4 a[168:171], v3, s[52:55], s91 offen offset:0 sc1
buffer_load_dwordx4 a[172:175], v3, s[52:55], s100 offen offset:0 sc1
buffer_load_dwordx4 a[176:179], v3, s[52:55], s101 offen offset:0 sc1
buffer_load_dwordx4 a[180:183], v3, s[52:55], s82 offen offset:1024 sc1
buffer_load_dwordx4 a[184:187], v3, s[52:55], s90 offen offset:1024 sc1
buffer_load_dwordx4 a[188:191], v3, s[52:55], s91 offen offset:1024 sc1
ds_read_b128 a[80:83], v221 offset:0
ds_read_b128 a[84:87], v221 offset:1024
ds_read_b128 a[88:91], v221 offset:2048
ds_read_b128 a[92:95], v221 offset:3072
ds_read_b128 a[96:99], v221 offset:4096
ds_read_b128 a[100:103], v221 offset:5120
ds_read_b128 a[104:107], v221 offset:6144
s_waitcnt lgkmcnt(6)
v_mfma_f32_16x16x32_bf16 v[224:227], a[80:83], a[0:3], 0
s_waitcnt lgkmcnt(5)
ds_read_b128 a[108:111], v221 offset:7168
ds_read_b128 a[112:115], v221 offset:8192
v_mfma_f32_16x16x32_bf16 v[224:227], a[84:87], a[4:7], v[224:227]
)ASM"
R"ASM(
s_waitcnt lgkmcnt(6)
ds_read_b128 a[116:119], v221 offset:9216
ds_read_b128 a[120:123], v221 offset:10240
v_mfma_f32_16x16x32_bf16 v[224:227], a[88:91], a[8:11], v[224:227]
s_waitcnt lgkmcnt(7)
ds_read_b128 a[124:127], v221 offset:11264
ds_read_b128 a[128:131], v221 offset:12288
v_mfma_f32_16x16x32_bf16 v[224:227], a[92:95], a[12:15], v[224:227]
s_waitcnt lgkmcnt(8)
ds_read_b128 a[132:135], v221 offset:13312
ds_read_b128 a[136:139], v221 offset:14336
v_mfma_f32_16x16x32_bf16 v[224:227], a[96:99], a[16:19], v[224:227]
s_waitcnt lgkmcnt(8)
ds_read_b128 a[140:143], v221 offset:15360
ds_read_b128 a[144:147], v221 offset:16384
v_mfma_f32_16x16x32_bf16 v[224:227], a[100:103], a[20:23], v[224:227]
ds_read_b128 a[148:151], v221 offset:17408
ds_read_b128 a[152:155], v221 offset:18432
v_mfma_f32_16x16x32_bf16 v[224:227], a[104:107], a[24:27], v[224:227]
s_waitcnt lgkmcnt(8)
ds_read_b128 a[156:159], v221 offset:19456
v_mfma_f32_16x16x32_bf16 v[224:227], a[108:111], a[28:31], v[224:227]
v_mfma_f32_16x16x32_bf16 v[224:227], a[112:115], a[32:35], v[224:227]
v_mfma_f32_16x16x32_bf16 v[224:227], a[116:119], a[36:39], v[224:227]
v_mfma_f32_16x16x32_bf16 v[224:227], a[120:123], a[40:43], v[224:227]
s_waitcnt lgkmcnt(1)
v_mfma_f32_16x16x32_bf16 v[224:227], a[124:127], a[44:47], v[224:227]
v_mfma_f32_16x16x32_bf16 v[224:227], a[128:131], a[48:51], v[224:227]
v_mfma_f32_16x16x32_bf16 v[224:227], a[132:135], a[52:55], v[224:227]
v_mfma_f32_16x16x32_bf16 v[224:227], a[136:139], a[56:59], v[224:227]
v_mfma_f32_16x16x32_bf16 v[224:227], a[140:143], a[60:63], v[224:227]
v_mfma_f32_16x16x32_bf16 v[224:227], a[144:147], a[64:67], v[224:227]
v_mfma_f32_16x16x32_bf16 v[224:227], a[148:151], a[68:71], v[224:227]
v_mfma_f32_16x16x32_bf16 v[224:227], a[152:155], a[72:75], v[224:227]
s_waitcnt lgkmcnt(0)
v_mfma_f32_16x16x32_bf16 v[224:227], a[156:159], a[76:79], v[224:227]
s_waitcnt vmcnt(8)
s_nop 6
ds_write_b128 v223, v[224:227] offset:512
v_cmp_ne_u64 vcc, s[64:65], v[10:11]
s_nop 0
s_cbranch_vccnz L_stub_x1
L_ret_x1:
buffer_load_dwordx2 v[72:73], v216, s[52:55], s68 offen offset:0 sc1
buffer_load_dwordx2 v[74:75], v216, s[52:55], s68 offen offset:128 sc1
buffer_load_dwordx2 v[76:77], v216, s[52:55], s68 offen offset:1024 sc1
buffer_load_dwordx2 v[78:79], v216, s[52:55], s68 offen offset:1152 sc1
v_add_u32 v126, 0, v1
buffer_load_dwordx4 a[0:3], v126, s[52:55], s66 offen offset:0 sc1
buffer_load_dwordx4 a[4:7], v126, s[52:55], s67 offen offset:0 sc1
buffer_load_dwordx4 a[8:11], v126, s[52:55], s66 offen offset:1024 sc1
buffer_load_dwordx4 a[12:15], v126, s[52:55], s67 offen offset:1024 sc1
v_add_u32 v92, 2048, v127
buffer_load_dwordx2 a[16:17], v92, s[52:55], s66 offen offset:0 sc1
buffer_load_dwordx2 a[18:19], v92, s[52:55], s66 offen offset:512 sc1
buffer_load_dwordx2 a[20:21], v92, s[52:55], s67 offen offset:0 sc1
buffer_load_dwordx2 a[22:23], v92, s[52:55], s67 offen offset:512 sc1
buffer_load_dwordx4 a[24:27], v126, s[52:55], s66 offen offset:3072 sc1
buffer_load_dwordx4 a[28:31], v126, s[52:55], s67 offen offset:3072 sc1
v_add_u32 v126, 4096, v1
buffer_load_dwordx4 a[32:35], v126, s[52:55], s66 offen offset:0 sc1
buffer_load_dwordx4 a[36:39], v126, s[52:55], s67 offen offset:0 sc1
s_waitcnt lgkmcnt(0)
s_barrier
ds_read_b128 v[184:187], v1 offset:0
ds_read_b128 v[188:191], v1 offset:1024
ds_read_b128 v[192:195], v1 offset:20480
ds_read_b128 v[196:199], v1 offset:21504
s_waitcnt vmcnt(9) lgkmcnt(2)
v_mfma_f32_16x16x32_bf16 v[16:19], v[184:187], a[0:3], 0
s_add_u32 s83, s88, 55296
ds_read_b128 v[200:203], v1 offset:40960
v_add_u32 v86, 61440, v1
v_mfma_f32_16x16x32_bf16 v[16:19], v[188:191], a[8:11], v[16:19]
s_add_u32 s84, s89, 55296
ds_read_b128 v[204:207], v1 offset:41984
v_add_u32 v92, 7168, v127
v_mfma_f32_16x16x32_bf16 v[20:23], v[184:187], a[4:7], 0
s_waitcnt vmcnt(8)
ds_read_b128 v[208:211], v86 offset:0
v_add_u32 v87, 122880, v1
v_mfma_f32_16x16x32_bf16 v[20:23], v[188:191], a[12:15], v[20:23]
s_waitcnt lgkmcnt(4)
ds_read_b128 v[212:215], v86 offset:1024
v_mfma_f32_16x16x32_bf16 v[24:27], v[192:195], a[0:3], 0
s_waitcnt lgkmcnt(4)
ds_read_b128 v[184:187], v86 offset:20480
v_mfma_f32_16x16x32_bf16 v[24:27], v[196:199], a[8:11], v[24:27]
ds_read_b128 v[188:191], v86 offset:21504
v_mfma_f32_16x16x32_bf16 v[28:31], v[192:195], a[4:7], 0
buffer_load_dwordx4 a[40:43], v126, s[52:55], s66 offen offset:1024 sc1
v_mfma_f32_16x16x32_bf16 v[28:31], v[196:199], a[12:15], v[28:31]
s_waitcnt lgkmcnt(3)
ds_read_b128 v[192:195], v86 offset:40960
v_mfma_f32_16x16x32_bf16 v[32:35], v[200:203], a[0:3], 0
ds_read_b128 v[196:199], v86 offset:41984
v_mfma_f32_16x16x32_bf16 v[32:35], v[204:207], a[8:11], v[32:35]
ds_read_b128 v[128:131], v217 offset:0
v_mfma_f32_16x16x32_bf16 v[36:39], v[200:203], a[4:7], 0
buffer_load_dwordx4 a[44:47], v126, s[52:55], s67 offen offset:1024 sc1
v_mfma_f32_16x16x32_bf16 v[36:39], v[204:207], a[12:15], v[36:39]
ds_read_b128 v[200:203], v87 offset:0
v_mfma_f32_16x16x32_bf16 v[40:43], v[208:211], a[0:3], 0
s_waitcnt lgkmcnt(3)
ds_read_b128 v[204:207], v87 offset:1024
v_mfma_f32_16x16x32_bf16 v[40:43], v[212:215], a[8:11], v[40:43]
ds_read_b128 v[132:135], v217 offset:64
v_mfma_f32_16x16x32_bf16 v[44:47], v[208:211], a[4:7], 0
buffer_load_dwordx4 a[48:51], v126, s[52:55], s66 offen offset:2048 sc1
v_mfma_f32_16x16x32_bf16 v[44:47], v[212:215], a[12:15], v[44:47]
ds_read_b128 v[208:211], v1 offset:2048
v_mfma_f32_16x16x32_bf16 v[48:51], v[184:187], a[0:3], 0
ds_read_b128 v[212:215], v1 offset:3072
v_mfma_f32_16x16x32_bf16 v[48:51], v[188:191], a[8:11], v[48:51]
ds_read_b128 v[136:139], v217 offset:128
v_mfma_f32_16x16x32_bf16 v[52:55], v[184:187], a[4:7], 0
buffer_load_dwordx4 a[52:55], v126, s[52:55], s67 offen offset:2048 sc1
v_mfma_f32_16x16x32_bf16 v[52:55], v[188:191], a[12:15], v[52:55]
ds_read_b128 v[184:187], v1 offset:22528
v_add_u32 v126, 8192, v1
v_mfma_f32_16x16x32_bf16 v[56:59], v[192:195], a[0:3], 0
s_waitcnt lgkmcnt(3)
ds_read_b128 v[188:191], v1 offset:23552
v_mfma_f32_16x16x32_bf16 v[56:59], v[196:199], a[8:11], v[56:59]
ds_read_b128 v[140:143], v217 offset:192
v_mfma_f32_16x16x32_bf16 v[60:63], v[192:195], a[4:7], 0
buffer_load_dwordx2 a[56:57], v92, s[52:55], s66 offen offset:0 sc1
v_mfma_f32_16x16x32_bf16 v[60:63], v[196:199], a[12:15], v[60:63]
ds_read_b128 v[192:195], v1 offset:43008
v_mfma_f32_16x16x32_bf16 v[64:67], v[200:203], a[0:3], 0
ds_read_b128 v[196:199], v1 offset:44032
v_mfma_f32_16x16x32_bf16 v[64:67], v[204:207], a[8:11], v[64:67]
ds_read_b128 v[144:147], v217 offset:256
v_mfma_f32_16x16x32_bf16 v[68:71], v[200:203], a[4:7], 0
buffer_load_dwordx2 a[58:59], v92, s[52:55], s66 offen offset:512 sc1
v_mfma_f32_16x16x32_bf16 v[68:71], v[204:207], a[12:15], v[68:71]
s_waitcnt vmcnt(12)
ds_read_b128 v[200:203], v86 offset:2048
v_mfma_f32_16x16x32_bf16 v[16:19], v[208:211], a[16:19], v[16:19]
s_waitcnt vmcnt(9) lgkmcnt(3)
ds_read_b128 v[204:207], v86 offset:3072
v_mfma_f32_16x16x32_bf16 v[16:19], v[212:215], a[24:27], v[16:19]
s_waitcnt vmcnt(8)
ds_read_b128 v[148:151], v217 offset:320
v_add_f32 v72, v72, v74
v_mfma_f32_16x16x32_bf16 v[20:23], v[208:211], a[20:23], v[20:23]
buffer_load_dwordx2 a[60:61], v92, s[52:55], s67 offen offset:0 sc1
v_mfma_f32_16x16x32_bf16 v[20:23], v[212:215], a[28:31], v[20:23]
ds_read_b128 v[208:211], v86 offset:22528
v_add_f32 v73, v73, v75
v_mfma_f32_16x16x32_bf16 v[24:27], v[184:187], a[16:19], v[24:27]
ds_read_b128 v[212:215], v86 offset:23552
v_mov_b32 v74, v72
v_mfma_f32_16x16x32_bf16 v[24:27], v[188:191], a[24:27], v[24:27]
ds_read_b128 v[152:155], v217 offset:384
v_mov_b32 v75, v73
v_mfma_f32_16x16x32_bf16 v[28:31], v[184:187], a[20:23], v[28:31]
buffer_load_dwordx2 a[62:63], v92, s[52:55], s67 offen offset:512 sc1
v_mfma_f32_16x16x32_bf16 v[28:31], v[188:191], a[28:31], v[28:31]
ds_read_b128 v[184:187], v86 offset:43008
v_add_u32 v92, 12288, v127
v_mfma_f32_16x16x32_bf16 v[32:35], v[192:195], a[16:19], v[32:35]
s_waitcnt lgkmcnt(3)
ds_read_b128 v[188:191], v86 offset:44032
v_add_f32 v76, v76, v78
v_mfma_f32_16x16x32_bf16 v[32:35], v[196:199], a[24:27], v[32:35]
ds_read_b128 v[156:159], v217 offset:448
v_add_f32 v77, v77, v79
v_mfma_f32_16x16x32_bf16 v[36:39], v[192:195], a[20:23], v[36:39]
buffer_load_dwordx4 a[64:67], v126, s[52:55], s66 offen offset:0 sc1
v_mfma_f32_16x16x32_bf16 v[36:39], v[196:199], a[28:31], v[36:39]
ds_read_b128 v[192:195], v87 offset:2048
v_mov_b32 v78, v76
v_mfma_f32_16x16x32_bf16 v[40:43], v[200:203], a[16:19], v[40:43]
ds_read_b128 v[196:199], v87 offset:3072
v_mov_b32 v79, v77
v_mfma_f32_16x16x32_bf16 v[40:43], v[204:207], a[24:27], v[40:43]
ds_read_b128 v[160:163], v217 offset:512
v_mfma_f32_16x16x32_bf16 v[44:47], v[200:203], a[20:23], v[44:47]
buffer_load_dwordx4 a[68:71], v126, s[52:55], s67 offen offset:0 sc1
v_mfma_f32_16x16x32_bf16 v[44:47], v[204:207], a[28:31], v[44:47]
ds_read_b128 v[200:203], v1 offset:4096
v_mfma_f32_16x16x32_bf16 v[48:51], v[208:211], a[16:19], v[48:51]
s_waitcnt lgkmcnt(3)
ds_read_b128 v[204:207], v1 offset:5120
v_mfma_f32_16x16x32_bf16 v[48:51], v[212:215], a[24:27], v[48:51]
ds_read_b128 v[164:167], v217 offset:576
v_mfma_f32_16x16x32_bf16 v[52:55], v[208:211], a[20:23], v[52:55]
buffer_load_dwordx4 a[72:75], v126, s[52:55], s66 offen offset:1024 sc1
v_mfma_f32_16x16x32_bf16 v[52:55], v[212:215], a[28:31], v[52:55]
ds_read_b128 v[208:211], v1 offset:24576
v_mfma_f32_16x16x32_bf16 v[56:59], v[184:187], a[16:19], v[56:59]
ds_read_b128 v[212:215], v1 offset:25600
v_mfma_f32_16x16x32_bf16 v[56:59], v[188:191], a[24:27], v[56:59]
ds_read_b128 v[168:171], v217 offset:640
v_mfma_f32_16x16x32_bf16 v[60:63], v[184:187], a[20:23], v[60:63]
buffer_load_dwordx4 a[76:79], v126, s[52:55], s67 offen offset:1024 sc1
v_mfma_f32_16x16x32_bf16 v[60:63], v[188:191], a[28:31], v[60:63]
ds_read_b128 v[184:187], v1 offset:45056
v_mfma_f32_16x16x32_bf16 v[64:67], v[192:195], a[16:19], v[64:67]
s_waitcnt lgkmcnt(3)
ds_read_b128 v[188:191], v1 offset:46080
v_mfma_f32_16x16x32_bf16 v[64:67], v[196:199], a[24:27], v[64:67]
ds_read_b128 v[172:175], v217 offset:704
v_mfma_f32_16x16x32_bf16 v[68:71], v[192:195], a[20:23], v[68:71]
buffer_load_dwordx4 a[80:83], v126, s[52:55], s66 offen offset:2048 sc1
v_mfma_f32_16x16x32_bf16 v[68:71], v[196:199], a[28:31], v[68:71]
s_waitcnt vmcnt(14)
ds_read_b128 v[192:195], v86 offset:4096
v_mfma_f32_16x16x32_bf16 v[16:19], v[200:203], a[32:35], v[16:19]
s_waitcnt vmcnt(12)
ds_read_b128 v[196:199], v86 offset:5120
v_mfma_f32_16x16x32_bf16 v[16:19], v[204:207], a[40:43], v[16:19]
s_waitcnt vmcnt(11)
ds_read_b128 v[176:179], v217 offset:768
v_mfma_f32_16x16x32_bf16 v[20:23], v[200:203], a[36:39], v[20:23]
buffer_load_dwordx4 a[84:87], v126, s[52:55], s67 offen offset:2048 sc1
v_mfma_f32_16x16x32_bf16 v[20:23], v[204:207], a[44:47], v[20:23]
ds_read_b128 v[200:203], v86 offset:24576
v_mfma_f32_16x16x32_bf16 v[24:27], v[208:211], a[32:35], v[24:27]
s_waitcnt lgkmcnt(3)
ds_read_b128 v[204:207], v86 offset:25600
v_mfma_f32_16x16x32_bf16 v[24:27], v[212:215], a[40:43], v[24:27]
ds_read_b128 v[180:183], v217 offset:832
v_mfma_f32_16x16x32_bf16 v[28:31], v[208:211], a[36:39], v[28:31]
buffer_load_dwordx4 a[88:91], v126, s[52:55], s66 offen offset:3072 sc1
v_mfma_f32_16x16x32_bf16 v[28:31], v[212:215], a[44:47], v[28:31]
ds_read_b128 v[208:211], v86 offset:45056
v_mfma_f32_16x16x32_bf16 v[32:35], v[184:187], a[32:35], v[32:35]
ds_read_b128 v[212:215], v86 offset:46080
v_mfma_f32_16x16x32_bf16 v[32:35], v[188:191], a[40:43], v[32:35]
v_permlane32_swap_b32 v72, v74
v_mfma_f32_16x16x32_bf16 v[36:39], v[184:187], a[36:39], v[36:39]
buffer_load_dwordx4 a[92:95], v126, s[52:55], s67 offen offset:3072 sc1
v_mfma_f32_16x16x32_bf16 v[36:39], v[188:191], a[44:47], v[36:39]
ds_read_b128 v[184:187], v87 offset:4096
v_add_u32 v126, 12288, v1
v_mfma_f32_16x16x32_bf16 v[40:43], v[192:195], a[32:35], v[40:43]
s_waitcnt lgkmcnt(2)
ds_read_b128 v[188:191], v87 offset:5120
v_add_f32 v72, v72, v74
v_mfma_f32_16x16x32_bf16 v[40:43], v[196:199], a[40:43], v[40:43]
v_permlane32_swap_b32 v73, v75
v_mfma_f32_16x16x32_bf16 v[44:47], v[192:195], a[36:39], v[44:47]
buffer_load_dwordx2 a[96:97], v92, s[52:55], s66 offen offset:0 sc1
v_mfma_f32_16x16x32_bf16 v[44:47], v[196:199], a[44:47], v[44:47]
ds_read_b128 v[192:195], v1 offset:6144
v_add_f32 v73, v73, v75
v_mfma_f32_16x16x32_bf16 v[48:51], v[200:203], a[32:35], v[48:51]
ds_read_b128 v[196:199], v1 offset:7168
v_mov_b32 v74, v72
v_mfma_f32_16x16x32_bf16 v[48:51], v[204:207], a[40:43], v[48:51]
v_mov_b32 v75, v73
v_mfma_f32_16x16x32_bf16 v[52:55], v[200:203], a[36:39], v[52:55]
buffer_load_dwordx2 a[98:99], v92, s[52:55], s66 offen offset:512 sc1
v_mfma_f32_16x16x32_bf16 v[52:55], v[204:207], a[44:47], v[52:55]
ds_read_b128 v[200:203], v1 offset:26624
v_mfma_f32_16x16x32_bf16 v[56:59], v[208:211], a[32:35], v[56:59]
s_waitcnt lgkmcnt(2)
ds_read_b128 v[204:207], v1 offset:27648
v_mfma_f32_16x16x32_bf16 v[56:59], v[212:215], a[40:43], v[56:59]
v_permlane16_swap_b32 v72, v74
v_mfma_f32_16x16x32_bf16 v[60:63], v[208:211], a[36:39], v[60:63]
buffer_load_dwordx2 a[100:101], v92, s[52:55], s67 offen offset:0 sc1
v_mfma_f32_16x16x32_bf16 v[60:63], v[212:215], a[44:47], v[60:63]
ds_read_b128 v[208:211], v1 offset:47104
v_add_f32 v72, v72, v74
v_mfma_f32_16x16x32_bf16 v[64:67], v[184:187], a[32:35], v[64:67]
ds_read_b128 v[212:215], v1 offset:48128
v_mul_f32 v80, 0.0015625, v72
v_mfma_f32_16x16x32_bf16 v[64:67], v[188:191], a[40:43], v[64:67]
v_permlane16_swap_b32 v73, v75
v_mfma_f32_16x16x32_bf16 v[68:71], v[184:187], a[36:39], v[68:71]
buffer_load_dwordx2 a[102:103], v92, s[52:55], s67 offen offset:512 sc1
v_mfma_f32_16x16x32_bf16 v[68:71], v[188:191], a[44:47], v[68:71]
s_waitcnt vmcnt(17)
ds_read_b128 v[184:187], v86 offset:6144
v_add_u32 v92, 17408, v127
v_mfma_f32_16x16x32_bf16 v[16:19], v[192:195], a[48:51], v[16:19]
s_waitcnt vmcnt(14) lgkmcnt(2)
ds_read_b128 v[188:191], v86 offset:7168
v_add_f32 v73, v73, v75
v_mfma_f32_16x16x32_bf16 v[16:19], v[196:199], a[56:59], v[16:19]
s_waitcnt vmcnt(12)
v_mul_f32 v73, 0.0015625, v73
v_fma_f32 v73, -v80, v80, v73
v_mfma_f32_16x16x32_bf16 v[20:23], v[192:195], a[52:55], v[20:23]
buffer_load_dwordx4 a[104:107], v126, s[52:55], s66 offen offset:1024 sc1
v_mfma_f32_16x16x32_bf16 v[20:23], v[196:199], a[60:63], v[20:23]
ds_read_b128 v[192:195], v86 offset:26624
v_max_f32 v73, 0, v73
v_mfma_f32_16x16x32_bf16 v[24:27], v[200:203], a[48:51], v[24:27]
ds_read_b128 v[196:199], v86 offset:27648
v_add_f32 v73, 1e-05, v73
v_mfma_f32_16x16x32_bf16 v[24:27], v[204:207], a[56:59], v[24:27]
v_rsq_f32 v74, v73
v_mfma_f32_16x16x32_bf16 v[28:31], v[200:203], a[52:55], v[28:31]
buffer_load_dwordx4 a[108:111], v126, s[52:55], s67 offen offset:1024 sc1
v_mfma_f32_16x16x32_bf16 v[28:31], v[204:207], a[60:63], v[28:31]
ds_read_b128 v[200:203], v86 offset:47104
v_mul_f32 v82, v73, v74
v_mfma_f32_16x16x32_bf16 v[32:35], v[208:211], a[48:51], v[32:35]
s_waitcnt lgkmcnt(2)
ds_read_b128 v[204:207], v86 offset:48128
v_mul_f32 v88, -1.4426950408889634, v74
v_mfma_f32_16x16x32_bf16 v[32:35], v[212:215], a[56:59], v[32:35]
v_mov_b32 v83, v82
v_mov_b32 v89, v88
v_mfma_f32_16x16x32_bf16 v[36:39], v[208:211], a[52:55], v[36:39]
buffer_load_dwordx4 a[112:115], v126, s[52:55], s66 offen offset:2048 sc1
v_mfma_f32_16x16x32_bf16 v[36:39], v[212:215], a[60:63], v[36:39]
ds_read_b128 v[208:211], v87 offset:6144
v_mfma_f32_16x16x32_bf16 v[40:43], v[184:187], a[48:51], v[40:43]
ds_read_b128 v[212:215], v87 offset:7168
v_mfma_f32_16x16x32_bf16 v[40:43], v[188:191], a[56:59], v[40:43]
v_permlane32_swap_b32 v76, v78
v_mfma_f32_16x16x32_bf16 v[44:47], v[184:187], a[52:55], v[44:47]
buffer_load_dwordx4 a[116:119], v126, s[52:55], s67 offen offset:2048 sc1
v_mfma_f32_16x16x32_bf16 v[44:47], v[188:191], a[60:63], v[44:47]
ds_read_b128 v[184:187], v1 offset:8192
v_add_f32 v76, v76, v78
v_mfma_f32_16x16x32_bf16 v[48:51], v[192:195], a[48:51], v[48:51]
s_waitcnt lgkmcnt(2)
ds_read_b128 v[188:191], v1 offset:9216
v_mov_b32 v78, v76
v_mfma_f32_16x16x32_bf16 v[48:51], v[196:199], a[56:59], v[48:51]
v_permlane32_swap_b32 v77, v79
v_mfma_f32_16x16x32_bf16 v[52:55], v[192:195], a[52:55], v[52:55]
buffer_load_dwordx4 a[120:123], v126, s[52:55], s66 offen offset:3072 sc1
v_mfma_f32_16x16x32_bf16 v[52:55], v[196:199], a[60:63], v[52:55]
ds_read_b128 v[192:195], v1 offset:28672
v_add_f32 v77, v77, v79
v_mfma_f32_16x16x32_bf16 v[56:59], v[200:203], a[48:51], v[56:59]
ds_read_b128 v[196:199], v1 offset:29696
v_mov_b32 v79, v77
v_mfma_f32_16x16x32_bf16 v[56:59], v[204:207], a[56:59], v[56:59]
v_permlane16_swap_b32 v76, v78
v_mfma_f32_16x16x32_bf16 v[60:63], v[200:203], a[52:55], v[60:63]
buffer_load_dwordx4 a[124:127], v126, s[52:55], s67 offen offset:3072 sc1
v_mfma_f32_16x16x32_bf16 v[60:63], v[204:207], a[60:63], v[60:63]
ds_read_b128 v[200:203], v1 offset:49152
v_add_u32 v126, 16384, v1
v_mfma_f32_16x16x32_bf16 v[64:67], v[208:211], a[48:51], v[64:67]
s_waitcnt lgkmcnt(2)
ds_read_b128 v[204:207], v1 offset:50176
v_add_f32 v76, v76, v78
v_mfma_f32_16x16x32_bf16 v[64:67], v[212:215], a[56:59], v[64:67]
v_permlane16_swap_b32 v77, v79
v_mfma_f32_16x16x32_bf16 v[68:71], v[208:211], a[52:55], v[68:71]
buffer_load_dwordx4 a[128:131], v126, s[52:55], s66 offen offset:0 sc1
v_mfma_f32_16x16x32_bf16 v[68:71], v[212:215], a[60:63], v[68:71]
s_waitcnt vmcnt(18)
ds_read_b128 v[208:211], v86 offset:8192
v_add_f32 v77, v77, v79
v_mfma_f32_16x16x32_bf16 v[16:19], v[184:187], a[64:67], v[16:19]
s_waitcnt vmcnt(16)
ds_read_b128 v[212:215], v86 offset:9216
v_mul_f32 v81, 0.0015625, v76
v_mfma_f32_16x16x32_bf16 v[16:19], v[188:191], a[72:75], v[16:19]
s_waitcnt vmcnt(15)
v_mul_f32 v77, 0.0015625, v77
v_fma_f32 v77, -v81, v81, v77
v_mfma_f32_16x16x32_bf16 v[20:23], v[184:187], a[68:71], v[20:23]
buffer_load_dwordx4 a[132:135], v126, s[52:55], s67 offen offset:0 sc1
v_mfma_f32_16x16x32_bf16 v[20:23], v[188:191], a[76:79], v[20:23]
ds_read_b128 v[184:187], v86 offset:28672
v_max_f32 v77, 0, v77
v_mfma_f32_16x16x32_bf16 v[24:27], v[192:195], a[64:67], v[24:27]
s_waitcnt lgkmcnt(2)
ds_read_b128 v[188:191], v86 offset:29696
v_add_f32 v77, 1e-05, v77
v_mfma_f32_16x16x32_bf16 v[24:27], v[196:199], a[72:75], v[24:27]
v_rsq_f32 v78, v77
v_mfma_f32_16x16x32_bf16 v[28:31], v[192:195], a[68:71], v[28:31]
buffer_load_dwordx2 a[136:137], v92, s[52:55], s66 offen offset:0 sc1
v_mfma_f32_16x16x32_bf16 v[28:31], v[196:199], a[76:79], v[28:31]
ds_read_b128 v[192:195], v86 offset:49152
v_mul_f32 v84, v77, v78
v_mfma_f32_16x16x32_bf16 v[32:35], v[200:203], a[64:67], v[32:35]
ds_read_b128 v[196:199], v86 offset:50176
v_mul_f32 v90, -1.4426950408889634, v78
v_mfma_f32_16x16x32_bf16 v[32:35], v[204:207], a[72:75], v[32:35]
v_mov_b32 v85, v84
v_mov_b32 v91, v90
v_mfma_f32_16x16x32_bf16 v[36:39], v[200:203], a[68:71], v[36:39]
buffer_load_dwordx2 a[138:139], v92, s[52:55], s66 offen offset:512 sc1
v_mfma_f32_16x16x32_bf16 v[36:39], v[204:207], a[76:79], v[36:39]
ds_read_b128 v[200:203], v87 offset:8192
v_mul_f32 v218, v128, v84
v_mfma_f32_16x16x32_bf16 v[40:43], v[208:211], a[64:67], v[40:43]
s_waitcnt lgkmcnt(2)
ds_read_b128 v[204:207], v87 offset:9216
v_mul_f32 v128, v128, v82
v_mfma_f32_16x16x32_bf16 v[40:43], v[212:215], a[72:75], v[40:43]
v_fma_f32 v128, -v80, v132, v128
v_fma_f32 v132, -v81, v132, v218
v_mfma_f32_16x16x32_bf16 v[44:47], v[208:211], a[68:71], v[44:47]
buffer_load_dwordx2 a[140:141], v92, s[52:55], s67 offen offset:0 sc1
v_mfma_f32_16x16x32_bf16 v[44:47], v[212:215], a[76:79], v[44:47]
ds_read_b128 v[208:211], v1 offset:10240
)ASM"
R"ASM(
v_mul_f32 v219, v129, v84
v_mfma_f32_16x16x32_bf16 v[48:51], v[184:187], a[64:67], v[48:51]
ds_read_b128 v[212:215], v1 offset:11264
v_mul_f32 v129, v129, v82
v_mfma_f32_16x16x32_bf16 v[48:51], v[188:191], a[72:75], v[48:51]
v_fma_f32 v129, -v80, v133, v129
v_fma_f32 v133, -v81, v133, v219
v_mfma_f32_16x16x32_bf16 v[52:55], v[184:187], a[68:71], v[52:55]
buffer_load_dwordx2 a[142:143], v92, s[52:55], s67 offen offset:512 sc1
v_mfma_f32_16x16x32_bf16 v[52:55], v[188:191], a[76:79], v[52:55]
ds_read_b128 v[184:187], v1 offset:30720
v_mul_f32 v218, v130, v84
v_mfma_f32_16x16x32_bf16 v[56:59], v[192:195], a[64:67], v[56:59]
s_waitcnt lgkmcnt(2)
ds_read_b128 v[188:191], v1 offset:31744
v_mul_f32 v130, v130, v82
v_mfma_f32_16x16x32_bf16 v[56:59], v[196:199], a[72:75], v[56:59]
v_fma_f32 v130, -v80, v134, v130
v_fma_f32 v134, -v81, v134, v218
v_mfma_f32_16x16x32_bf16 v[60:63], v[192:195], a[68:71], v[60:63]
buffer_load_dwordx4 a[144:147], v126, s[52:55], s66 offen offset:2048 sc1
v_mfma_f32_16x16x32_bf16 v[60:63], v[196:199], a[76:79], v[60:63]
ds_read_b128 v[192:195], v1 offset:51200
v_mul_f32 v219, v131, v84
v_mfma_f32_16x16x32_bf16 v[64:67], v[200:203], a[64:67], v[64:67]
ds_read_b128 v[196:199], v1 offset:52224
v_mul_f32 v131, v131, v82
v_mfma_f32_16x16x32_bf16 v[64:67], v[204:207], a[72:75], v[64:67]
v_fma_f32 v131, -v80, v135, v131
v_fma_f32 v135, -v81, v135, v219
v_mfma_f32_16x16x32_bf16 v[68:71], v[200:203], a[68:71], v[68:71]
buffer_load_dwordx4 a[148:151], v126, s[52:55], s67 offen offset:2048 sc1
v_mfma_f32_16x16x32_bf16 v[68:71], v[204:207], a[76:79], v[68:71]
s_waitcnt vmcnt(21)
ds_read_b128 v[200:203], v86 offset:10240
v_mul_f32 v218, v136, v84
v_mfma_f32_16x16x32_bf16 v[16:19], v[208:211], a[80:83], v[16:19]
s_waitcnt vmcnt(19) lgkmcnt(2)
ds_read_b128 v[204:207], v86 offset:11264
v_mul_f32 v136, v136, v82
v_mfma_f32_16x16x32_bf16 v[16:19], v[212:215], a[88:91], v[16:19]
s_waitcnt vmcnt(18)
v_fma_f32 v136, -v80, v140, v136
v_fma_f32 v140, -v81, v140, v218
v_mfma_f32_16x16x32_bf16 v[20:23], v[208:211], a[84:87], v[20:23]
buffer_load_dwordx4 a[152:155], v126, s[52:55], s66 offen offset:3072 sc1
v_mfma_f32_16x16x32_bf16 v[20:23], v[212:215], a[92:95], v[20:23]
ds_read_b128 v[208:211], v86 offset:30720
v_mul_f32 v219, v137, v84
v_mfma_f32_16x16x32_bf16 v[24:27], v[184:187], a[80:83], v[24:27]
ds_read_b128 v[212:215], v86 offset:31744
v_mul_f32 v137, v137, v82
v_mfma_f32_16x16x32_bf16 v[24:27], v[188:191], a[88:91], v[24:27]
v_fma_f32 v137, -v80, v141, v137
v_fma_f32 v141, -v81, v141, v219
v_mfma_f32_16x16x32_bf16 v[28:31], v[184:187], a[84:87], v[28:31]
buffer_load_dwordx4 a[156:159], v126, s[52:55], s67 offen offset:3072 sc1
v_mfma_f32_16x16x32_bf16 v[28:31], v[188:191], a[92:95], v[28:31]
ds_read_b128 v[184:187], v86 offset:51200
v_mul_f32 v218, v138, v84
v_mfma_f32_16x16x32_bf16 v[32:35], v[192:195], a[80:83], v[32:35]
s_waitcnt lgkmcnt(2)
ds_read_b128 v[188:191], v86 offset:52224
v_mul_f32 v138, v138, v82
v_mfma_f32_16x16x32_bf16 v[32:35], v[196:199], a[88:91], v[32:35]
v_fma_f32 v138, -v80, v142, v138
v_fma_f32 v142, -v81, v142, v218
v_mfma_f32_16x16x32_bf16 v[36:39], v[192:195], a[84:87], v[36:39]
buffer_load_dwordx4 a[192:195], v3, s[52:55], s100 offen offset:1024 sc1
v_mfma_f32_16x16x32_bf16 v[36:39], v[196:199], a[92:95], v[36:39]
ds_read_b128 v[192:195], v87 offset:10240
v_mul_f32 v219, v139, v84
v_mfma_f32_16x16x32_bf16 v[40:43], v[200:203], a[80:83], v[40:43]
ds_read_b128 v[196:199], v87 offset:11264
v_mul_f32 v139, v139, v82
v_mfma_f32_16x16x32_bf16 v[40:43], v[204:207], a[88:91], v[40:43]
v_fma_f32 v139, -v80, v143, v139
v_fma_f32 v143, -v81, v143, v219
v_mfma_f32_16x16x32_bf16 v[44:47], v[200:203], a[84:87], v[44:47]
buffer_load_dwordx4 a[196:199], v3, s[52:55], s101 offen offset:1024 sc1
v_mfma_f32_16x16x32_bf16 v[44:47], v[204:207], a[92:95], v[44:47]
ds_read_b128 v[200:203], v1 offset:12288
v_mul_f32 v218, v144, v84
v_mfma_f32_16x16x32_bf16 v[48:51], v[208:211], a[80:83], v[48:51]
s_waitcnt lgkmcnt(2)
ds_read_b128 v[204:207], v1 offset:13312
v_mul_f32 v144, v144, v82
v_mfma_f32_16x16x32_bf16 v[48:51], v[212:215], a[88:91], v[48:51]
v_fma_f32 v144, -v80, v148, v144
v_fma_f32 v148, -v81, v148, v218
v_mfma_f32_16x16x32_bf16 v[52:55], v[208:211], a[84:87], v[52:55]
buffer_load_dwordx4 a[200:203], v3, s[52:55], s82 offen offset:2048 sc1
v_mfma_f32_16x16x32_bf16 v[52:55], v[212:215], a[92:95], v[52:55]
ds_read_b128 v[208:211], v1 offset:32768
v_mul_f32 v219, v145, v84
v_mfma_f32_16x16x32_bf16 v[56:59], v[184:187], a[80:83], v[56:59]
ds_read_b128 v[212:215], v1 offset:33792
v_mul_f32 v145, v145, v82
v_mfma_f32_16x16x32_bf16 v[56:59], v[188:191], a[88:91], v[56:59]
v_fma_f32 v145, -v80, v149, v145
v_fma_f32 v149, -v81, v149, v219
v_mfma_f32_16x16x32_bf16 v[60:63], v[184:187], a[84:87], v[60:63]
buffer_load_dwordx4 a[204:207], v3, s[52:55], s90 offen offset:2048 sc1
v_mfma_f32_16x16x32_bf16 v[60:63], v[188:191], a[92:95], v[60:63]
ds_read_b128 v[184:187], v1 offset:53248
v_mul_f32 v218, v146, v84
v_mfma_f32_16x16x32_bf16 v[64:67], v[192:195], a[80:83], v[64:67]
s_waitcnt lgkmcnt(2)
ds_read_b128 v[188:191], v1 offset:54272
v_mul_f32 v146, v146, v82
v_mfma_f32_16x16x32_bf16 v[64:67], v[196:199], a[88:91], v[64:67]
v_fma_f32 v146, -v80, v150, v146
v_fma_f32 v150, -v81, v150, v218
v_mfma_f32_16x16x32_bf16 v[68:71], v[192:195], a[84:87], v[68:71]
buffer_load_dwordx4 a[208:211], v3, s[52:55], s91 offen offset:2048 sc1
v_mfma_f32_16x16x32_bf16 v[68:71], v[196:199], a[92:95], v[68:71]
s_waitcnt vmcnt(23)
ds_read_b128 v[192:195], v86 offset:12288
v_mul_f32 v219, v147, v84
v_mfma_f32_16x16x32_bf16 v[16:19], v[200:203], a[96:99], v[16:19]
s_waitcnt vmcnt(20)
ds_read_b128 v[196:199], v86 offset:13312
v_mul_f32 v147, v147, v82
v_mfma_f32_16x16x32_bf16 v[16:19], v[204:207], a[104:107], v[16:19]
s_waitcnt vmcnt(19)
v_fma_f32 v147, -v80, v151, v147
v_fma_f32 v151, -v81, v151, v219
v_mfma_f32_16x16x32_bf16 v[20:23], v[200:203], a[100:103], v[20:23]
buffer_load_dwordx4 a[212:215], v3, s[52:55], s100 offen offset:2048 sc1
v_mfma_f32_16x16x32_bf16 v[20:23], v[204:207], a[108:111], v[20:23]
ds_read_b128 v[200:203], v86 offset:32768
v_mul_f32 v218, v152, v84
v_mfma_f32_16x16x32_bf16 v[24:27], v[208:211], a[96:99], v[24:27]
s_waitcnt lgkmcnt(2)
ds_read_b128 v[204:207], v86 offset:33792
v_mul_f32 v152, v152, v82
v_mfma_f32_16x16x32_bf16 v[24:27], v[212:215], a[104:107], v[24:27]
L_pad_b_3:
s_cbranch_execz L_padret_b
v_fma_f32 v152, -v80, v156, v152
v_fma_f32 v156, -v81, v156, v218
v_mfma_f32_16x16x32_bf16 v[28:31], v[208:211], a[100:103], v[28:31]
buffer_load_dwordx4 a[216:219], v3, s[52:55], s101 offen offset:2048 sc1
v_mfma_f32_16x16x32_bf16 v[28:31], v[212:215], a[108:111], v[28:31]
ds_read_b128 v[208:211], v86 offset:53248
v_mul_f32 v219, v153, v84
v_mfma_f32_16x16x32_bf16 v[32:35], v[184:187], a[96:99], v[32:35]
ds_read_b128 v[212:215], v86 offset:54272
v_mul_f32 v153, v153, v82
v_mfma_f32_16x16x32_bf16 v[32:35], v[188:191], a[104:107], v[32:35]
v_fma_f32 v153, -v80, v157, v153
v_fma_f32 v157, -v81, v157, v219
v_mfma_f32_16x16x32_bf16 v[36:39], v[184:187], a[100:103], v[36:39]
buffer_load_dwordx4 a[220:223], v3, s[52:55], s82 offen offset:3072 sc1
v_mfma_f32_16x16x32_bf16 v[36:39], v[188:191], a[108:111], v[36:39]
ds_read_b128 v[184:187], v87 offset:12288
v_mul_f32 v218, v154, v84
v_mfma_f32_16x16x32_bf16 v[40:43], v[192:195], a[96:99], v[40:43]
s_waitcnt lgkmcnt(2)
ds_read_b128 v[188:191], v87 offset:13312
v_mul_f32 v154, v154, v82
v_mfma_f32_16x16x32_bf16 v[40:43], v[196:199], a[104:107], v[40:43]
v_fma_f32 v154, -v80, v158, v154
v_fma_f32 v158, -v81, v158, v218
v_mfma_f32_16x16x32_bf16 v[44:47], v[192:195], a[100:103], v[44:47]
buffer_load_dwordx4 a[224:227], v3, s[52:55], s90 offen offset:3072 sc1
v_mfma_f32_16x16x32_bf16 v[44:47], v[196:199], a[108:111], v[44:47]
ds_read_b128 v[192:195], v1 offset:14336
v_mul_f32 v219, v155, v84
v_mfma_f32_16x16x32_bf16 v[48:51], v[200:203], a[96:99], v[48:51]
ds_read_b128 v[196:199], v1 offset:15360
v_mul_f32 v155, v155, v82
v_mfma_f32_16x16x32_bf16 v[48:51], v[204:207], a[104:107], v[48:51]
v_fma_f32 v155, -v80, v159, v155
v_fma_f32 v159, -v81, v159, v219
v_mfma_f32_16x16x32_bf16 v[52:55], v[200:203], a[100:103], v[52:55]
buffer_load_dwordx4 a[228:231], v3, s[52:55], s91 offen offset:3072 sc1
v_mfma_f32_16x16x32_bf16 v[52:55], v[204:207], a[108:111], v[52:55]
ds_read_b128 v[200:203], v1 offset:34816
v_mul_f32 v218, v160, v84
v_mfma_f32_16x16x32_bf16 v[56:59], v[208:211], a[96:99], v[56:59]
s_waitcnt lgkmcnt(2)
ds_read_b128 v[204:207], v1 offset:35840
v_mul_f32 v160, v160, v82
v_mfma_f32_16x16x32_bf16 v[56:59], v[212:215], a[104:107], v[56:59]
v_fma_f32 v160, -v80, v164, v160
v_fma_f32 v164, -v81, v164, v218
v_mfma_f32_16x16x32_bf16 v[60:63], v[208:211], a[100:103], v[60:63]
buffer_load_dwordx4 a[232:235], v3, s[52:55], s100 offen offset:3072 sc1
v_mfma_f32_16x16x32_bf16 v[60:63], v[212:215], a[108:111], v[60:63]
ds_read_b128 v[208:211], v1 offset:55296
v_mul_f32 v219, v161, v84
v_mfma_f32_16x16x32_bf16 v[64:67], v[184:187], a[96:99], v[64:67]
ds_read_b128 v[212:215], v1 offset:56320
v_mul_f32 v161, v161, v82
v_mfma_f32_16x16x32_bf16 v[64:67], v[188:191], a[104:107], v[64:67]
v_fma_f32 v161, -v80, v165, v161
v_fma_f32 v165, -v81, v165, v219
v_mfma_f32_16x16x32_bf16 v[68:71], v[184:187], a[100:103], v[68:71]
buffer_load_dwordx4 a[236:239], v3, s[52:55], s101 offen offset:3072 sc1
v_mfma_f32_16x16x32_bf16 v[68:71], v[188:191], a[108:111], v[68:71]
s_waitcnt vmcnt(25)
ds_read_b128 v[184:187], v86 offset:14336
v_mul_f32 v218, v162, v84
v_mfma_f32_16x16x32_bf16 v[16:19], v[192:195], a[112:115], v[16:19]
s_waitcnt vmcnt(23) lgkmcnt(2)
ds_read_b128 v[188:191], v86 offset:15360
v_mul_f32 v162, v162, v82
v_mfma_f32_16x16x32_bf16 v[16:19], v[196:199], a[120:123], v[16:19]
v_fma_f32 v162, -v80, v166, v162
v_fma_f32 v166, -v81, v166, v218
v_mfma_f32_16x16x32_bf16 v[20:23], v[192:195], a[116:119], v[20:23]
s_waitcnt vmcnt(22)
v_mul_f32 v219, v163, v84
v_mul_f32 v163, v163, v82
v_mfma_f32_16x16x32_bf16 v[20:23], v[196:199], a[124:127], v[20:23]
ds_read_b128 v[192:195], v86 offset:34816
v_fma_f32 v163, -v80, v167, v163
v_mfma_f32_16x16x32_bf16 v[24:27], v[200:203], a[112:115], v[24:27]
ds_read_b128 v[196:199], v86 offset:35840
v_fma_f32 v167, -v81, v167, v219
v_mfma_f32_16x16x32_bf16 v[24:27], v[204:207], a[120:123], v[24:27]
v_mul_f32 v218, v168, v84
v_mul_f32 v168, v168, v82
v_mfma_f32_16x16x32_bf16 v[28:31], v[200:203], a[116:119], v[28:31]
v_fma_f32 v168, -v80, v172, v168
v_fma_f32 v172, -v81, v172, v218
v_mfma_f32_16x16x32_bf16 v[28:31], v[204:207], a[124:127], v[28:31]
ds_read_b128 v[200:203], v86 offset:55296
v_mul_f32 v219, v169, v84
v_mfma_f32_16x16x32_bf16 v[32:35], v[208:211], a[112:115], v[32:35]
s_waitcnt lgkmcnt(2)
ds_read_b128 v[204:207], v86 offset:56320
v_mul_f32 v169, v169, v82
v_mfma_f32_16x16x32_bf16 v[32:35], v[212:215], a[120:123], v[32:35]
v_fma_f32 v169, -v80, v173, v169
v_fma_f32 v173, -v81, v173, v219
v_mfma_f32_16x16x32_bf16 v[36:39], v[208:211], a[116:119], v[36:39]
v_mul_f32 v218, v170, v84
v_mul_f32 v170, v170, v82
v_mfma_f32_16x16x32_bf16 v[36:39], v[212:215], a[124:127], v[36:39]
ds_read_b128 v[208:211], v87 offset:14336
v_fma_f32 v170, -v80, v174, v170
v_mfma_f32_16x16x32_bf16 v[40:43], v[184:187], a[112:115], v[40:43]
ds_read_b128 v[212:215], v87 offset:15360
v_fma_f32 v174, -v81, v174, v218
v_mfma_f32_16x16x32_bf16 v[40:43], v[188:191], a[120:123], v[40:43]
v_mul_f32 v219, v171, v84
v_mul_f32 v171, v171, v82
v_mfma_f32_16x16x32_bf16 v[44:47], v[184:187], a[116:119], v[44:47]
v_fma_f32 v171, -v80, v175, v171
v_fma_f32 v175, -v81, v175, v219
v_mfma_f32_16x16x32_bf16 v[44:47], v[188:191], a[124:127], v[44:47]
ds_read_b128 v[184:187], v1 offset:16384
v_mul_f32 v218, v176, v84
v_mfma_f32_16x16x32_bf16 v[48:51], v[192:195], a[112:115], v[48:51]
s_waitcnt lgkmcnt(2)
ds_read_b128 v[188:191], v1 offset:17408
v_mul_f32 v176, v176, v82
v_mfma_f32_16x16x32_bf16 v[48:51], v[196:199], a[120:123], v[48:51]
v_fma_f32 v176, -v80, v180, v176
v_fma_f32 v180, -v81, v180, v218
v_mfma_f32_16x16x32_bf16 v[52:55], v[192:195], a[116:119], v[52:55]
v_mul_f32 v219, v177, v84
v_mul_f32 v177, v177, v82
v_mfma_f32_16x16x32_bf16 v[52:55], v[196:199], a[124:127], v[52:55]
ds_read_b128 v[192:195], v1 offset:36864
v_fma_f32 v177, -v80, v181, v177
v_mfma_f32_16x16x32_bf16 v[56:59], v[200:203], a[112:115], v[56:59]
ds_read_b128 v[196:199], v1 offset:37888
v_fma_f32 v181, -v81, v181, v219
v_mfma_f32_16x16x32_bf16 v[56:59], v[204:207], a[120:123], v[56:59]
v_mul_f32 v218, v178, v84
v_mul_f32 v178, v178, v82
v_mfma_f32_16x16x32_bf16 v[60:63], v[200:203], a[116:119], v[60:63]
v_fma_f32 v178, -v80, v182, v178
v_fma_f32 v182, -v81, v182, v218
v_mfma_f32_16x16x32_bf16 v[60:63], v[204:207], a[124:127], v[60:63]
ds_read_b128 v[200:203], v1 offset:57344
v_mul_f32 v219, v179, v84
v_mfma_f32_16x16x32_bf16 v[64:67], v[208:211], a[112:115], v[64:67]
s_waitcnt lgkmcnt(2)
ds_read_b128 v[204:207], v1 offset:58368
v_mul_f32 v179, v179, v82
v_mfma_f32_16x16x32_bf16 v[64:67], v[212:215], a[120:123], v[64:67]
v_fma_f32 v179, -v80, v183, v179
v_fma_f32 v183, -v81, v183, v219
v_mfma_f32_16x16x32_bf16 v[68:71], v[208:211], a[116:119], v[68:71]
s_waitcnt vmcnt(21)
ds_read_b128 v[208:211], v86 offset:16384
v_add_u32 v3, 4096, v1
v_mfma_f32_16x16x32_bf16 v[68:71], v[212:215], a[124:127], v[68:71]
buffer_load_dwordx4 a[240:243], v3, s[52:55], s82 offen offset:0 sc1
v_mfma_f32_16x16x32_bf16 v[16:19], v[184:187], a[128:131], v[16:19]
s_waitcnt vmcnt(19)
ds_read_b128 v[212:215], v86 offset:17408
v_mfma_f32_16x16x32_bf16 v[16:19], v[188:191], a[136:139], v[16:19]
v_mfma_f32_16x16x32_bf16 v[20:23], v[184:187], a[132:135], v[20:23]
s_waitcnt vmcnt(17)
ds_read_b128 v[184:187], v86 offset:36864
v_mfma_f32_16x16x32_bf16 v[20:23], v[188:191], a[140:143], v[20:23]
buffer_load_dwordx4 a[244:247], v3, s[52:55], s90 offen offset:0 sc1
v_mfma_f32_16x16x32_bf16 v[24:27], v[192:195], a[128:131], v[24:27]
s_waitcnt lgkmcnt(2)
ds_read_b128 v[188:191], v86 offset:37888
v_mfma_f32_16x16x32_bf16 v[24:27], v[196:199], a[136:139], v[24:27]
v_mfma_f32_16x16x32_bf16 v[28:31], v[192:195], a[132:135], v[28:31]
ds_read_b128 v[192:195], v86 offset:57344
v_mfma_f32_16x16x32_bf16 v[28:31], v[196:199], a[140:143], v[28:31]
buffer_load_dwordx4 a[248:251], v3, s[52:55], s91 offen offset:0 sc1
v_mfma_f32_16x16x32_bf16 v[32:35], v[200:203], a[128:131], v[32:35]
ds_read_b128 v[196:199], v86 offset:58368
v_mfma_f32_16x16x32_bf16 v[32:35], v[204:207], a[136:139], v[32:35]
v_mfma_f32_16x16x32_bf16 v[36:39], v[200:203], a[132:135], v[36:39]
ds_read_b128 v[200:203], v87 offset:16384
v_mfma_f32_16x16x32_bf16 v[36:39], v[204:207], a[140:143], v[36:39]
buffer_load_dwordx4 a[252:255], v3, s[52:55], s100 offen offset:0 sc1
v_mfma_f32_16x16x32_bf16 v[40:43], v[208:211], a[128:131], v[40:43]
s_waitcnt lgkmcnt(2)
ds_read_b128 v[204:207], v87 offset:17408
v_mfma_f32_16x16x32_bf16 v[40:43], v[212:215], a[136:139], v[40:43]
v_mfma_f32_16x16x32_bf16 v[44:47], v[208:211], a[132:135], v[44:47]
ds_read_b128 v[208:211], v1 offset:18432
v_mfma_f32_16x16x32_bf16 v[44:47], v[212:215], a[140:143], v[44:47]
buffer_load_dwordx4 v[232:235], v3, s[52:55], s101 offen offset:0 sc1
v_mfma_f32_16x16x32_bf16 v[48:51], v[184:187], a[128:131], v[48:51]
ds_read_b128 v[212:215], v1 offset:19456
v_mfma_f32_16x16x32_bf16 v[48:51], v[188:191], a[136:139], v[48:51]
v_mfma_f32_16x16x32_bf16 v[52:55], v[184:187], a[132:135], v[52:55]
ds_read_b128 v[184:187], v1 offset:38912
v_mfma_f32_16x16x32_bf16 v[52:55], v[188:191], a[140:143], v[52:55]
buffer_load_dwordx4 v[236:239], v3, s[52:55], s82 offen offset:1024 sc1
v_mfma_f32_16x16x32_bf16 v[56:59], v[192:195], a[128:131], v[56:59]
s_waitcnt lgkmcnt(2)
ds_read_b128 v[188:191], v1 offset:39936
v_mfma_f32_16x16x32_bf16 v[56:59], v[196:199], a[136:139], v[56:59]
v_mfma_f32_16x16x32_bf16 v[60:63], v[192:195], a[132:135], v[60:63]
ds_read_b128 v[192:195], v1 offset:59392
v_mfma_f32_16x16x32_bf16 v[60:63], v[196:199], a[140:143], v[60:63]
buffer_load_dwordx4 v[240:243], v3, s[52:55], s90 offen offset:1024 sc1
v_mfma_f32_16x16x32_bf16 v[64:67], v[200:203], a[128:131], v[64:67]
ds_read_b128 v[196:199], v1 offset:60416
v_mfma_f32_16x16x32_bf16 v[64:67], v[204:207], a[136:139], v[64:67]
v_mfma_f32_16x16x32_bf16 v[68:71], v[200:203], a[132:135], v[68:71]
s_waitcnt vmcnt(22)
ds_read_b128 v[200:203], v86 offset:18432
v_mfma_f32_16x16x32_bf16 v[68:71], v[204:207], a[140:143], v[68:71]
buffer_load_dwordx4 v[244:247], v3, s[52:55], s91 offen offset:1024 sc1
v_mfma_f32_16x16x32_bf16 v[16:19], v[208:211], a[144:147], v[16:19]
s_waitcnt vmcnt(21) lgkmcnt(2)
ds_read_b128 v[204:207], v86 offset:19456
v_mfma_f32_16x16x32_bf16 v[16:19], v[212:215], a[152:155], v[16:19]
v_mfma_f32_16x16x32_bf16 v[20:23], v[208:211], a[148:151], v[20:23]
s_waitcnt vmcnt(20)
ds_read_b128 v[208:211], v86 offset:38912
v_mfma_f32_16x16x32_bf16 v[20:23], v[212:215], a[156:159], v[20:23]
buffer_load_dwordx4 v[248:251], v3, s[52:55], s100 offen offset:1024 sc1
v_mfma_f32_16x16x32_bf16 v[24:27], v[184:187], a[144:147], v[24:27]
ds_read_b128 v[212:215], v86 offset:39936
v_mfma_f32_16x16x32_bf16 v[24:27], v[188:191], a[152:155], v[24:27]
v_mfma_f32_16x16x32_bf16 v[28:31], v[184:187], a[148:151], v[28:31]
ds_read_b128 v[184:187], v86 offset:59392
v_mfma_f32_16x16x32_bf16 v[28:31], v[188:191], a[156:159], v[28:31]
buffer_load_dwordx4 v[252:255], v3, s[52:55], s101 offen offset:1024 sc1
v_mfma_f32_16x16x32_bf16 v[32:35], v[192:195], a[144:147], v[32:35]
s_waitcnt lgkmcnt(2)
ds_read_b128 v[188:191], v86 offset:60416
v_mfma_f32_16x16x32_bf16 v[32:35], v[196:199], a[152:155], v[32:35]
v_mfma_f32_16x16x32_bf16 v[36:39], v[192:195], a[148:151], v[36:39]
ds_read_b128 v[192:195], v87 offset:18432
v_mfma_f32_16x16x32_bf16 v[36:39], v[196:199], a[156:159], v[36:39]
buffer_load_dwordx4 a[0:3], v3, s[52:55], s82 offen offset:2048 sc1
v_mfma_f32_16x16x32_bf16 v[40:43], v[200:203], a[144:147], v[40:43]
ds_read_b128 v[196:199], v87 offset:19456
v_mfma_f32_16x16x32_bf16 v[40:43], v[204:207], a[152:155], v[40:43]
v_mfma_f32_16x16x32_bf16 v[44:47], v[200:203], a[148:151], v[44:47]
v_mfma_f32_16x16x32_bf16 v[44:47], v[204:207], a[156:159], v[44:47]
buffer_load_dwordx4 a[4:7], v3, s[52:55], s90 offen offset:2048 sc1
v_mfma_f32_16x16x32_bf16 v[48:51], v[208:211], a[144:147], v[48:51]
s_waitcnt lgkmcnt(1)
v_mfma_f32_16x16x32_bf16 v[48:51], v[212:215], a[152:155], v[48:51]
v_mfma_f32_16x16x32_bf16 v[52:55], v[208:211], a[148:151], v[52:55]
v_mfma_f32_16x16x32_bf16 v[52:55], v[212:215], a[156:159], v[52:55]
buffer_load_dwordx4 a[8:11], v3, s[52:55], s91 offen offset:2048 sc1
v_mfma_f32_16x16x32_bf16 v[56:59], v[184:187], a[144:147], v[56:59]
v_mfma_f32_16x16x32_bf16 v[56:59], v[188:191], a[152:155], v[56:59]
v_mfma_f32_16x16x32_bf16 v[60:63], v[184:187], a[148:151], v[60:63]
v_mfma_f32_16x16x32_bf16 v[60:63], v[188:191], a[156:159], v[60:63]
buffer_load_dwordx4 a[12:15], v3, s[52:55], s100 offen offset:2048 sc1
v_mfma_f32_16x16x32_bf16 v[64:67], v[192:195], a[144:147], v[64:67]
s_waitcnt lgkmcnt(0)
v_mfma_f32_16x16x32_bf16 v[64:67], v[196:199], a[152:155], v[64:67]
v_mfma_f32_16x16x32_bf16 v[68:71], v[192:195], a[148:151], v[68:71]
v_mfma_f32_16x16x32_bf16 v[68:71], v[196:199], a[156:159], v[68:71]
buffer_load_dwordx4 a[16:19], v3, s[52:55], s101 offen offset:2048 sc1
buffer_load_dwordx4 a[20:23], v3, s[52:55], s82 offen offset:3072 sc1
buffer_load_dwordx4 a[24:27], v3, s[52:55], s90 offen offset:3072 sc1
buffer_load_dwordx4 a[28:31], v3, s[52:55], s91 offen offset:3072 sc1
buffer_load_dwordx4 a[32:35], v3, s[52:55], s100 offen offset:3072 sc1
buffer_load_dwordx4 a[36:39], v3, s[52:55], s101 offen offset:3072 sc1
)ASM"
R"ASM(
v_pk_add_f32 v[16:17], v[16:17], v[128:129]
v_pk_add_f32 v[18:19], v[18:19], v[130:131]
v_pk_add_f32 v[24:25], v[24:25], v[136:137]
v_pk_add_f32 v[26:27], v[26:27], v[138:139]
v_pk_mul_f32 v[224:225], v[16:17], v[88:89]
v_pk_mul_f32 v[226:227], v[18:19], v[88:89]
v_pk_mul_f32 v[228:229], v[24:25], v[88:89]
v_pk_mul_f32 v[230:231], v[26:27], v[88:89]
v_exp_f32 v224, v224
v_exp_f32 v225, v225
v_exp_f32 v226, v226
v_exp_f32 v227, v227
v_exp_f32 v228, v228
v_exp_f32 v229, v229
v_exp_f32 v230, v230
v_exp_f32 v231, v231
v_pk_fma_f32 v[224:225], v[224:225], v[82:83], v[82:83]
v_pk_fma_f32 v[226:227], v[226:227], v[82:83], v[82:83]
v_pk_fma_f32 v[228:229], v[228:229], v[82:83], v[82:83]
v_pk_fma_f32 v[230:231], v[230:231], v[82:83], v[82:83]
v_rcp_f32 v224, v224
v_rcp_f32 v225, v225
v_rcp_f32 v226, v226
v_rcp_f32 v227, v227
v_rcp_f32 v228, v228
v_rcp_f32 v229, v229
v_rcp_f32 v230, v230
v_rcp_f32 v231, v231
v_pk_mul_f32 v[16:17], v[16:17], v[224:225]
v_pk_mul_f32 v[18:19], v[18:19], v[226:227]
v_pk_mul_f32 v[24:25], v[24:25], v[228:229]
v_pk_mul_f32 v[26:27], v[26:27], v[230:231]
v_cvt_pk_bf16_f32 v16, v16, v17
v_cvt_pk_bf16_f32 v17, v18, v19
v_cvt_pk_bf16_f32 v18, v24, v25
v_cvt_pk_bf16_f32 v19, v26, v27
buffer_store_dwordx4 v[16:19], v1, s[52:55], s88 offen offset:0 sc1
v_pk_add_f32 v[20:21], v[20:21], v[132:133]
v_pk_add_f32 v[22:23], v[22:23], v[134:135]
v_pk_add_f32 v[28:29], v[28:29], v[140:141]
v_pk_add_f32 v[30:31], v[30:31], v[142:143]
v_pk_mul_f32 v[224:225], v[20:21], v[90:91]
v_pk_mul_f32 v[226:227], v[22:23], v[90:91]
v_pk_mul_f32 v[228:229], v[28:29], v[90:91]
v_pk_mul_f32 v[230:231], v[30:31], v[90:91]
v_exp_f32 v224, v224
v_exp_f32 v225, v225
v_exp_f32 v226, v226
v_exp_f32 v227, v227
v_exp_f32 v228, v228
v_exp_f32 v229, v229
v_exp_f32 v230, v230
v_exp_f32 v231, v231
v_pk_fma_f32 v[224:225], v[224:225], v[84:85], v[84:85]
v_pk_fma_f32 v[226:227], v[226:227], v[84:85], v[84:85]
v_pk_fma_f32 v[228:229], v[228:229], v[84:85], v[84:85]
v_pk_fma_f32 v[230:231], v[230:231], v[84:85], v[84:85]
v_rcp_f32 v224, v224
v_rcp_f32 v225, v225
v_rcp_f32 v226, v226
v_rcp_f32 v227, v227
v_rcp_f32 v228, v228
v_rcp_f32 v229, v229
v_rcp_f32 v230, v230
v_rcp_f32 v231, v231
v_pk_mul_f32 v[20:21], v[20:21], v[224:225]
v_pk_mul_f32 v[22:23], v[22:23], v[226:227]
v_pk_mul_f32 v[28:29], v[28:29], v[228:229]
v_pk_mul_f32 v[30:31], v[30:31], v[230:231]
v_cvt_pk_bf16_f32 v20, v20, v21
v_cvt_pk_bf16_f32 v21, v22, v23
v_cvt_pk_bf16_f32 v22, v28, v29
v_cvt_pk_bf16_f32 v23, v30, v31
buffer_store_dwordx4 v[20:23], v1, s[52:55], s83 offen offset:0 sc1
v_pk_add_f32 v[32:33], v[32:33], v[144:145]
v_pk_add_f32 v[34:35], v[34:35], v[146:147]
v_pk_add_f32 v[40:41], v[40:41], v[152:153]
v_pk_add_f32 v[42:43], v[42:43], v[154:155]
v_pk_mul_f32 v[224:225], v[32:33], v[88:89]
v_pk_mul_f32 v[226:227], v[34:35], v[88:89]
v_pk_mul_f32 v[228:229], v[40:41], v[88:89]
v_pk_mul_f32 v[230:231], v[42:43], v[88:89]
v_exp_f32 v224, v224
v_exp_f32 v225, v225
v_exp_f32 v226, v226
v_exp_f32 v227, v227
v_exp_f32 v228, v228
v_exp_f32 v229, v229
v_exp_f32 v230, v230
v_exp_f32 v231, v231
v_pk_fma_f32 v[224:225], v[224:225], v[82:83], v[82:83]
v_pk_fma_f32 v[226:227], v[226:227], v[82:83], v[82:83]
v_pk_fma_f32 v[228:229], v[228:229], v[82:83], v[82:83]
v_pk_fma_f32 v[230:231], v[230:231], v[82:83], v[82:83]
v_rcp_f32 v224, v224
v_rcp_f32 v225, v225
v_rcp_f32 v226, v226
v_rcp_f32 v227, v227
v_rcp_f32 v228, v228
v_rcp_f32 v229, v229
v_rcp_f32 v230, v230
v_rcp_f32 v231, v231
v_pk_mul_f32 v[32:33], v[32:33], v[224:225]
v_pk_mul_f32 v[34:35], v[34:35], v[226:227]
v_pk_mul_f32 v[40:41], v[40:41], v[228:229]
v_pk_mul_f32 v[42:43], v[42:43], v[230:231]
v_cvt_pk_bf16_f32 v32, v32, v33
v_cvt_pk_bf16_f32 v33, v34, v35
v_cvt_pk_bf16_f32 v34, v40, v41
v_cvt_pk_bf16_f32 v35, v42, v43
buffer_store_dwordx4 v[32:35], v1, s[52:55], s88 offen offset:1024 sc1
v_pk_add_f32 v[36:37], v[36:37], v[148:149]
v_pk_add_f32 v[38:39], v[38:39], v[150:151]
v_pk_add_f32 v[44:45], v[44:45], v[156:157]
v_pk_add_f32 v[46:47], v[46:47], v[158:159]
v_pk_mul_f32 v[224:225], v[36:37], v[90:91]
v_pk_mul_f32 v[226:227], v[38:39], v[90:91]
v_pk_mul_f32 v[228:229], v[44:45], v[90:91]
v_pk_mul_f32 v[230:231], v[46:47], v[90:91]
v_exp_f32 v224, v224
v_exp_f32 v225, v225
v_exp_f32 v226, v226
v_exp_f32 v227, v227
v_exp_f32 v228, v228
v_exp_f32 v229, v229
v_exp_f32 v230, v230
v_exp_f32 v231, v231
v_pk_fma_f32 v[224:225], v[224:225], v[84:85], v[84:85]
v_pk_fma_f32 v[226:227], v[226:227], v[84:85], v[84:85]
v_pk_fma_f32 v[228:229], v[228:229], v[84:85], v[84:85]
v_pk_fma_f32 v[230:231], v[230:231], v[84:85], v[84:85]
v_rcp_f32 v224, v224
v_rcp_f32 v225, v225
v_rcp_f32 v226, v226
v_rcp_f32 v227, v227
v_rcp_f32 v228, v228
v_rcp_f32 v229, v229
v_rcp_f32 v230, v230
v_rcp_f32 v231, v231
v_pk_mul_f32 v[36:37], v[36:37], v[224:225]
v_pk_mul_f32 v[38:39], v[38:39], v[226:227]
v_pk_mul_f32 v[44:45], v[44:45], v[228:229]
v_pk_mul_f32 v[46:47], v[46:47], v[230:231]
v_cvt_pk_bf16_f32 v36, v36, v37
v_cvt_pk_bf16_f32 v37, v38, v39
v_cvt_pk_bf16_f32 v38, v44, v45
v_cvt_pk_bf16_f32 v39, v46, v47
buffer_store_dwordx4 v[36:39], v1, s[52:55], s83 offen offset:1024 sc1
v_pk_add_f32 v[48:49], v[48:49], v[160:161]
v_pk_add_f32 v[50:51], v[50:51], v[162:163]
v_pk_add_f32 v[56:57], v[56:57], v[168:169]
v_pk_add_f32 v[58:59], v[58:59], v[170:171]
v_pk_mul_f32 v[224:225], v[48:49], v[88:89]
v_pk_mul_f32 v[226:227], v[50:51], v[88:89]
v_pk_mul_f32 v[228:229], v[56:57], v[88:89]
v_pk_mul_f32 v[230:231], v[58:59], v[88:89]
v_exp_f32 v224, v224
v_exp_f32 v225, v225
v_exp_f32 v226, v226
v_exp_f32 v227, v227
v_exp_f32 v228, v228
v_exp_f32 v229, v229
v_exp_f32 v230, v230
v_exp_f32 v231, v231
v_pk_fma_f32 v[224:225], v[224:225], v[82:83], v[82:83]
v_pk_fma_f32 v[226:227], v[226:227], v[82:83], v[82:83]
v_pk_fma_f32 v[228:229], v[228:229], v[82:83], v[82:83]
v_pk_fma_f32 v[230:231], v[230:231], v[82:83], v[82:83]
v_rcp_f32 v224, v224
v_rcp_f32 v225, v225
v_rcp_f32 v226, v226
v_rcp_f32 v227, v227
v_rcp_f32 v228, v228
v_rcp_f32 v229, v229
v_rcp_f32 v230, v230
v_rcp_f32 v231, v231
v_pk_mul_f32 v[48:49], v[48:49], v[224:225]
v_pk_mul_f32 v[50:51], v[50:51], v[226:227]
v_pk_mul_f32 v[56:57], v[56:57], v[228:229]
v_pk_mul_f32 v[58:59], v[58:59], v[230:231]
v_cvt_pk_bf16_f32 v48, v48, v49
v_cvt_pk_bf16_f32 v49, v50, v51
v_cvt_pk_bf16_f32 v50, v56, v57
v_cvt_pk_bf16_f32 v51, v58, v59
buffer_store_dwordx4 v[48:51], v1, s[52:55], s88 offen offset:2048 sc1
v_pk_add_f32 v[52:53], v[52:53], v[164:165]
v_pk_add_f32 v[54:55], v[54:55], v[166:167]
v_pk_add_f32 v[60:61], v[60:61], v[172:173]
v_pk_add_f32 v[62:63], v[62:63], v[174:175]
v_pk_mul_f32 v[224:225], v[52:53], v[90:91]
v_pk_mul_f32 v[226:227], v[54:55], v[90:91]
v_pk_mul_f32 v[228:229], v[60:61], v[90:91]
v_pk_mul_f32 v[230:231], v[62:63], v[90:91]
v_exp_f32 v224, v224
v_exp_f32 v225, v225
v_exp_f32 v226, v226
v_exp_f32 v227, v227
v_exp_f32 v228, v228
v_exp_f32 v229, v229
v_exp_f32 v230, v230
v_exp_f32 v231, v231
v_pk_fma_f32 v[224:225], v[224:225], v[84:85], v[84:85]
v_pk_fma_f32 v[226:227], v[226:227], v[84:85], v[84:85]
v_pk_fma_f32 v[228:229], v[228:229], v[84:85], v[84:85]
v_pk_fma_f32 v[230:231], v[230:231], v[84:85], v[84:85]
v_rcp_f32 v224, v224
v_rcp_f32 v225, v225
v_rcp_f32 v226, v226
v_rcp_f32 v227, v227
v_rcp_f32 v228, v228
v_rcp_f32 v229, v229
v_rcp_f32 v230, v230
v_rcp_f32 v231, v231
v_pk_mul_f32 v[52:53], v[52:53], v[224:225]
v_pk_mul_f32 v[54:55], v[54:55], v[226:227]
v_pk_mul_f32 v[60:61], v[60:61], v[228:229]
v_pk_mul_f32 v[62:63], v[62:63], v[230:231]
v_cvt_pk_bf16_f32 v52, v52, v53
v_cvt_pk_bf16_f32 v53, v54, v55
v_cvt_pk_bf16_f32 v54, v60, v61
v_cvt_pk_bf16_f32 v55, v62, v63
buffer_store_dwordx4 v[52:55], v1, s[52:55], s83 offen offset:2048 sc1
v_pk_add_f32 v[64:65], v[64:65], v[176:177]
v_pk_add_f32 v[66:67], v[66:67], v[178:179]
v_pk_mul_f32 v[224:225], v[64:65], v[88:89]
v_pk_mul_f32 v[226:227], v[66:67], v[88:89]
v_exp_f32 v224, v224
v_exp_f32 v225, v225
v_exp_f32 v226, v226
v_exp_f32 v227, v227
v_pk_fma_f32 v[224:225], v[224:225], v[82:83], v[82:83]
v_pk_fma_f32 v[226:227], v[226:227], v[82:83], v[82:83]
v_rcp_f32 v224, v224
v_rcp_f32 v225, v225
v_rcp_f32 v226, v226
v_rcp_f32 v227, v227
v_pk_mul_f32 v[64:65], v[64:65], v[224:225]
v_pk_mul_f32 v[66:67], v[66:67], v[226:227]
v_cvt_pk_bf16_f32 v64, v64, v65
v_cvt_pk_bf16_f32 v65, v66, v67
buffer_store_dwordx2 v[64:65], v127, s[52:55], s89 offen sc1
v_pk_add_f32 v[68:69], v[68:69], v[180:181]
v_pk_add_f32 v[70:71], v[70:71], v[182:183]
v_pk_mul_f32 v[224:225], v[68:69], v[90:91]
v_pk_mul_f32 v[226:227], v[70:71], v[90:91]
v_exp_f32 v224, v224
v_exp_f32 v225, v225
v_exp_f32 v226, v226
v_exp_f32 v227, v227
v_pk_fma_f32 v[224:225], v[224:225], v[84:85], v[84:85]
v_pk_fma_f32 v[226:227], v[226:227], v[84:85], v[84:85]
v_rcp_f32 v224, v224
v_rcp_f32 v225, v225
v_rcp_f32 v226, v226
v_rcp_f32 v227, v227
v_pk_mul_f32 v[68:69], v[68:69], v[224:225]
v_pk_mul_f32 v[70:71], v[70:71], v[226:227]
v_cvt_pk_bf16_f32 v68, v68, v69
v_cvt_pk_bf16_f32 v69, v70, v71
buffer_store_dwordx2 v[68:69], v127, s[52:55], s84 offen sc1
v_add_u32 v3, 8192, v1
buffer_load_dwordx4 a[40:43], v3, s[52:55], s82 offen offset:0 sc1
buffer_load_dwordx4 a[44:47], v3, s[52:55], s90 offen offset:0 sc1
buffer_load_dwordx4 a[48:51], v3, s[52:55], s91 offen offset:0 sc1
buffer_load_dwordx4 a[52:55], v3, s[52:55], s100 offen offset:0 sc1
buffer_load_dwordx4 a[56:59], v3, s[52:55], s101 offen offset:0 sc1
buffer_load_dwordx4 a[60:63], v3, s[52:55], s82 offen offset:1024 sc1
s_add_u32 s83, s60, 1
s_and_b32 s83, s83, 3
s_lshl_b32 s84, s80, 2
s_add_u32 s84, s84, s83
s_mul_i32 s84, s84, 55296
s_mul_i32 s85, s60, 14336
s_add_u32 s84, s84, s85
s_add_u32 s66, s84, 20971520
s_add_u32 s83, s60, 2
s_and_b32 s83, s83, 3
s_lshl_b32 s84, s80, 2
s_add_u32 s84, s84, s83
s_mul_i32 s84, s84, 55296
s_mul_i32 s85, s60, 14336
s_add_u32 s84, s84, s85
s_add_u32 s67, s84, 20971520
s_add_u32 s83, s60, 3
s_and_b32 s83, s83, 3
s_lshl_b32 s84, s80, 2
s_add_u32 s84, s84, s83
s_mul_i32 s84, s84, 55296
s_mul_i32 s85, s60, 14336
s_add_u32 s84, s84, s85
s_add_u32 s68, s84, 20971520
s_add_u32 s83, s60, 4
s_and_b32 s83, s83, 3
s_lshl_b32 s84, s80, 2
s_add_u32 s84, s84, s83
s_mul_i32 s84, s84, 55296
s_mul_i32 s85, s60, 14336
s_add_u32 s84, s84, s85
s_add_u32 s69, s84, 20971520
s_cmp_lt_u32 s60, 3
s_cselect_b32 s76, 0, 0x40000000
s_cselect_b32 s84, 3, 4
s_cselect_b32 s85, 512, 8
v_lshlrev_b32 v127, s84, v2
v_add_u32 v13, s85, v127
s_lshl_b32 s83, s78, 2
s_add_u32 s83, s83, s60
s_lshl_b32 s83, s83, 10
s_add_u32 s83, s83, 458752
v_and_b32 v6, 3, v2
v_lshlrev_b32 v6, 8, v6
v_add_u32 v6, s83, v6
v_mov_b32 v10, s64
v_mov_b32 v11, s65
s_add_u32 s86, s87, 458752
s_waitcnt vmcnt(6)
s_barrier
buffer_store_dwordx2 v[10:11], v5, s[52:55], s86 offen sc1
buffer_load_dwordx4 a[64:67], v3, s[52:55], s90 offen offset:1024 sc1
buffer_load_dwordx4 a[68:71], v3, s[52:55], s91 offen offset:1024 sc1
buffer_load_dwordx4 a[72:75], v3, s[52:55], s100 offen offset:1024 sc1
buffer_load_dwordx4 a[76:79], v3, s[52:55], s101 offen offset:1024 sc1
buffer_load_dwordx4 a[80:83], v3, s[52:55], s82 offen offset:2048 sc1
buffer_load_dwordx4 a[84:87], v3, s[52:55], s90 offen offset:2048 sc1
buffer_load_dwordx4 a[88:91], v3, s[52:55], s91 offen offset:2048 sc1
buffer_load_dwordx4 a[92:95], v3, s[52:55], s100 offen offset:2048 sc1
buffer_load_dwordx4 a[96:99], v3, s[52:55], s101 offen offset:2048 sc1
buffer_load_dwordx4 a[100:103], v3, s[52:55], s82 offen offset:3072 sc1
buffer_load_dwordx4 a[104:107], v3, s[52:55], s90 offen offset:3072 sc1
buffer_load_dwordx4 a[108:111], v3, s[52:55], s91 offen offset:3072 sc1
buffer_load_dwordx4 a[112:115], v3, s[52:55], s100 offen offset:3072 sc1
buffer_load_dwordx4 a[116:119], v3, s[52:55], s101 offen offset:3072 sc1
v_add_u32 v3, 12288, v1
buffer_load_dwordx4 a[120:123], v3, s[52:55], s82 offen offset:0 sc1
buffer_load_dwordx4 a[124:127], v3, s[52:55], s90 offen offset:0 sc1
buffer_load_dwordx4 a[128:131], v3, s[52:55], s91 offen offset:0 sc1
buffer_load_dwordx4 a[132:135], v3, s[52:55], s100 offen offset:0 sc1
buffer_load_dwordx4 a[136:139], v3, s[52:55], s101 offen offset:0 sc1
buffer_load_dwordx4 a[140:143], v3, s[52:55], s82 offen offset:1024 sc1
buffer_load_dwordx4 a[144:147], v3, s[52:55], s90 offen offset:1024 sc1
buffer_load_dwordx4 a[148:151], v3, s[52:55], s91 offen offset:1024 sc1
buffer_load_dwordx4 a[152:155], v3, s[52:55], s100 offen offset:1024 sc1
buffer_load_dwordx4 a[156:159], v3, s[52:55], s101 offen offset:1024 sc1
s_sleep 4
buffer_load_dwordx2 v[10:11], v6, s[52:55], 0 offen sc1
s_waitcnt vmcnt(0)
v_cmp_ne_u64 vcc, s[64:65], v[10:11]
s_nop 0
s_cbranch_vccnz L_stub_h1
L_ret_h1:
s_add_u32 s0, s66, 0
buffer_load_dwordx4 v[128:131], v1, s[52:55], s0 offen offset:0 sc1
buffer_load_dwordx4 v[132:135], v1, s[52:55], s0 offen offset:1024 sc1
buffer_load_dwordx4 v[136:139], v1, s[52:55], s0 offen offset:2048 sc1
buffer_load_dwordx2 v[140:141], v127, s[52:55], s0 offen offset:3072 sc1
L_pad_b_0_1:
s_cbranch_execz L_padret_b
buffer_load_dwordx2 v[142:143], v13, s[52:55], s0 offen offset:3072 sc1
s_add_u32 s0, s66, 4096
buffer_load_dwordx4 v[144:147], v1, s[52:55], s0 offen offset:0 sc1
buffer_load_dwordx4 v[148:151], v1, s[52:55], s0 offen offset:1024 sc1
buffer_load_dwordx4 v[152:155], v1, s[52:55], s0 offen offset:2048 sc1
buffer_load_dwordx4 v[156:159], v1, s[52:55], s0 offen offset:3072 sc1
s_add_u32 s0, s66, 8192
buffer_load_dwordx4 v[160:163], v1, s[52:55], s0 offen offset:0 sc1
buffer_load_dwordx4 v[164:167], v1, s[52:55], s0 offen offset:1024 sc1
buffer_load_dwordx2 v[168:169], v127, s[52:55], s0 offen offset:2048 sc1
buffer_load_dwordx2 v[170:171], v13, s[52:55], s0 offen offset:2048 sc1
buffer_load_dwordx4 v[172:175], v1, s[52:55], s0 offen offset:3072 sc1
s_waitcnt vmcnt(12)
v_mfma_f32_16x16x32_bf16 v[16:19], a[160:163], v[128:131], 0
s_add_u32 s0, s66, 12288
s_add_u32 s0, s0, s76
buffer_load_dwordx4 v[176:179], v1, s[52:55], s0 offen offset:0 sc1
v_mfma_f32_16x16x32_bf16 v[16:19], a[180:183], v[132:135], v[16:19]
buffer_load_dwordx4 v[180:183], v1, s[52:55], s0 offen offset:1024 sc1
v_mfma_f32_16x16x32_bf16 v[20:23], a[164:167], v[128:131], 0
s_add_u32 s0, s67, 0
v_and_b32 v57, 0xffff0000, v116
v_lshlrev_b32 v56, 16, v116
v_mfma_f32_16x16x32_bf16 v[20:23], a[184:187], v[132:135], v[20:23]
s_lshl_b32 s85, s60, 12
v_and_b32 v59, 0xffff0000, v117
v_lshlrev_b32 v58, 16, v117
v_mfma_f32_16x16x32_bf16 v[24:27], a[168:171], v[128:131], 0
s_add_u32 s83, s70, s75
v_and_b32 v61, 0xffff0000, v118
v_lshlrev_b32 v60, 16, v118
v_mfma_f32_16x16x32_bf16 v[24:27], a[188:191], v[132:135], v[24:27]
buffer_load_dwordx4 v[184:187], v1, s[52:55], s0 offen offset:0 sc1
v_mfma_f32_16x16x32_bf16 v[28:31], a[172:175], v[128:131], 0
s_add_u32 s84, s71, s75
v_and_b32 v63, 0xffff0000, v119
v_lshlrev_b32 v62, 16, v119
v_mfma_f32_16x16x32_bf16 v[28:31], a[192:195], v[132:135], v[28:31]
v_and_b32 v65, 0xffff0000, v120
v_lshlrev_b32 v64, 16, v120
v_mfma_f32_16x16x32_bf16 v[32:35], a[176:179], v[128:131], 0
s_waitcnt vmcnt(14)
v_and_b32 v67, 0xffff0000, v121
)ASM"
R"ASM(
v_lshlrev_b32 v66, 16, v121
v_mfma_f32_16x16x32_bf16 v[32:35], a[196:199], v[132:135], v[32:35]
buffer_load_dwordx4 v[188:191], v1, s[52:55], s0 offen offset:1024 sc1
v_mfma_f32_16x16x32_bf16 v[16:19], a[200:203], v[136:139], v[16:19]
s_waitcnt vmcnt(13)
v_and_b32 v69, 0xffff0000, v122
v_lshlrev_b32 v68, 16, v122
v_mfma_f32_16x16x32_bf16 v[16:19], a[220:223], v[140:143], v[16:19]
v_and_b32 v71, 0xffff0000, v123
v_lshlrev_b32 v70, 16, v123
v_mfma_f32_16x16x32_bf16 v[20:23], a[204:207], v[136:139], v[20:23]
v_and_b32 v73, 0xffff0000, v124
v_lshlrev_b32 v72, 16, v124
v_mfma_f32_16x16x32_bf16 v[20:23], a[224:227], v[140:143], v[20:23]
buffer_load_dwordx4 v[192:195], v1, s[52:55], s0 offen offset:2048 sc1
v_mfma_f32_16x16x32_bf16 v[24:27], a[208:211], v[136:139], v[24:27]
v_and_b32 v75, 0xffff0000, v125
v_lshlrev_b32 v74, 16, v125
v_mfma_f32_16x16x32_bf16 v[24:27], a[228:231], v[140:143], v[24:27]
v_lshrrev_b32 v226, 2, v2
v_and_b32 v227, 3, v2
v_mfma_f32_16x16x32_bf16 v[28:31], a[212:215], v[136:139], v[28:31]
v_lshlrev_b32 v227, 4, v227
v_mul_u32_u24 v225, 176, v226
v_mfma_f32_16x16x32_bf16 v[28:31], a[232:235], v[140:143], v[28:31]
buffer_load_dwordx2 v[196:197], v127, s[52:55], s0 offen offset:3072 sc1
v_mfma_f32_16x16x32_bf16 v[32:35], a[216:219], v[136:139], v[32:35]
v_and_b32 v224, 15, v2
v_mul_u32_u24 v226, 1280, v226
v_mfma_f32_16x16x32_bf16 v[32:35], a[236:239], v[140:143], v[32:35]
s_waitcnt vmcnt(14)
v_add3_u32 v225, v225, v227, s85
v_add_u32 v226, v226, v227
v_mfma_f32_16x16x32_bf16 v[16:19], a[240:243], v[144:147], v[16:19]
s_waitcnt vmcnt(13)
v_mul_u32_u24 v224, 176, v224
v_and_b32 v227, 2, v2
v_mfma_f32_16x16x32_bf16 v[16:19], v[236:239], v[148:151], v[16:19]
buffer_load_dwordx2 v[198:199], v13, s[52:55], s0 offen offset:3072 sc1
v_mfma_f32_16x16x32_bf16 v[20:23], a[244:247], v[144:147], v[20:23]
s_add_u32 s0, s67, 4096
v_add3_u32 v224, v224, v12, s85
v_lshl_add_u32 v227, v227, 28, v226
v_mfma_f32_16x16x32_bf16 v[20:23], v[240:243], v[148:151], v[20:23]
v_mfma_f32_16x16x32_bf16 v[24:27], a[248:251], v[144:147], v[24:27]
v_mfma_f32_16x16x32_bf16 v[24:27], v[244:247], v[148:151], v[24:27]
buffer_load_dwordx4 v[200:203], v1, s[52:55], s0 offen offset:0 sc1
v_mfma_f32_16x16x32_bf16 v[28:31], a[252:255], v[144:147], v[28:31]
v_mfma_f32_16x16x32_bf16 v[28:31], v[248:251], v[148:151], v[28:31]
v_mfma_f32_16x16x32_bf16 v[32:35], v[232:235], v[144:147], v[32:35]
s_waitcnt vmcnt(14)
v_mfma_f32_16x16x32_bf16 v[32:35], v[252:255], v[148:151], v[32:35]
buffer_load_dwordx4 v[204:207], v1, s[52:55], s0 offen offset:1024 sc1
v_mfma_f32_16x16x32_bf16 v[16:19], a[0:3], v[152:155], v[16:19]
s_waitcnt vmcnt(14)
v_mfma_f32_16x16x32_bf16 v[16:19], a[20:23], v[156:159], v[16:19]
v_mfma_f32_16x16x32_bf16 v[20:23], a[4:7], v[152:155], v[20:23]
v_mfma_f32_16x16x32_bf16 v[20:23], a[24:27], v[156:159], v[20:23]
buffer_load_dwordx4 v[208:211], v1, s[52:55], s0 offen offset:2048 sc1
v_mfma_f32_16x16x32_bf16 v[24:27], a[8:11], v[152:155], v[24:27]
v_mfma_f32_16x16x32_bf16 v[24:27], a[28:31], v[156:159], v[24:27]
v_mfma_f32_16x16x32_bf16 v[28:31], a[12:15], v[152:155], v[28:31]
v_mfma_f32_16x16x32_bf16 v[28:31], a[32:35], v[156:159], v[28:31]
buffer_load_dwordx4 v[212:215], v1, s[52:55], s0 offen offset:3072 sc1
v_mfma_f32_16x16x32_bf16 v[32:35], a[16:19], v[152:155], v[32:35]
s_add_u32 s0, s67, 8192
v_mfma_f32_16x16x32_bf16 v[32:35], a[36:39], v[156:159], v[32:35]
s_waitcnt vmcnt(15)
v_mfma_f32_16x16x32_bf16 v[16:19], a[40:43], v[160:163], v[16:19]
s_waitcnt vmcnt(14)
v_mfma_f32_16x16x32_bf16 v[16:19], a[60:63], v[164:167], v[16:19]
buffer_load_dwordx4 v[216:219], v1, s[52:55], s0 offen offset:0 sc1
v_mfma_f32_16x16x32_bf16 v[20:23], a[44:47], v[160:163], v[20:23]
v_mfma_f32_16x16x32_bf16 v[20:23], a[64:67], v[164:167], v[20:23]
v_mfma_f32_16x16x32_bf16 v[24:27], a[48:51], v[160:163], v[24:27]
v_mfma_f32_16x16x32_bf16 v[24:27], a[68:71], v[164:167], v[24:27]
buffer_load_dwordx4 v[220:223], v1, s[52:55], s0 offen offset:1024 sc1
v_mfma_f32_16x16x32_bf16 v[28:31], a[52:55], v[160:163], v[28:31]
v_mfma_f32_16x16x32_bf16 v[28:31], a[72:75], v[164:167], v[28:31]
v_mfma_f32_16x16x32_bf16 v[32:35], a[56:59], v[160:163], v[32:35]
s_waitcnt vmcnt(14)
v_mfma_f32_16x16x32_bf16 v[32:35], a[76:79], v[164:167], v[32:35]
buffer_load_dwordx2 v[128:129], v127, s[52:55], s0 offen offset:2048 sc1
v_mfma_f32_16x16x32_bf16 v[16:19], a[80:83], v[168:171], v[16:19]
s_waitcnt vmcnt(14)
v_mfma_f32_16x16x32_bf16 v[16:19], a[100:103], v[172:175], v[16:19]
v_mfma_f32_16x16x32_bf16 v[20:23], a[84:87], v[168:171], v[20:23]
v_mfma_f32_16x16x32_bf16 v[20:23], a[104:107], v[172:175], v[20:23]
buffer_load_dwordx2 v[130:131], v13, s[52:55], s0 offen offset:2048 sc1
v_mfma_f32_16x16x32_bf16 v[24:27], a[88:91], v[168:171], v[24:27]
v_mfma_f32_16x16x32_bf16 v[24:27], a[108:111], v[172:175], v[24:27]
v_mfma_f32_16x16x32_bf16 v[28:31], a[92:95], v[168:171], v[28:31]
v_mfma_f32_16x16x32_bf16 v[28:31], a[112:115], v[172:175], v[28:31]
buffer_load_dwordx4 v[132:135], v1, s[52:55], s0 offen offset:3072 sc1
v_mfma_f32_16x16x32_bf16 v[32:35], a[96:99], v[168:171], v[32:35]
s_add_u32 s0, s67, 12288
s_add_u32 s0, s0, s76
v_mfma_f32_16x16x32_bf16 v[32:35], a[116:119], v[172:175], v[32:35]
s_waitcnt vmcnt(15)
v_mfma_f32_16x16x32_bf16 v[16:19], a[120:123], v[176:179], v[16:19]
s_waitcnt vmcnt(14)
v_mfma_f32_16x16x32_bf16 v[16:19], a[140:143], v[180:183], v[16:19]
buffer_load_dwordx4 v[136:139], v1, s[52:55], s0 offen offset:0 sc1
v_mfma_f32_16x16x32_bf16 v[20:23], a[124:127], v[176:179], v[20:23]
v_mfma_f32_16x16x32_bf16 v[20:23], a[144:147], v[180:183], v[20:23]
v_mfma_f32_16x16x32_bf16 v[24:27], a[128:131], v[176:179], v[24:27]
v_mfma_f32_16x16x32_bf16 v[24:27], a[148:151], v[180:183], v[24:27]
buffer_load_dwordx4 v[140:143], v1, s[52:55], s0 offen offset:1024 sc1
v_mfma_f32_16x16x32_bf16 v[28:31], a[132:135], v[176:179], v[28:31]
s_add_u32 s0, s68, 0
v_mfma_f32_16x16x32_bf16 v[28:31], a[152:155], v[180:183], v[28:31]
v_mfma_f32_16x16x32_bf16 v[32:35], a[136:139], v[176:179], v[32:35]
s_waitcnt vmcnt(15)
v_mfma_f32_16x16x32_bf16 v[32:35], a[156:159], v[180:183], v[32:35]
buffer_load_dwordx4 v[144:147], v1, s[52:55], s0 offen offset:0 sc1
v_mfma_f32_16x16x32_bf16 v[36:39], a[160:163], v[184:187], 0
s_waitcnt vmcnt(15)
ds_write_b128 v4, v[16:19] offset:0
v_mfma_f32_16x16x32_bf16 v[36:39], a[180:183], v[188:191], v[36:39]
ds_write_b128 v4, v[20:23] offset:1024
v_mfma_f32_16x16x32_bf16 v[40:43], a[164:167], v[184:187], 0
v_mfma_f32_16x16x32_bf16 v[40:43], a[184:187], v[188:191], v[40:43]
buffer_load_dwordx4 v[148:151], v1, s[52:55], s0 offen offset:1024 sc1
v_mfma_f32_16x16x32_bf16 v[44:47], a[168:171], v[184:187], 0
ds_write_b128 v4, v[24:27] offset:2048
v_mfma_f32_16x16x32_bf16 v[44:47], a[188:191], v[188:191], v[44:47]
ds_write_b128 v4, v[28:31] offset:3072
v_mfma_f32_16x16x32_bf16 v[48:51], a[172:175], v[184:187], 0
v_mfma_f32_16x16x32_bf16 v[48:51], a[192:195], v[188:191], v[48:51]
buffer_load_dwordx4 v[152:155], v1, s[52:55], s0 offen offset:2048 sc1
v_mfma_f32_16x16x32_bf16 v[52:55], a[176:179], v[184:187], 0
ds_write_b128 v4, v[32:35] offset:4096
v_mfma_f32_16x16x32_bf16 v[52:55], a[196:199], v[188:191], v[52:55]
s_waitcnt vmcnt(16)
v_mfma_f32_16x16x32_bf16 v[36:39], a[200:203], v[192:195], v[36:39]
s_waitcnt vmcnt(14)
v_mfma_f32_16x16x32_bf16 v[36:39], a[220:223], v[196:199], v[36:39]
buffer_load_dwordx2 v[156:157], v127, s[52:55], s0 offen offset:3072 sc1
v_mfma_f32_16x16x32_bf16 v[40:43], a[204:207], v[192:195], v[40:43]
v_mfma_f32_16x16x32_bf16 v[40:43], a[224:227], v[196:199], v[40:43]
v_mfma_f32_16x16x32_bf16 v[44:47], a[208:211], v[192:195], v[44:47]
v_mfma_f32_16x16x32_bf16 v[44:47], a[228:231], v[196:199], v[44:47]
buffer_load_dwordx2 v[158:159], v13, s[52:55], s0 offen offset:3072 sc1
v_mfma_f32_16x16x32_bf16 v[48:51], a[212:215], v[192:195], v[48:51]
s_add_u32 s0, s68, 4096
v_mfma_f32_16x16x32_bf16 v[48:51], a[232:235], v[196:199], v[48:51]
v_mfma_f32_16x16x32_bf16 v[52:55], a[216:219], v[192:195], v[52:55]
s_waitcnt vmcnt(15)
v_mfma_f32_16x16x32_bf16 v[52:55], a[236:239], v[196:199], v[52:55]
buffer_load_dwordx4 v[160:163], v1, s[52:55], s0 offen offset:0 sc1
v_mfma_f32_16x16x32_bf16 v[36:39], a[240:243], v[200:203], v[36:39]
s_waitcnt vmcnt(15)
v_mfma_f32_16x16x32_bf16 v[36:39], v[236:239], v[204:207], v[36:39]
v_mfma_f32_16x16x32_bf16 v[40:43], a[244:247], v[200:203], v[40:43]
v_mfma_f32_16x16x32_bf16 v[40:43], v[240:243], v[204:207], v[40:43]
buffer_load_dwordx4 v[164:167], v1, s[52:55], s0 offen offset:1024 sc1
v_mfma_f32_16x16x32_bf16 v[44:47], a[248:251], v[200:203], v[44:47]
v_mfma_f32_16x16x32_bf16 v[44:47], v[244:247], v[204:207], v[44:47]
v_mfma_f32_16x16x32_bf16 v[48:51], a[252:255], v[200:203], v[48:51]
v_mfma_f32_16x16x32_bf16 v[48:51], v[248:251], v[204:207], v[48:51]
buffer_load_dwordx4 v[168:171], v1, s[52:55], s0 offen offset:2048 sc1
v_mfma_f32_16x16x32_bf16 v[52:55], v[232:235], v[200:203], v[52:55]
v_mfma_f32_16x16x32_bf16 v[52:55], v[252:255], v[204:207], v[52:55]
s_waitcnt vmcnt(16)
v_mfma_f32_16x16x32_bf16 v[36:39], a[0:3], v[208:211], v[36:39]
s_waitcnt vmcnt(15)
v_mfma_f32_16x16x32_bf16 v[36:39], a[20:23], v[212:215], v[36:39]
buffer_load_dwordx4 v[172:175], v1, s[52:55], s0 offen offset:3072 sc1
v_mfma_f32_16x16x32_bf16 v[40:43], a[4:7], v[208:211], v[40:43]
s_add_u32 s0, s68, 8192
v_mfma_f32_16x16x32_bf16 v[40:43], a[24:27], v[212:215], v[40:43]
v_mfma_f32_16x16x32_bf16 v[44:47], a[8:11], v[208:211], v[44:47]
v_mfma_f32_16x16x32_bf16 v[44:47], a[28:31], v[212:215], v[44:47]
buffer_load_dwordx4 v[176:179], v1, s[52:55], s0 offen offset:0 sc1
v_mfma_f32_16x16x32_bf16 v[48:51], a[12:15], v[208:211], v[48:51]
v_mfma_f32_16x16x32_bf16 v[48:51], a[32:35], v[212:215], v[48:51]
v_mfma_f32_16x16x32_bf16 v[52:55], a[16:19], v[208:211], v[52:55]
s_waitcnt vmcnt(16)
v_mfma_f32_16x16x32_bf16 v[52:55], a[36:39], v[212:215], v[52:55]
buffer_load_dwordx4 v[180:183], v1, s[52:55], s0 offen offset:1024 sc1
v_mfma_f32_16x16x32_bf16 v[36:39], a[40:43], v[216:219], v[36:39]
s_waitcnt vmcnt(16)
v_mfma_f32_16x16x32_bf16 v[36:39], a[60:63], v[220:223], v[36:39]
v_mfma_f32_16x16x32_bf16 v[40:43], a[44:47], v[216:219], v[40:43]
v_mfma_f32_16x16x32_bf16 v[40:43], a[64:67], v[220:223], v[40:43]
buffer_load_dwordx2 v[184:185], v127, s[52:55], s0 offen offset:2048 sc1
v_mfma_f32_16x16x32_bf16 v[44:47], a[48:51], v[216:219], v[44:47]
v_mfma_f32_16x16x32_bf16 v[44:47], a[68:71], v[220:223], v[44:47]
v_mfma_f32_16x16x32_bf16 v[48:51], a[52:55], v[216:219], v[48:51]
v_mfma_f32_16x16x32_bf16 v[48:51], a[72:75], v[220:223], v[48:51]
buffer_load_dwordx2 v[186:187], v13, s[52:55], s0 offen offset:2048 sc1
v_mfma_f32_16x16x32_bf16 v[52:55], a[56:59], v[216:219], v[52:55]
v_mfma_f32_16x16x32_bf16 v[52:55], a[76:79], v[220:223], v[52:55]
s_waitcnt vmcnt(16)
v_mfma_f32_16x16x32_bf16 v[36:39], a[80:83], v[128:131], v[36:39]
s_waitcnt vmcnt(15)
v_mfma_f32_16x16x32_bf16 v[36:39], a[100:103], v[132:135], v[36:39]
buffer_load_dwordx4 v[188:191], v1, s[52:55], s0 offen offset:3072 sc1
v_mfma_f32_16x16x32_bf16 v[40:43], a[84:87], v[128:131], v[40:43]
s_add_u32 s0, s68, 12288
s_add_u32 s0, s0, s76
v_mfma_f32_16x16x32_bf16 v[40:43], a[104:107], v[132:135], v[40:43]
v_mfma_f32_16x16x32_bf16 v[44:47], a[88:91], v[128:131], v[44:47]
v_mfma_f32_16x16x32_bf16 v[44:47], a[108:111], v[132:135], v[44:47]
buffer_load_dwordx4 v[192:195], v1, s[52:55], s0 offen offset:0 sc1
v_mfma_f32_16x16x32_bf16 v[48:51], a[92:95], v[128:131], v[48:51]
v_mfma_f32_16x16x32_bf16 v[48:51], a[112:115], v[132:135], v[48:51]
v_mfma_f32_16x16x32_bf16 v[52:55], a[96:99], v[128:131], v[52:55]
s_waitcnt vmcnt(16)
v_mfma_f32_16x16x32_bf16 v[52:55], a[116:119], v[132:135], v[52:55]
buffer_load_dwordx4 v[196:199], v1, s[52:55], s0 offen offset:1024 sc1
v_mfma_f32_16x16x32_bf16 v[36:39], a[120:123], v[136:139], v[36:39]
s_waitcnt vmcnt(16)
s_add_u32 s0, s69, 0
v_mfma_f32_16x16x32_bf16 v[36:39], a[140:143], v[140:143], v[36:39]
v_mfma_f32_16x16x32_bf16 v[40:43], a[124:127], v[136:139], v[40:43]
v_mfma_f32_16x16x32_bf16 v[40:43], a[144:147], v[140:143], v[40:43]
buffer_load_dwordx4 v[200:203], v1, s[52:55], s0 offen offset:0 sc1
v_mfma_f32_16x16x32_bf16 v[44:47], a[128:131], v[136:139], v[44:47]
v_mfma_f32_16x16x32_bf16 v[44:47], a[148:151], v[140:143], v[44:47]
v_mfma_f32_16x16x32_bf16 v[48:51], a[132:135], v[136:139], v[48:51]
v_mfma_f32_16x16x32_bf16 v[48:51], a[152:155], v[140:143], v[48:51]
buffer_load_dwordx4 v[204:207], v1, s[52:55], s0 offen offset:1024 sc1
v_mfma_f32_16x16x32_bf16 v[52:55], a[136:139], v[136:139], v[52:55]
v_mfma_f32_16x16x32_bf16 v[52:55], a[156:159], v[140:143], v[52:55]
s_waitcnt vmcnt(17)
ds_write_b128 v7, v[36:39] offset:0
v_mfma_f32_16x16x32_bf16 v[16:19], a[160:163], v[144:147], 0
s_waitcnt vmcnt(16)
v_mfma_f32_16x16x32_bf16 v[16:19], a[180:183], v[148:151], v[16:19]
buffer_load_dwordx4 v[208:211], v1, s[52:55], s0 offen offset:2048 sc1
v_mfma_f32_16x16x32_bf16 v[20:23], a[164:167], v[144:147], 0
ds_write_b128 v7, v[40:43] offset:1024
v_mfma_f32_16x16x32_bf16 v[20:23], a[184:187], v[148:151], v[20:23]
ds_write_b128 v7, v[44:47] offset:2048
v_mfma_f32_16x16x32_bf16 v[24:27], a[168:171], v[144:147], 0
v_mfma_f32_16x16x32_bf16 v[24:27], a[188:191], v[148:151], v[24:27]
buffer_load_dwordx2 v[212:213], v127, s[52:55], s0 offen offset:3072 sc1
v_mfma_f32_16x16x32_bf16 v[28:31], a[172:175], v[144:147], 0
ds_write_b128 v7, v[48:51] offset:3072
v_mfma_f32_16x16x32_bf16 v[28:31], a[192:195], v[148:151], v[28:31]
ds_write_b128 v7, v[52:55] offset:4096
v_mfma_f32_16x16x32_bf16 v[32:35], a[176:179], v[144:147], 0
s_waitcnt vmcnt(17)
v_mfma_f32_16x16x32_bf16 v[32:35], a[196:199], v[148:151], v[32:35]
buffer_load_dwordx2 v[214:215], v13, s[52:55], s0 offen offset:3072 sc1
v_mfma_f32_16x16x32_bf16 v[16:19], a[200:203], v[152:155], v[16:19]
s_waitcnt vmcnt(16)
s_add_u32 s0, s69, 4096
v_mfma_f32_16x16x32_bf16 v[16:19], a[220:223], v[156:159], v[16:19]
v_mfma_f32_16x16x32_bf16 v[20:23], a[204:207], v[152:155], v[20:23]
v_mfma_f32_16x16x32_bf16 v[20:23], a[224:227], v[156:159], v[20:23]
buffer_load_dwordx4 v[216:219], v1, s[52:55], s0 offen offset:0 sc1
v_mfma_f32_16x16x32_bf16 v[24:27], a[208:211], v[152:155], v[24:27]
v_mfma_f32_16x16x32_bf16 v[24:27], a[228:231], v[156:159], v[24:27]
v_mfma_f32_16x16x32_bf16 v[28:31], a[212:215], v[152:155], v[28:31]
v_mfma_f32_16x16x32_bf16 v[28:31], a[232:235], v[156:159], v[28:31]
buffer_load_dwordx4 v[220:223], v1, s[52:55], s0 offen offset:1024 sc1
v_mfma_f32_16x16x32_bf16 v[32:35], a[216:219], v[152:155], v[32:35]
v_mfma_f32_16x16x32_bf16 v[32:35], a[236:239], v[156:159], v[32:35]
s_waitcnt vmcnt(17)
v_mfma_f32_16x16x32_bf16 v[16:19], a[240:243], v[160:163], v[16:19]
s_waitcnt vmcnt(16)
v_mfma_f32_16x16x32_bf16 v[16:19], v[236:239], v[164:167], v[16:19]
buffer_load_dwordx4 v[128:131], v1, s[52:55], s0 offen offset:2048 sc1
v_mfma_f32_16x16x32_bf16 v[20:23], a[244:247], v[160:163], v[20:23]
v_mfma_f32_16x16x32_bf16 v[20:23], v[240:243], v[164:167], v[20:23]
v_mfma_f32_16x16x32_bf16 v[24:27], a[248:251], v[160:163], v[24:27]
v_mfma_f32_16x16x32_bf16 v[24:27], v[244:247], v[164:167], v[24:27]
buffer_load_dwordx4 v[132:135], v1, s[52:55], s0 offen offset:3072 sc1
v_mfma_f32_16x16x32_bf16 v[28:31], a[252:255], v[160:163], v[28:31]
s_add_u32 s0, s69, 8192
v_mfma_f32_16x16x32_bf16 v[28:31], v[248:251], v[164:167], v[28:31]
v_mfma_f32_16x16x32_bf16 v[32:35], v[232:235], v[160:163], v[32:35]
s_waitcnt vmcnt(17)
v_mfma_f32_16x16x32_bf16 v[32:35], v[252:255], v[164:167], v[32:35]
buffer_load_dwordx4 v[136:139], v1, s[52:55], s0 offen offset:0 sc1
v_mfma_f32_16x16x32_bf16 v[16:19], a[0:3], v[168:171], v[16:19]
s_waitcnt vmcnt(17)
v_mfma_f32_16x16x32_bf16 v[16:19], a[20:23], v[172:175], v[16:19]
v_mfma_f32_16x16x32_bf16 v[20:23], a[4:7], v[168:171], v[20:23]
v_mfma_f32_16x16x32_bf16 v[20:23], a[24:27], v[172:175], v[20:23]
buffer_load_dwordx4 v[140:143], v1, s[52:55], s0 offen offset:1024 sc1
v_mfma_f32_16x16x32_bf16 v[24:27], a[8:11], v[168:171], v[24:27]
v_mfma_f32_16x16x32_bf16 v[24:27], a[28:31], v[172:175], v[24:27]
v_mfma_f32_16x16x32_bf16 v[28:31], a[12:15], v[168:171], v[28:31]
v_mfma_f32_16x16x32_bf16 v[28:31], a[32:35], v[172:175], v[28:31]
buffer_load_dwordx2 v[144:145], v127, s[52:55], s0 offen offset:2048 sc1
v_mfma_f32_16x16x32_bf16 v[32:35], a[16:19], v[168:171], v[32:35]
v_mfma_f32_16x16x32_bf16 v[32:35], a[36:39], v[172:175], v[32:35]
s_waitcnt vmcnt(18)
v_mfma_f32_16x16x32_bf16 v[16:19], a[40:43], v[176:179], v[16:19]
s_waitcnt vmcnt(17)
v_mfma_f32_16x16x32_bf16 v[16:19], a[60:63], v[180:183], v[16:19]
buffer_load_dwordx2 v[146:147], v13, s[52:55], s0 offen offset:2048 sc1
v_mfma_f32_16x16x32_bf16 v[20:23], a[44:47], v[176:179], v[20:23]
v_mfma_f32_16x16x32_bf16 v[20:23], a[64:67], v[180:183], v[20:23]
v_mfma_f32_16x16x32_bf16 v[24:27], a[48:51], v[176:179], v[24:27]
v_mfma_f32_16x16x32_bf16 v[24:27], a[68:71], v[180:183], v[24:27]
buffer_load_dwordx4 v[148:151], v1, s[52:55], s0 offen offset:3072 sc1
v_mfma_f32_16x16x32_bf16 v[28:31], a[52:55], v[176:179], v[28:31]
s_add_u32 s0, s69, 12288
s_add_u32 s0, s0, s76
v_mfma_f32_16x16x32_bf16 v[28:31], a[72:75], v[180:183], v[28:31]
v_mfma_f32_16x16x32_bf16 v[32:35], a[56:59], v[176:179], v[32:35]
s_waitcnt vmcnt(17)
v_mfma_f32_16x16x32_bf16 v[32:35], a[76:79], v[180:183], v[32:35]
buffer_load_dwordx4 v[152:155], v1, s[52:55], s0 offen offset:0 sc1
v_mfma_f32_16x16x32_bf16 v[16:19], a[80:83], v[184:187], v[16:19]
s_waitcnt vmcnt(17)
v_mfma_f32_16x16x32_bf16 v[16:19], a[100:103], v[188:191], v[16:19]
v_mfma_f32_16x16x32_bf16 v[20:23], a[84:87], v[184:187], v[20:23]
v_mfma_f32_16x16x32_bf16 v[20:23], a[104:107], v[188:191], v[20:23]
buffer_load_dwordx4 v[156:159], v1, s[52:55], s0 offen offset:1024 sc1
v_mfma_f32_16x16x32_bf16 v[24:27], a[88:91], v[184:187], v[24:27]
v_mfma_f32_16x16x32_bf16 v[24:27], a[108:111], v[188:191], v[24:27]
v_mfma_f32_16x16x32_bf16 v[28:31], a[92:95], v[184:187], v[28:31]
v_mfma_f32_16x16x32_bf16 v[28:31], a[112:115], v[188:191], v[28:31]
v_mfma_f32_16x16x32_bf16 v[32:35], a[96:99], v[184:187], v[32:35]
v_mfma_f32_16x16x32_bf16 v[32:35], a[116:119], v[188:191], v[32:35]
s_waitcnt vmcnt(17)
v_mfma_f32_16x16x32_bf16 v[16:19], a[120:123], v[192:195], v[16:19]
s_waitcnt vmcnt(16)
v_mfma_f32_16x16x32_bf16 v[16:19], a[140:143], v[196:199], v[16:19]
v_mfma_f32_16x16x32_bf16 v[20:23], a[124:127], v[192:195], v[20:23]
v_mfma_f32_16x16x32_bf16 v[20:23], a[144:147], v[196:199], v[20:23]
v_mfma_f32_16x16x32_bf16 v[24:27], a[128:131], v[192:195], v[24:27]
v_mfma_f32_16x16x32_bf16 v[24:27], a[148:151], v[196:199], v[24:27]
v_mfma_f32_16x16x32_bf16 v[28:31], a[132:135], v[192:195], v[28:31]
v_mfma_f32_16x16x32_bf16 v[28:31], a[152:155], v[196:199], v[28:31]
v_mfma_f32_16x16x32_bf16 v[32:35], a[136:139], v[192:195], v[32:35]
v_mfma_f32_16x16x32_bf16 v[32:35], a[156:159], v[196:199], v[32:35]
s_waitcnt vmcnt(15)
ds_write_b128 v8, v[16:19] offset:0
v_mfma_f32_16x16x32_bf16 v[36:39], a[160:163], v[200:203], 0
s_waitcnt vmcnt(14)
v_mfma_f32_16x16x32_bf16 v[36:39], a[180:183], v[204:207], v[36:39]
ds_write_b128 v8, v[20:23] offset:1024
v_mfma_f32_16x16x32_bf16 v[40:43], a[164:167], v[200:203], 0
v_mfma_f32_16x16x32_bf16 v[40:43], a[184:187], v[204:207], v[40:43]
ds_write_b128 v8, v[24:27] offset:2048
v_mfma_f32_16x16x32_bf16 v[44:47], a[168:171], v[200:203], 0
v_mfma_f32_16x16x32_bf16 v[44:47], a[188:191], v[204:207], v[44:47]
ds_write_b128 v8, v[28:31] offset:3072
v_mfma_f32_16x16x32_bf16 v[48:51], a[172:175], v[200:203], 0
v_mfma_f32_16x16x32_bf16 v[48:51], a[192:195], v[204:207], v[48:51]
ds_write_b128 v8, v[32:35] offset:4096
v_mfma_f32_16x16x32_bf16 v[52:55], a[176:179], v[200:203], 0
v_mfma_f32_16x16x32_bf16 v[52:55], a[196:199], v[204:207], v[52:55]
s_waitcnt vmcnt(13)
v_mfma_f32_16x16x32_bf16 v[36:39], a[200:203], v[208:211], v[36:39]
s_waitcnt vmcnt(11)
v_mfma_f32_16x16x32_bf16 v[36:39], a[220:223], v[212:215], v[36:39]
v_mfma_f32_16x16x32_bf16 v[40:43], a[204:207], v[208:211], v[40:43]
v_mfma_f32_16x16x32_bf16 v[40:43], a[224:227], v[212:215], v[40:43]
v_mfma_f32_16x16x32_bf16 v[44:47], a[208:211], v[208:211], v[44:47]
v_mfma_f32_16x16x32_bf16 v[44:47], a[228:231], v[212:215], v[44:47]
v_mfma_f32_16x16x32_bf16 v[48:51], a[212:215], v[208:211], v[48:51]
v_mfma_f32_16x16x32_bf16 v[48:51], a[232:235], v[212:215], v[48:51]
v_mfma_f32_16x16x32_bf16 v[52:55], a[216:219], v[208:211], v[52:55]
v_mfma_f32_16x16x32_bf16 v[52:55], a[236:239], v[212:215], v[52:55]
s_waitcnt vmcnt(10)
v_mfma_f32_16x16x32_bf16 v[36:39], a[240:243], v[216:219], v[36:39]
s_waitcnt vmcnt(9)
v_mfma_f32_16x16x32_bf16 v[36:39], v[236:239], v[220:223], v[36:39]
s_waitcnt lgkmcnt(0)
v_mfma_f32_16x16x32_bf16 v[40:43], a[244:247], v[216:219], v[40:43]
s_barrier
ds_read_b128 v[76:79], v9 offset:0
v_mfma_f32_16x16x32_bf16 v[40:43], v[240:243], v[220:223], v[40:43]
ds_read_b128 v[80:83], v9 offset:1024
v_mfma_f32_16x16x32_bf16 v[44:47], a[248:251], v[216:219], v[44:47]
ds_read_b128 v[84:87], v9 offset:2048
v_mfma_f32_16x16x32_bf16 v[44:47], v[244:247], v[220:223], v[44:47]
ds_read_b128 v[88:91], v9 offset:3072
v_mfma_f32_16x16x32_bf16 v[48:51], a[252:255], v[216:219], v[48:51]
ds_read_b128 v[92:95], v9 offset:4096
v_mfma_f32_16x16x32_bf16 v[48:51], v[248:251], v[220:223], v[48:51]
v_mfma_f32_16x16x32_bf16 v[52:55], v[232:235], v[216:219], v[52:55]
v_mfma_f32_16x16x32_bf16 v[52:55], v[252:255], v[220:223], v[52:55]
s_waitcnt vmcnt(8) lgkmcnt(2)
v_fmac_f32 v96, v56, v76
v_fmac_f32 v97, v57, v77
v_mfma_f32_16x16x32_bf16 v[36:39], a[0:3], v[128:131], v[36:39]
s_waitcnt vmcnt(7)
v_fmac_f32 v98, v58, v78
v_fmac_f32 v99, v59, v79
v_mfma_f32_16x16x32_bf16 v[36:39], a[20:23], v[132:135], v[36:39]
ds_read_b128 v[76:79], v9 offset:5120
v_fmac_f32 v100, v60, v80
v_mfma_f32_16x16x32_bf16 v[40:43], a[4:7], v[128:131], v[40:43]
v_fmac_f32 v101, v61, v81
v_fmac_f32 v102, v62, v82
v_mfma_f32_16x16x32_bf16 v[40:43], a[24:27], v[132:135], v[40:43]
v_fmac_f32 v103, v63, v83
v_fmac_f32 v104, v64, v84
v_mfma_f32_16x16x32_bf16 v[44:47], a[8:11], v[128:131], v[44:47]
ds_read_b128 v[80:83], v9 offset:6144
v_fmac_f32 v105, v65, v85
v_mfma_f32_16x16x32_bf16 v[44:47], a[28:31], v[132:135], v[44:47]
)ASM"
R"ASM(
v_fmac_f32 v106, v66, v86
v_fmac_f32 v107, v67, v87
v_mfma_f32_16x16x32_bf16 v[48:51], a[12:15], v[128:131], v[48:51]
s_waitcnt lgkmcnt(1)
ds_read_b128 v[84:87], v9 offset:7168
v_fmac_f32 v108, v68, v88
v_mfma_f32_16x16x32_bf16 v[48:51], a[32:35], v[132:135], v[48:51]
v_fmac_f32 v109, v69, v89
v_fmac_f32 v110, v70, v90
v_mfma_f32_16x16x32_bf16 v[52:55], a[16:19], v[128:131], v[52:55]
v_fmac_f32 v111, v71, v91
v_fmac_f32 v112, v72, v92
v_mfma_f32_16x16x32_bf16 v[52:55], a[36:39], v[132:135], v[52:55]
s_waitcnt vmcnt(6)
ds_read_b128 v[88:91], v9 offset:8192
v_fmac_f32 v113, v73, v93
v_mfma_f32_16x16x32_bf16 v[36:39], a[40:43], v[136:139], v[36:39]
s_waitcnt vmcnt(5)
v_fmac_f32 v114, v74, v94
v_fmac_f32 v115, v75, v95
v_mfma_f32_16x16x32_bf16 v[36:39], a[60:63], v[140:143], v[36:39]
ds_read_b128 v[92:95], v9 offset:9216
v_fmac_f32 v96, v56, v76
v_mfma_f32_16x16x32_bf16 v[40:43], a[44:47], v[136:139], v[40:43]
v_fmac_f32 v97, v57, v77
v_fmac_f32 v98, v58, v78
v_mfma_f32_16x16x32_bf16 v[40:43], a[64:67], v[140:143], v[40:43]
s_waitcnt lgkmcnt(2)
v_fmac_f32 v99, v59, v79
v_fmac_f32 v100, v60, v80
v_mfma_f32_16x16x32_bf16 v[44:47], a[48:51], v[136:139], v[44:47]
ds_read_b128 v[76:79], v9 offset:10240
v_fmac_f32 v101, v61, v81
v_mfma_f32_16x16x32_bf16 v[44:47], a[68:71], v[140:143], v[44:47]
v_fmac_f32 v102, v62, v82
v_fmac_f32 v103, v63, v83
v_mfma_f32_16x16x32_bf16 v[48:51], a[52:55], v[136:139], v[48:51]
ds_read_b128 v[80:83], v9 offset:11264
v_fmac_f32 v104, v64, v84
v_mfma_f32_16x16x32_bf16 v[48:51], a[72:75], v[140:143], v[48:51]
v_fmac_f32 v105, v65, v85
v_fmac_f32 v106, v66, v86
v_mfma_f32_16x16x32_bf16 v[52:55], a[56:59], v[136:139], v[52:55]
s_waitcnt lgkmcnt(2)
v_fmac_f32 v107, v67, v87
v_fmac_f32 v108, v68, v88
v_mfma_f32_16x16x32_bf16 v[52:55], a[76:79], v[140:143], v[52:55]
s_waitcnt vmcnt(3)
ds_read_b128 v[84:87], v9 offset:12288
v_fmac_f32 v109, v69, v89
v_mfma_f32_16x16x32_bf16 v[36:39], a[80:83], v[144:147], v[36:39]
s_waitcnt vmcnt(2)
v_fmac_f32 v110, v70, v90
v_fmac_f32 v111, v71, v91
v_mfma_f32_16x16x32_bf16 v[36:39], a[100:103], v[148:151], v[36:39]
ds_read_b128 v[88:91], v9 offset:13312
v_fmac_f32 v112, v72, v92
v_mfma_f32_16x16x32_bf16 v[40:43], a[84:87], v[144:147], v[40:43]
v_fmac_f32 v113, v73, v93
v_fmac_f32 v114, v74, v94
v_mfma_f32_16x16x32_bf16 v[40:43], a[104:107], v[148:151], v[40:43]
s_waitcnt lgkmcnt(2)
v_fmac_f32 v115, v75, v95
v_fmac_f32 v96, v56, v76
v_mfma_f32_16x16x32_bf16 v[44:47], a[88:91], v[144:147], v[44:47]
ds_read_b128 v[92:95], v9 offset:14336
v_fmac_f32 v97, v57, v77
v_mfma_f32_16x16x32_bf16 v[44:47], a[108:111], v[148:151], v[44:47]
v_fmac_f32 v98, v58, v78
v_fmac_f32 v99, v59, v79
v_mfma_f32_16x16x32_bf16 v[48:51], a[92:95], v[144:147], v[48:51]
v_fmac_f32 v100, v60, v80
v_fmac_f32 v101, v61, v81
v_mfma_f32_16x16x32_bf16 v[48:51], a[112:115], v[148:151], v[48:51]
v_fmac_f32 v102, v62, v82
v_fmac_f32 v103, v63, v83
v_mfma_f32_16x16x32_bf16 v[52:55], a[96:99], v[144:147], v[52:55]
s_waitcnt lgkmcnt(1)
v_fmac_f32 v104, v64, v84
v_fmac_f32 v105, v65, v85
v_mfma_f32_16x16x32_bf16 v[52:55], a[116:119], v[148:151], v[52:55]
s_waitcnt vmcnt(1)
v_fmac_f32 v106, v66, v86
v_fmac_f32 v107, v67, v87
v_mfma_f32_16x16x32_bf16 v[36:39], a[120:123], v[152:155], v[36:39]
s_waitcnt vmcnt(0)
v_fmac_f32 v108, v68, v88
v_fmac_f32 v109, v69, v89
v_mfma_f32_16x16x32_bf16 v[36:39], a[140:143], v[156:159], v[36:39]
v_fmac_f32 v110, v70, v90
v_fmac_f32 v111, v71, v91
v_mfma_f32_16x16x32_bf16 v[40:43], a[124:127], v[152:155], v[40:43]
s_waitcnt lgkmcnt(0)
v_fmac_f32 v112, v72, v92
v_fmac_f32 v113, v73, v93
v_mfma_f32_16x16x32_bf16 v[40:43], a[144:147], v[156:159], v[40:43]
v_fmac_f32 v114, v74, v94
v_fmac_f32 v115, v75, v95
v_mfma_f32_16x16x32_bf16 v[44:47], a[128:131], v[152:155], v[44:47]
v_mfma_f32_16x16x32_bf16 v[44:47], a[148:151], v[156:159], v[44:47]
v_mfma_f32_16x16x32_bf16 v[48:51], a[132:135], v[152:155], v[48:51]
v_mfma_f32_16x16x32_bf16 v[48:51], a[152:155], v[156:159], v[48:51]
v_mfma_f32_16x16x32_bf16 v[52:55], a[136:139], v[152:155], v[52:55]
v_mfma_f32_16x16x32_bf16 v[52:55], a[156:159], v[156:159], v[52:55]
v_pk_fma_f32 v[96:97], v[56:57], v[36:37], v[96:97]
v_pk_fma_f32 v[98:99], v[58:59], v[38:39], v[98:99]
v_cvt_pk_bf16_f32 v16, v96, v97
v_cvt_pk_bf16_f32 v17, v98, v99
ds_write_b64 v224, v[16:17] offset:0
v_pk_fma_f32 v[100:101], v[60:61], v[40:41], v[100:101]
v_pk_fma_f32 v[102:103], v[62:63], v[42:43], v[102:103]
v_cvt_pk_bf16_f32 v18, v100, v101
v_cvt_pk_bf16_f32 v19, v102, v103
ds_write_b64 v224, v[18:19] offset:32
ds_read_b128 v[76:79], v225 offset:0
v_pk_fma_f32 v[104:105], v[64:65], v[44:45], v[104:105]
v_pk_fma_f32 v[106:107], v[66:67], v[46:47], v[106:107]
v_cvt_pk_bf16_f32 v20, v104, v105
v_cvt_pk_bf16_f32 v21, v106, v107
ds_write_b64 v224, v[20:21] offset:64
v_pk_fma_f32 v[108:109], v[68:69], v[48:49], v[108:109]
v_pk_fma_f32 v[110:111], v[70:71], v[50:51], v[110:111]
v_cvt_pk_bf16_f32 v22, v108, v109
v_cvt_pk_bf16_f32 v23, v110, v111
ds_write_b64 v224, v[22:23] offset:96
ds_read_b128 v[80:83], v225 offset:64
v_pk_fma_f32 v[112:113], v[72:73], v[52:53], v[112:113]
v_pk_fma_f32 v[114:115], v[74:75], v[54:55], v[114:115]
v_cvt_pk_bf16_f32 v24, v112, v113
v_cvt_pk_bf16_f32 v25, v114, v115
ds_write_b64 v224, v[24:25] offset:128
ds_read_b128 v[84:87], v225 offset:128
s_waitcnt lgkmcnt(0)
buffer_store_dwordx4 v[76:79], v226, s[48:51], s83 offen offset:0
buffer_store_dwordx4 v[80:83], v226, s[48:51], s83 offen offset:64
buffer_store_dwordx4 v[84:87], v227, s[48:51], s84 offen offset:0
s_endpgm
L_ch_b1:
L_attn_redo:
s_mov_b32 s94, -1
s_lshl_b32 s78, s62, 1
s_add_u32 s79, s78, 1
s_sub_u32 s80, 30, s78
s_sub_u32 s81, 31, s78
s_mov_b32 s82, 0
s_lshl_b32 s66, s60, 12
s_mov_b32 s67, s66
v_mov_b32 v12, v1
v_mov_b32 v228, 0xff7fffff
v_mov_b32 v229, 0xff7fffff
v_mov_b32 v230, 0xff7fffff
v_mov_b32 v231, 0xff7fffff
s_waitcnt vmcnt(0) lgkmcnt(0)
s_barrier
L_redo_loop:
s_mov_b32 m0, s66
s_nop 0
buffer_load_dwordx4 v1, s[56:59], s67 offen offset:0 sc1 lds
buffer_load_dwordx4 v1, s[56:59], s67 offen offset:1024 sc1 lds
buffer_load_dwordx4 v1, s[56:59], s67 offen offset:2048 sc1 lds
buffer_load_dwordx4 v1, s[56:59], s67 offen offset:3072 sc1 lds
s_waitcnt vmcnt(0)
s_barrier
ds_read_b128 a[128:131], v12 offset:0
ds_read_b128 a[132:135], v12 offset:1024
ds_read_b128 a[136:139], v12 offset:2048
ds_read_b128 a[140:143], v12 offset:3072
ds_read_b128 a[144:147], v12 offset:4096
ds_read_b128 a[148:151], v12 offset:5120
ds_read_b128 a[152:155], v12 offset:6144
ds_read_b128 a[156:159], v12 offset:7168
ds_read_b128 a[160:163], v12 offset:8192
ds_read_b128 a[164:167], v12 offset:9216
ds_read_b128 a[168:171], v12 offset:10240
ds_read_b128 a[172:175], v12 offset:11264
ds_read_b128 a[176:179], v12 offset:12288
ds_read_b128 a[180:183], v12 offset:13312
ds_read_b128 a[184:187], v12 offset:14336
ds_read_b128 a[188:191], v12 offset:15360
s_waitcnt lgkmcnt(0)
s_barrier
s_cmp_gt_u32 s82, s78
s_cbranch_scc1 L_redo_skip0
s_cmp_eq_u32 s82, s78
s_cselect_b32 s83, 1.0, 0
v_mul_f32 v80, s83, v212
v_mul_f32 v81, s83, v213
v_mul_f32 v82, s83, v214
v_mul_f32 v83, s83, v215
v_mul_f32 v84, s83, v216
v_mul_f32 v85, s83, v217
v_mul_f32 v86, s83, v218
v_mul_f32 v87, s83, v219
v_mul_f32 v88, s83, v220
v_mul_f32 v89, s83, v221
v_mul_f32 v90, s83, v222
v_mul_f32 v91, s83, v223
v_mul_f32 v92, s83, v224
v_mul_f32 v93, s83, v225
v_mul_f32 v94, s83, v226
v_mul_f32 v95, s83, v227
v_mfma_f32_16x16x32_bf16 v[80:83], a[128:131], a[64:67], v[80:83]
v_mfma_f32_16x16x32_bf16 v[80:83], a[132:135], a[68:71], v[80:83]
v_mfma_f32_16x16x32_bf16 v[80:83], a[136:139], a[72:75], v[80:83]
v_mfma_f32_16x16x32_bf16 v[80:83], a[140:143], a[76:79], v[80:83]
v_mfma_f32_16x16x32_bf16 v[84:87], a[144:147], a[64:67], v[84:87]
v_mfma_f32_16x16x32_bf16 v[84:87], a[148:151], a[68:71], v[84:87]
v_mfma_f32_16x16x32_bf16 v[84:87], a[152:155], a[72:75], v[84:87]
v_mfma_f32_16x16x32_bf16 v[84:87], a[156:159], a[76:79], v[84:87]
v_mfma_f32_16x16x32_bf16 v[88:91], a[160:163], a[64:67], v[88:91]
v_mfma_f32_16x16x32_bf16 v[88:91], a[164:167], a[68:71], v[88:91]
v_mfma_f32_16x16x32_bf16 v[88:91], a[168:171], a[72:75], v[88:91]
v_mfma_f32_16x16x32_bf16 v[88:91], a[172:175], a[76:79], v[88:91]
v_mfma_f32_16x16x32_bf16 v[92:95], a[176:179], a[64:67], v[92:95]
v_mfma_f32_16x16x32_bf16 v[92:95], a[180:183], a[68:71], v[92:95]
v_mfma_f32_16x16x32_bf16 v[92:95], a[184:187], a[72:75], v[92:95]
v_mfma_f32_16x16x32_bf16 v[92:95], a[188:191], a[76:79], v[92:95]
s_nop 7
v_max3_f32 v228, v228, v80, v81
v_max3_f32 v228, v228, v82, v83
v_max3_f32 v228, v228, v84, v85
v_max3_f32 v228, v228, v86, v87
v_max3_f32 v228, v228, v88, v89
v_max3_f32 v228, v228, v90, v91
v_max3_f32 v228, v228, v92, v93
v_max3_f32 v228, v228, v94, v95
L_redo_skip0:
s_cmp_gt_u32 s82, s79
s_cbranch_scc1 L_redo_skip1
s_cmp_eq_u32 s82, s79
s_cselect_b32 s83, 1.0, 0
v_mul_f32 v80, s83, v212
v_mul_f32 v81, s83, v213
v_mul_f32 v82, s83, v214
v_mul_f32 v83, s83, v215
v_mul_f32 v84, s83, v216
v_mul_f32 v85, s83, v217
v_mul_f32 v86, s83, v218
v_mul_f32 v87, s83, v219
v_mul_f32 v88, s83, v220
v_mul_f32 v89, s83, v221
v_mul_f32 v90, s83, v222
v_mul_f32 v91, s83, v223
v_mul_f32 v92, s83, v224
v_mul_f32 v93, s83, v225
v_mul_f32 v94, s83, v226
v_mul_f32 v95, s83, v227
v_mfma_f32_16x16x32_bf16 v[80:83], a[128:131], a[80:83], v[80:83]
v_mfma_f32_16x16x32_bf16 v[80:83], a[132:135], a[84:87], v[80:83]
v_mfma_f32_16x16x32_bf16 v[80:83], a[136:139], a[88:91], v[80:83]
v_mfma_f32_16x16x32_bf16 v[80:83], a[140:143], a[92:95], v[80:83]
v_mfma_f32_16x16x32_bf16 v[84:87], a[144:147], a[80:83], v[84:87]
v_mfma_f32_16x16x32_bf16 v[84:87], a[148:151], a[84:87], v[84:87]
v_mfma_f32_16x16x32_bf16 v[84:87], a[152:155], a[88:91], v[84:87]
v_mfma_f32_16x16x32_bf16 v[84:87], a[156:159], a[92:95], v[84:87]
v_mfma_f32_16x16x32_bf16 v[88:91], a[160:163], a[80:83], v[88:91]
v_mfma_f32_16x16x32_bf16 v[88:91], a[164:167], a[84:87], v[88:91]
v_mfma_f32_16x16x32_bf16 v[88:91], a[168:171], a[88:91], v[88:91]
v_mfma_f32_16x16x32_bf16 v[88:91], a[172:175], a[92:95], v[88:91]
v_mfma_f32_16x16x32_bf16 v[92:95], a[176:179], a[80:83], v[92:95]
v_mfma_f32_16x16x32_bf16 v[92:95], a[180:183], a[84:87], v[92:95]
v_mfma_f32_16x16x32_bf16 v[92:95], a[184:187], a[88:91], v[92:95]
v_mfma_f32_16x16x32_bf16 v[92:95], a[188:191], a[92:95], v[92:95]
s_nop 7
v_max3_f32 v229, v229, v80, v81
v_max3_f32 v229, v229, v82, v83
v_max3_f32 v229, v229, v84, v85
v_max3_f32 v229, v229, v86, v87
v_max3_f32 v229, v229, v88, v89
v_max3_f32 v229, v229, v90, v91
v_max3_f32 v229, v229, v92, v93
v_max3_f32 v229, v229, v94, v95
L_redo_skip1:
s_cmp_gt_u32 s82, s80
s_cbranch_scc1 L_redo_skip2
s_cmp_eq_u32 s82, s80
s_cselect_b32 s83, 1.0, 0
v_mul_f32 v80, s83, v212
v_mul_f32 v81, s83, v213
v_mul_f32 v82, s83, v214
v_mul_f32 v83, s83, v215
v_mul_f32 v84, s83, v216
v_mul_f32 v85, s83, v217
v_mul_f32 v86, s83, v218
v_mul_f32 v87, s83, v219
v_mul_f32 v88, s83, v220
v_mul_f32 v89, s83, v221
v_mul_f32 v90, s83, v222
v_mul_f32 v91, s83, v223
v_mul_f32 v92, s83, v224
v_mul_f32 v93, s83, v225
v_mul_f32 v94, s83, v226
v_mul_f32 v95, s83, v227
v_mfma_f32_16x16x32_bf16 v[80:83], a[128:131], a[96:99], v[80:83]
v_mfma_f32_16x16x32_bf16 v[80:83], a[132:135], a[100:103], v[80:83]
v_mfma_f32_16x16x32_bf16 v[80:83], a[136:139], a[104:107], v[80:83]
v_mfma_f32_16x16x32_bf16 v[80:83], a[140:143], a[108:111], v[80:83]
v_mfma_f32_16x16x32_bf16 v[84:87], a[144:147], a[96:99], v[84:87]
v_mfma_f32_16x16x32_bf16 v[84:87], a[148:151], a[100:103], v[84:87]
v_mfma_f32_16x16x32_bf16 v[84:87], a[152:155], a[104:107], v[84:87]
v_mfma_f32_16x16x32_bf16 v[84:87], a[156:159], a[108:111], v[84:87]
v_mfma_f32_16x16x32_bf16 v[88:91], a[160:163], a[96:99], v[88:91]
v_mfma_f32_16x16x32_bf16 v[88:91], a[164:167], a[100:103], v[88:91]
v_mfma_f32_16x16x32_bf16 v[88:91], a[168:171], a[104:107], v[88:91]
v_mfma_f32_16x16x32_bf16 v[88:91], a[172:175], a[108:111], v[88:91]
v_mfma_f32_16x16x32_bf16 v[92:95], a[176:179], a[96:99], v[92:95]
v_mfma_f32_16x16x32_bf16 v[92:95], a[180:183], a[100:103], v[92:95]
v_mfma_f32_16x16x32_bf16 v[92:95], a[184:187], a[104:107], v[92:95]
v_mfma_f32_16x16x32_bf16 v[92:95], a[188:191], a[108:111], v[92:95]
s_nop 7
v_max3_f32 v230, v230, v80, v81
v_max3_f32 v230, v230, v82, v83
v_max3_f32 v230, v230, v84, v85
v_max3_f32 v230, v230, v86, v87
v_max3_f32 v230, v230, v88, v89
v_max3_f32 v230, v230, v90, v91
v_max3_f32 v230, v230, v92, v93
v_max3_f32 v230, v230, v94, v95
L_redo_skip2:
s_cmp_gt_u32 s82, s81
s_cbranch_scc1 L_redo_skip3
s_cmp_eq_u32 s82, s81
s_cselect_b32 s83, 1.0, 0
v_mul_f32 v80, s83, v212
v_mul_f32 v81, s83, v213
v_mul_f32 v82, s83, v214
v_mul_f32 v83, s83, v215
v_mul_f32 v84, s83, v216
v_mul_f32 v85, s83, v217
v_mul_f32 v86, s83, v218
v_mul_f32 v87, s83, v219
v_mul_f32 v88, s83, v220
v_mul_f32 v89, s83, v221
v_mul_f32 v90, s83, v222
v_mul_f32 v91, s83, v223
v_mul_f32 v92, s83, v224
v_mul_f32 v93, s83, v225
v_mul_f32 v94, s83, v226
v_mul_f32 v95, s83, v227
v_mfma_f32_16x16x32_bf16 v[80:83], a[128:131], a[112:115], v[80:83]
v_mfma_f32_16x16x32_bf16 v[80:83], a[132:135], a[116:119], v[80:83]
v_mfma_f32_16x16x32_bf16 v[80:83], a[136:139], a[120:123], v[80:83]
v_mfma_f32_16x16x32_bf16 v[80:83], a[140:143], a[124:127], v[80:83]
v_mfma_f32_16x16x32_bf16 v[84:87], a[144:147], a[112:115], v[84:87]
v_mfma_f32_16x16x32_bf16 v[84:87], a[148:151], a[116:119], v[84:87]
v_mfma_f32_16x16x32_bf16 v[84:87], a[152:155], a[120:123], v[84:87]
v_mfma_f32_16x16x32_bf16 v[84:87], a[156:159], a[124:127], v[84:87]
v_mfma_f32_16x16x32_bf16 v[88:91], a[160:163], a[112:115], v[88:91]
v_mfma_f32_16x16x32_bf16 v[88:91], a[164:167], a[116:119], v[88:91]
v_mfma_f32_16x16x32_bf16 v[88:91], a[168:171], a[120:123], v[88:91]
v_mfma_f32_16x16x32_bf16 v[88:91], a[172:175], a[124:127], v[88:91]
v_mfma_f32_16x16x32_bf16 v[92:95], a[176:179], a[112:115], v[92:95]
v_mfma_f32_16x16x32_bf16 v[92:95], a[180:183], a[116:119], v[92:95]
v_mfma_f32_16x16x32_bf16 v[92:95], a[184:187], a[120:123], v[92:95]
v_mfma_f32_16x16x32_bf16 v[92:95], a[188:191], a[124:127], v[92:95]
s_nop 7
v_max3_f32 v231, v231, v80, v81
v_max3_f32 v231, v231, v82, v83
v_max3_f32 v231, v231, v84, v85
v_max3_f32 v231, v231, v86, v87
v_max3_f32 v231, v231, v88, v89
v_max3_f32 v231, v231, v90, v91
v_max3_f32 v231, v231, v92, v93
v_max3_f32 v231, v231, v94, v95
L_redo_skip3:
s_add_u32 s67, s67, 37888
s_add_u32 s82, s82, 1
s_cmp_le_u32 s82, s81
s_cbranch_scc1 L_redo_loop
v_mov_b32 v232, v228
s_nop 1
v_permlane32_swap_b32 v228, v232
v_max_f32 v228, v228, v232
v_mov_b32 v232, v228
s_nop 1
v_permlane16_swap_b32 v228, v232
v_max_f32 v228, v228, v232
v_sub_f32 v176, 0, v228
v_sub_f32 v177, 0, v228
v_sub_f32 v178, 0, v228
v_sub_f32 v179, 0, v228
v_mov_b32 v232, v229
s_nop 1
v_permlane32_swap_b32 v229, v232
v_max_f32 v229, v229, v232
v_mov_b32 v232, v229
s_nop 1
v_permlane16_swap_b32 v229, v232
v_max_f32 v229, v229, v232
v_sub_f32 v180, 0, v229
v_sub_f32 v181, 0, v229
v_sub_f32 v182, 0, v229
v_sub_f32 v183, 0, v229
v_mov_b32 v232, v230
s_nop 1
v_permlane32_swap_b32 v230, v232
v_max_f32 v230, v230, v232
v_mov_b32 v232, v230
s_nop 1
v_permlane16_swap_b32 v230, v232
)ASM"
R"ASM(
v_max_f32 v230, v230, v232
v_sub_f32 v184, 0, v230
v_sub_f32 v185, 0, v230
v_sub_f32 v186, 0, v230
v_sub_f32 v187, 0, v230
v_mov_b32 v232, v231
s_nop 1
v_permlane32_swap_b32 v231, v232
v_max_f32 v231, v231, v232
v_mov_b32 v232, v231
s_nop 1
v_permlane16_swap_b32 v231, v232
v_max_f32 v231, v231, v232
v_sub_f32 v188, 0, v231
v_sub_f32 v189, 0, v231
v_sub_f32 v190, 0, v231
v_sub_f32 v191, 0, v231
s_mov_b64 s[80:81], s[52:53]
s_mov_b32 s82, 0
s_mov_b32 s83, 0x20000
s_mov_b32 s84, 0
s_mov_b32 s85, 0
s_mov_b32 s86, 0
s_mov_b32 s87, 1073741824
s_mov_b32 s88, 0
s_mov_b32 s79, 0
s_lshl_b32 s90, s60, 12
s_add_u32 s90, s90, 131072
s_mov_b32 s78, 1073741824
s_mov_b32 s93, 1073741824
s_mov_b32 s101, 0
s_add_u32 m0, s66, 98304
s_add_u32 s67, s66, 0
buffer_load_dwordx4 v1, s[56:59], s67 offen offset:0 sc1 lds
buffer_load_dwordx4 v1, s[56:59], s67 offen offset:1024 sc1 lds
buffer_load_dwordx4 v1, s[56:59], s67 offen offset:2048 sc1 lds
buffer_load_dwordx4 v1, s[56:59], s67 offen offset:3072 sc1 lds
s_add_u32 m0, s66, 0
s_add_u32 s67, s66, 37888
buffer_load_dwordx4 v1, s[56:59], s67 offen offset:0 sc1 lds
buffer_load_dwordx4 v1, s[56:59], s67 offen offset:1024 sc1 lds
buffer_load_dwordx4 v1, s[56:59], s67 offen offset:2048 sc1 lds
buffer_load_dwordx4 v1, s[56:59], s67 offen offset:3072 sc1 lds
s_add_u32 m0, s66, 32768
s_add_u32 s67, s66, 75776
buffer_load_dwordx4 v1, s[56:59], s67 offen offset:0 sc1 lds
buffer_load_dwordx4 v1, s[56:59], s67 offen offset:1024 sc1 lds
buffer_load_dwordx4 v1, s[56:59], s67 offen offset:2048 sc1 lds
buffer_load_dwordx4 v1, s[56:59], s67 offen offset:3072 sc1 lds
s_add_u32 m0, s66, 49152
s_add_u32 s67, s66, 16384
buffer_load_dwordx4 v1, s[56:59], s67 offen offset:0 sc1 lds
buffer_load_dwordx4 v1, s[56:59], s67 offen offset:1024 sc1 lds
buffer_load_dwordx4 v1, s[56:59], s67 offen offset:2048 sc1 lds
buffer_load_dwordx4 v1, s[56:59], s67 offen offset:3072 sc1 lds
s_add_u32 m0, s66, 65536
s_add_u32 s67, s66, 113664
buffer_load_dwordx4 v1, s[56:59], s67 offen offset:0 sc1 lds
buffer_load_dwordx4 v1, s[56:59], s67 offen offset:1024 sc1 lds
buffer_load_dwordx4 v1, s[56:59], s67 offen offset:2048 sc1 lds
buffer_load_dwordx4 v1, s[56:59], s67 offen offset:3072 sc1 lds
s_add_u32 m0, s66, 81920
s_add_u32 s67, s66, 54272
buffer_load_dwordx4 v1, s[56:59], s67 offen offset:0 sc1 lds
buffer_load_dwordx4 v1, s[56:59], s67 offen offset:1024 sc1 lds
buffer_load_dwordx4 v1, s[56:59], s67 offen offset:2048 sc1 lds
buffer_load_dwordx4 v1, s[56:59], s67 offen offset:3072 sc1 lds
s_add_u32 s68, s66, 98304
s_add_u32 s66, s66, 151552
s_mov_b32 s69, 0
v_accvgpr_write_b32 a0, 0
v_accvgpr_write_b32 a1, 0
v_accvgpr_write_b32 a2, 0
v_accvgpr_write_b32 a3, 0
v_accvgpr_write_b32 a4, 0
v_accvgpr_write_b32 a5, 0
v_accvgpr_write_b32 a6, 0
v_accvgpr_write_b32 a7, 0
v_accvgpr_write_b32 a8, 0
v_accvgpr_write_b32 a9, 0
v_accvgpr_write_b32 a10, 0
v_accvgpr_write_b32 a11, 0
v_accvgpr_write_b32 a12, 0
v_accvgpr_write_b32 a13, 0
v_accvgpr_write_b32 a14, 0
v_accvgpr_write_b32 a15, 0
v_accvgpr_write_b32 a16, 0
v_accvgpr_write_b32 a17, 0
v_accvgpr_write_b32 a18, 0
v_accvgpr_write_b32 a19, 0
v_accvgpr_write_b32 a20, 0
v_accvgpr_write_b32 a21, 0
v_accvgpr_write_b32 a22, 0
v_accvgpr_write_b32 a23, 0
v_accvgpr_write_b32 a24, 0
v_accvgpr_write_b32 a25, 0
v_accvgpr_write_b32 a26, 0
v_accvgpr_write_b32 a27, 0
v_accvgpr_write_b32 a28, 0
v_accvgpr_write_b32 a29, 0
v_accvgpr_write_b32 a30, 0
v_accvgpr_write_b32 a31, 0
v_accvgpr_write_b32 a32, 0
v_accvgpr_write_b32 a33, 0
v_accvgpr_write_b32 a34, 0
v_accvgpr_write_b32 a35, 0
v_accvgpr_write_b32 a36, 0
v_accvgpr_write_b32 a37, 0
v_accvgpr_write_b32 a38, 0
v_accvgpr_write_b32 a39, 0
v_accvgpr_write_b32 a40, 0
v_accvgpr_write_b32 a41, 0
v_accvgpr_write_b32 a42, 0
v_accvgpr_write_b32 a43, 0
v_accvgpr_write_b32 a44, 0
v_accvgpr_write_b32 a45, 0
v_accvgpr_write_b32 a46, 0
v_accvgpr_write_b32 a47, 0
v_accvgpr_write_b32 a48, 0
v_accvgpr_write_b32 a49, 0
v_accvgpr_write_b32 a50, 0
v_accvgpr_write_b32 a51, 0
v_accvgpr_write_b32 a52, 0
v_accvgpr_write_b32 a53, 0
v_accvgpr_write_b32 a54, 0
v_accvgpr_write_b32 a55, 0
v_accvgpr_write_b32 a56, 0
v_accvgpr_write_b32 a57, 0
v_accvgpr_write_b32 a58, 0
v_accvgpr_write_b32 a59, 0
v_accvgpr_write_b32 a60, 0
v_accvgpr_write_b32 a61, 0
v_accvgpr_write_b32 a62, 0
v_accvgpr_write_b32 a63, 0
v_mov_b32 v16, 0
v_mov_b32 v17, 0
v_mov_b32 v18, 0
v_mov_b32 v19, 0
v_mov_b32 v20, 0
v_mov_b32 v21, 0
v_mov_b32 v22, 0
v_mov_b32 v23, 0
v_mov_b32 v24, 0
v_mov_b32 v25, 0
v_mov_b32 v26, 0
v_mov_b32 v27, 0
v_mov_b32 v28, 0
v_mov_b32 v29, 0
v_mov_b32 v30, 0
v_mov_b32 v31, 0
v_mov_b32 v32, 0
v_mov_b32 v33, 0
v_mov_b32 v34, 0
v_mov_b32 v35, 0
v_mov_b32 v36, 0
v_mov_b32 v37, 0
v_mov_b32 v38, 0
v_mov_b32 v39, 0
v_mov_b32 v40, 0
v_mov_b32 v41, 0
v_mov_b32 v42, 0
v_mov_b32 v43, 0
v_mov_b32 v44, 0
v_mov_b32 v45, 0
v_mov_b32 v46, 0
v_mov_b32 v47, 0
v_mov_b32 v48, 0
v_mov_b32 v49, 0
v_mov_b32 v50, 0
v_mov_b32 v51, 0
v_mov_b32 v52, 0
v_mov_b32 v53, 0
v_mov_b32 v54, 0
v_mov_b32 v55, 0
v_mov_b32 v56, 0
v_mov_b32 v57, 0
v_mov_b32 v58, 0
v_mov_b32 v59, 0
v_mov_b32 v60, 0
v_mov_b32 v61, 0
v_mov_b32 v62, 0
v_mov_b32 v63, 0
v_mov_b32 v64, 0
v_mov_b32 v65, 0
v_mov_b32 v66, 0
v_mov_b32 v67, 0
v_mov_b32 v68, 0
v_mov_b32 v69, 0
v_mov_b32 v70, 0
v_mov_b32 v71, 0
v_mov_b32 v72, 0
v_mov_b32 v73, 0
v_mov_b32 v74, 0
v_mov_b32 v75, 0
v_mov_b32 v76, 0
v_mov_b32 v77, 0
v_mov_b32 v78, 0
v_mov_b32 v79, 0
v_mov_b32 v192, 0
v_mov_b32 v193, 0
v_mov_b32 v194, 0
v_mov_b32 v195, 0
v_mov_b32 v196, 0
v_mov_b32 v197, 0
v_mov_b32 v198, 0
v_mov_b32 v199, 0
v_mov_b32 v200, 0
v_mov_b32 v201, 0
v_mov_b32 v202, 0
v_mov_b32 v203, 0
v_mov_b32 v204, 0
v_mov_b32 v205, 0
v_mov_b32 v206, 0
v_mov_b32 v207, 0
v_mov_b32 v13, 0
v_add_u32 v12, 98304, v1
s_waitcnt vmcnt(20)
s_barrier
ds_read_b128 a[128:131], v12 offset:0
ds_read_b128 a[132:135], v12 offset:1024
ds_read_b128 a[136:139], v12 offset:2048
ds_read_b128 a[140:143], v12 offset:3072
ds_read_b128 a[144:147], v12 offset:4096
ds_read_b128 a[148:151], v12 offset:5120
ds_read_b128 a[152:155], v12 offset:6144
ds_read_b128 a[156:159], v12 offset:7168
ds_read_b128 a[160:163], v12 offset:8192
ds_read_b128 a[164:167], v12 offset:9216
ds_read_b128 a[168:171], v12 offset:10240
ds_read_b128 a[172:175], v12 offset:11264
ds_read_b128 a[176:179], v12 offset:12288
ds_read_b128 a[180:183], v12 offset:13312
ds_read_b128 a[184:187], v12 offset:14336
ds_read_b128 a[188:191], v12 offset:15360
s_waitcnt vmcnt(16) lgkmcnt(0)
s_barrier
v_mov_b32 v12, v1
s_branch L_attn_P0
L_stub_mod:
buffer_load_dwordx2 v[252:253], v254, s[52:55], 0 offen sc1
s_waitcnt vmcnt(0)
v_cmp_ne_u64 vcc, s[64:65], v[252:253]
s_sub_u32 s95, s95, 1
s_cselect_b32 s95, 0, s95
s_cbranch_vccz L_ret_mod
s_cbranch_scc1 L_ret_mod
s_sleep 2
s_branch L_stub_mod
L_stub_h:
buffer_load_dwordx2 v[184:185], v186, s[52:55], 0 offen sc1
s_waitcnt vmcnt(0)
v_cmp_ne_u64 vcc, s[64:65], v[184:185]
s_sub_u32 s95, s95, 1
s_cselect_b32 s95, 0, s95
s_cbranch_vccz L_ret_h
s_cbranch_scc1 L_ret_h
s_sleep 2
s_branch L_stub_h
L_stub_kv0:
buffer_load_dwordx2 v[8:9], v6, s[52:55], 0 offen sc1
s_waitcnt vmcnt(0)
v_cmp_ne_u64 vcc, s[64:65], v[8:9]
s_sub_u32 s95, s95, 1
s_cselect_b32 s95, 0, s95
s_cbranch_vccz L_ret_kv0
s_cbranch_scc1 L_ret_kv0
s_sleep 2
s_branch L_stub_kv0
L_stub_kv1:
buffer_load_dwordx2 v[8:9], v6, s[52:55], 0 offen sc1
s_waitcnt vmcnt(0)
v_cmp_ne_u64 vcc, s[64:65], v[8:9]
s_sub_u32 s95, s95, 1
s_cselect_b32 s95, 0, s95
s_cbranch_vccz L_ret_kv1
s_cbranch_scc1 L_ret_kv1
s_sleep 2
s_branch L_stub_kv1
L_stub_kv2:
buffer_load_dwordx2 v[8:9], v6, s[52:55], 0 offen sc1
s_waitcnt vmcnt(0)
v_cmp_ne_u64 vcc, s[64:65], v[8:9]
s_sub_u32 s95, s95, 1
s_cselect_b32 s95, 0, s95
s_cbranch_vccz L_ret_kv2
s_cbranch_scc1 L_ret_kv2
s_sleep 2
s_branch L_stub_kv2
L_stub_kvT:
buffer_load_dwordx2 v[8:9], v6, s[52:55], 0 offen sc1
s_waitcnt vmcnt(0)
v_cmp_ne_u64 vcc, s[64:65], v[8:9]
s_sub_u32 s95, s95, 1
s_cselect_b32 s95, 0, s95
s_cbranch_vccz L_ret_kvT
s_cbranch_scc1 L_ret_kvT
s_sleep 2
s_branch L_stub_kvT
L_stub_sj:
buffer_load_dwordx2 v[8:9], v6, s[52:55], 0 offen sc1
s_waitcnt vmcnt(0)
v_cmp_ne_u64 vcc, s[64:65], v[8:9]
s_sub_u32 s95, s95, 1
s_cselect_b32 s95, 0, s95
s_cbranch_vccz L_ret_sj
s_cbranch_scc1 L_ret_sj
s_sleep 2
s_branch L_stub_sj
L_stub_ctx:
buffer_load_dwordx2 v[10:11], v6, s[52:55], 0 offen sc1
s_waitcnt vmcnt(0)
v_cmp_ne_u64 vcc, s[64:65], v[10:11]
s_sub_u32 s95, s95, 1
s_cselect_b32 s95, 0, s95
s_cbranch_vccz L_ret_ctx
s_cbranch_scc1 L_ret_ctx
s_sleep 2
s_branch L_stub_ctx
L_stub_ex:
buffer_load_dwordx2 v[10:11], v6, s[52:55], 0 offen sc1
s_waitcnt vmcnt(0)
v_cmp_ne_u64 vcc, s[64:65], v[10:11]
s_sub_u32 s95, s95, 1
s_cselect_b32 s95, 0, s95
s_cbranch_vccz L_ret_ex
s_cbranch_scc1 L_ret_ex
s_sleep 2
s_branch L_stub_ex
L_stub_x1:
buffer_load_dwordx2 v[10:11], v6, s[52:55], 0 offen sc1
s_waitcnt vmcnt(0)
v_cmp_ne_u64 vcc, s[64:65], v[10:11]
s_sub_u32 s95, s95, 1
s_cselect_b32 s95, 0, s95
s_cbranch_vccz L_ret_x1
s_cbranch_scc1 L_ret_x1
s_sleep 2
s_branch L_stub_x1
L_stub_h1:
buffer_load_dwordx2 v[10:11], v6, s[52:55], 0 offen sc1
s_waitcnt vmcnt(0)
v_cmp_ne_u64 vcc, s[64:65], v[10:11]
s_sub_u32 s95, s95, 1
s_cselect_b32 s95, 0, s95
s_cbranch_vccz L_ret_h1
s_cbranch_scc1 L_ret_h1
s_sleep 2
s_branch L_stub_h1
L_code_end:
)ASM"
    :: [ka]"s"(__builtin_amdgcn_kernarg_segment_ptr()), [bid]"s"(blockIdx.x), [tid]"v"(threadIdx.x), [lds]"s"((unsigned)(size_t)lds)
    : "v1", "v2", "v3", "v4", "v5", "v6", "v7", "v8", "v9", "v10", "v11", "v12", "v13", "v14", "v15", "v16", "v17", "v18", "v19", "v20", "v21", "v22", "v23", "v24", "v25", "v26", "v27", "v28", "v29", "v30", "v31", "v32", "v33", "v34", "v35", "v36", "v37", "v38", "v39", "v40", "v41", "v42", "v43", "v44", "v45", "v46", "v47", "v48", "v49", "v50", "v51", "v52", "v53", "v54", "v55", "v56", "v57", "v58", "v59", "v60", "v61", "v62", "v63", "v64", "v65", "v66", "v67", "v68", "v69", "v70", "v71", "v72", "v73", "v74", "v75", "v76", "v77", "v78", "v79", "v80", "v81", "v82", "v83", "v84", "v85", "v86", "v87", "v88", "v89", "v90", "v91", "v92", "v93", "v94", "v95", "v96", "v97", "v98", "v99", "v100", "v101", "v102", "v103", "v104", "v105", "v106", "v107", "v108", "v109", "v110", "v111", "v112", "v113", "v114", "v115", "v116", "v117", "v118", "v119", "v120", "v121", "v122", "v123", "v124", "v125", "v126", "v127", "v128", "v129", "v130", "v131", "v132", "v133", "v134", "v135", "v136", "v137", "v138", "v139", "v140", "v141", "v142", "v143", "v144", "v145", "v146", "v147", "v148", "v149", "v150", "v151", "v152", "v153", "v154", "v155", "v156", "v157", "v158", "v159", "v160", "v161", "v162", "v163", "v164", "v165", "v166", "v167", "v168", "v169", "v170", "v171", "v172", "v173", "v174", "v175", "v176", "v177", "v178", "v179", "v180", "v181", "v182", "v183", "v184", "v185", "v186", "v187", "v188", "v189", "v190", "v191", "v192", "v193", "v194", "v195", "v196", "v197", "v198", "v199", "v200", "v201", "v202", "v203", "v204", "v205", "v206", "v207", "v208", "v209", "v210", "v211", "v212", "v213", "v214", "v215", "v216", "v217", "v218", "v219", "v220", "v221", "v222", "v223", "v224", "v225", "v226", "v227", "v228", "v229", "v230", "v231", "v232", "v233", "v234", "v235", "v236", "v237", "v238", "v239", "v240", "v241", "v242", "v243", "v244", "v245", "v246", "v247", "v248", "v249", "v250", "v251", "v252", "v253", "v254", "v255", "a0", "a1", "a2", "a3", "a4", "a5", "a6", "a7", "a8", "a9", "a10", "a11", "a12", "a13", "a14", "a15", "a16", "a17", "a18", "a19", "a20", "a21", "a22", "a23", "a24", "a25", "a26", "a27", "a28", "a29", "a30", "a31", "a32", "a33", "a34", "a35", "a36", "a37", "a38", "a39", "a40", "a41", "a42", "a43", "a44", "a45", "a46", "a47", "a48", "a49", "a50", "a51", "a52", "a53", "a54", "a55", "a56", "a57", "a58", "a59", "a60", "a61", "a62", "a63", "a64", "a65", "a66", "a67", "a68", "a69", "a70", "a71", "a72", "a73", "a74", "a75", "a76", "a77", "a78", "a79", "a80", "a81", "a82", "a83", "a84", "a85", "a86", "a87", "a88", "a89", "a90", "a91", "a92", "a93", "a94", "a95", "a96", "a97", "a98", "a99", "a100", "a101", "a102", "a103", "a104", "a105", "a106", "a107", "a108", "a109", "a110", "a111", "a112", "a113", "a114", "a115", "a116", "a117", "a118", "a119", "a120", "a121", "a122", "a123", "a124", "a125", "a126", "a127", "a128", "a129", "a130", "a131", "a132", "a133", "a134", "a135", "a136", "a137", "a138", "a139", "a140", "a141", "a142", "a143", "a144", "a145", "a146", "a147", "a148", "a149", "a150", "a151", "a152", "a153", "a154", "a155", "a156", "a157", "a158", "a159", "a160", "a161", "a162", "a163", "a164", "a165", "a166", "a167", "a168", "a169", "a170", "a171", "a172", "a173", "a174", "a175", "a176", "a177", "a178", "a179", "a180", "a181", "a182", "a183", "a184", "a185", "a186", "a187", "a188", "a189", "a190", "a191", "a192", "a193", "a194", "a195", "a196", "a197", "a198", "a199", "a200", "a201", "a202", "a203", "a204", "a205", "a206", "a207", "a208", "a209", "a210", "a211", "a212", "a213", "a214", "a215", "a216", "a217", "a218", "a219", "a220", "a221", "a222", "a223", "a224", "a225", "a226", "a227", "a228", "a229", "a230", "a231", "a232", "a233", "a234", "a235", "a236", "a237", "a238", "a239", "a240", "a241", "a242", "a243", "a244", "a245", "a246", "a247", "a248", "a249", "a250", "a251", "a252", "a253", "a254", "a255", "s4", "s5", "s6", "s7", "s8", "s9", "s10", "s11", "s12", "s13", "s14", "s15", "s16", "s17", "s18", "s19", "s20", "s21", "s22", "s23", "s24", "s25", "s26", "s27", "s28", "s29", "s30", "s31", "s32", "s33", "s34", "s35", "s36", "s37", "s38", "s39", "s40", "s41", "s42", "s43", "s44", "s45", "s46", "s47", "s48", "s49", "s50", "s51", "s52", "s53", "s54", "s55", "s56", "s57", "s58", "s59", "s60", "s61", "s62", "s63", "s64", "s65", "s66", "s67", "s68", "s69", "s70", "s71", "s72", "s73", "s74", "s75", "s76", "s77", "s78", "s79", "s80", "s81", "s82", "s83", "s84", "s85", "s86", "s87", "s88", "s89", "s90", "s91", "s92", "s93", "s94", "s95", "s96", "s97", "s98", "s99", "s100", "s101", "vcc", "memory");
}

extern "C" void kernel_launch(void* const* d_in, const int* in_sizes, int n_in, void* d_out, int out_size, void* d_ws, size_t ws_size,
                              hipStream_t stream) {
  (void)in_sizes; (void)n_in; (void)out_size; (void)ws_size;
  hipLaunchKernelGGL(adaln_block_kernel, dim3(256), dim3(256), 0, stream, d_in[0], d_in[1], d_in[2], d_in[3], d_in[4], d_in[5], d_in[7], d_in[8], d_in[9],
                     d_in[10], d_out, d_ws);
}
```
